```python
import torch

def _softmax(x, axis=-1):
    return torch.softmax(x, dim=axis)
def _gelu(x):
    return torch.nn.functional.gelu(x, approximate="tanh")
def _silu(x):
    return torch.nn.functional.silu(x)
def _sigmoid(x):
    return torch.sigmoid(x)
def _layernorm(x, axis=-1, eps=1e-5):
    m = x.mean(dim=axis, keepdim=True)
    v = x.var(dim=axis, keepdim=True, unbiased=False)
    return (x - m) / torch.sqrt(v + eps)
def _scatter_add(tgt, idx, src, axis=0):
    return tgt.clone().index_add_(axis, idx, src)
def _segment_sum(src, seg, n):
    out = torch.zeros((n,) + src.shape[1:], dtype=src.dtype)
    return out.index_add_(0, seg, src)
def _pad(x, pw):
    flat = tuple(v for pair in reversed(pw) for v in pair)
    return torch.nn.functional.pad(x, flat)

kernel_name = 'gated_mix__spectral_gfnet__dense_activation_silu__prompt'

def setup_inputs():

    _w = (lambda a: (lambda t: t.to(torch.bfloat16) if t.is_floating_point() else t)(torch.as_tensor(a)))
    def _A_setup():
        B, S, D = 1, 2048, 512
        Df = D // 2 + 1
        return {
            "x": _w((lambda *s: torch.randn(*s, dtype=torch.bfloat16) * 0.05)(B, S, D)),
            "w_r": _w((lambda *s: torch.randn(*s, dtype=torch.bfloat16) * 0.4)(S, Df) * 0.02),
            "w_i": _w((lambda *s: torch.randn(*s, dtype=torch.bfloat16) * 0.4)(S, Df) * 0.02),
        }
    _A_in = _A_setup()

    def _B_setup():
        B, S, D, F = 1, 2048, 512, 1792
        return {
            "x": _w((lambda *s: torch.randn(*s, dtype=torch.bfloat16) * 0.05)(B, S, D)),
            "w1": _w((lambda *s: torch.randn(*s, dtype=torch.bfloat16) * 3.2)(D, F) * (D ** -0.5)),
            "w2": _w((lambda *s: torch.randn(*s, dtype=torch.bfloat16) * 0.05)(F, D) * (F ** -0.5)),
        }
    _B_in = _B_setup()

    return {
        "A_x": _A_in["x"],
        "A_w_r": _A_in["w_r"],
        "A_w_i": _A_in["w_i"],
        "B_x": _B_in["x"],
        "B_w1": _B_in["w1"],
        "B_w2": _B_in["w2"],
        "gate_w": _w((lambda *s: torch.randn(*s, dtype=torch.bfloat16) * 0.05)(512) * 16.0),
    }

def reference(A_x, A_w_r, A_w_i, B_x, B_w1, B_w2, gate_w):
    def _A_ref(x, w_r, w_i):
        B, S, D = x.shape
        X = (lambda x, axes=(-2,-1): torch.fft.rfft2(x.float(), dim=axes))(x, axes=(-2, -1))
        W = w_r + 1j * w_i
        X = X * W
        y = (lambda x, s, axes=(-2,-1): torch.fft.irfft2(x, s=s, dim=axes).to(torch.bfloat16))(X, (S, D), axes=(-2, -1))
        return y

    def _B_ref(x, w1, w2):
        h = torch.einsum("bsd,df->bsf", x, w1)
        h = _silu(h)
        out = torch.einsum("bsf,fd->bsd", h, w2)
        return out

    x = A_x
    g = _sigmoid((x * gate_w).sum(-1, keepdims=True))
    a = _A_ref(x, A_w_r, A_w_i)
    b = _B_ref(x, B_w1, B_w2)
    return g * a + (1 - g) * b
```

```cpp
#include <hip/hip_runtime.h>
#include <hip/hip_bf16.h>
#include <stdint.h>

typedef short    bf16x8 __attribute__((ext_vector_type(8)));
typedef short    s16x4  __attribute__((ext_vector_type(4)));
typedef float    f32x2  __attribute__((ext_vector_type(2)));
typedef float    f32x4  __attribute__((ext_vector_type(4)));
typedef float    f32x16 __attribute__((ext_vector_type(16)));
typedef unsigned u32x2  __attribute__((ext_vector_type(2)));
typedef unsigned u32x4  __attribute__((ext_vector_type(4)));
typedef __bf16   bf16x2_t __attribute__((ext_vector_type(2)));
#define LAS __attribute__((address_space(3)))
#define DI __device__ __forceinline__

constexpr int S_ = 2048, D_ = 512, F_ = 1792, DF = 257;

constexpr size_t WS_X  = 0;
constexpr size_t XJ_STRIDE = 260 * 32;
constexpr size_t WS_WT = WS_X + 256 * XJ_STRIDE;
constexpr size_t WS_Y  = WS_WT + (size_t)257 * 8192;
constexpr size_t WS_C  = WS_Y + (size_t)512 * 4096;
constexpr size_t WS_G  = WS_C + (size_t)512 * 1024;
constexpr size_t WS_H  = WS_G + 8192;
constexpr size_t WS_END = WS_H + (size_t)64 * 112 * 1024;

struct Params {
  const uint16_t *ax, *wr, *wi, *bx, *w1, *w2, *gw;
  uint16_t* out;
  char* ws;
};

DI unsigned pk2(float lo, float hi) {
  bf16x2_t v = __builtin_convertvector((f32x2){lo, hi}, bf16x2_t);
  return __builtin_bit_cast(unsigned, v);
}
DI float bflo(unsigned u) { return __uint_as_float(u << 16); }
DI float bfhi(unsigned u) { return __uint_as_float(u & 0xffff0000u); }
DI float rbf(float x) { return bflo(pk2(x, x)); }
DI float sin_rev(float x) { return __builtin_amdgcn_sinf(x); }
DI float cos_rev(float x) { return __builtin_amdgcn_cosf(x); }
DI f32x4 mfma16(bf16x8 a, bf16x8 b, f32x4 c) { return __builtin_amdgcn_mfma_f32_16x16x32_bf16(a, b, c, 0, 0, 0); }
DI f32x16 mfma32(bf16x8 a, bf16x8 b, f32x16 c) { return __builtin_amdgcn_mfma_f32_32x32x16_bf16(a, b, c, 0, 0, 0); }
DI bf16x8 mk8(unsigned a, unsigned b, unsigned c, unsigned d) { return __builtin_bit_cast(bf16x8, (u32x4){a, b, c, d}); }
DI bf16x8 neg8(bf16x8 v) { u32x4 u = __builtin_bit_cast(u32x4, v); u ^= 0x80008000u; return __builtin_bit_cast(bf16x8, u); }
DI void glds16(const void* g, void LAS* l) { __builtin_amdgcn_global_load_lds((const unsigned*)g, (LAS unsigned*)l, 16, 0, 0); }
DI s16x4 tr_read(unsigned addr) { return __builtin_amdgcn_ds_read_tr16_b64_v4i16((LAS s16x4*)addr); }
DI bf16x8 cat4(s16x4 lo, s16x4 hi) { return __builtin_shufflevector(lo, hi, 0, 1, 2, 3, 4, 5, 6, 7); }
#define WAIT_VM(n) asm volatile("s_waitcnt vmcnt(" #n ")" ::: "memory")
#define BAR() do { asm volatile("s_waitcnt lgkmcnt(0)" ::: "memory"); __builtin_amdgcn_s_barrier(); asm volatile("" ::: "memory"); } while (0)

DI void phase_p1(const Params& p, char LAS* lds, int b, int tid) {
  const int w = __builtin_amdgcn_readfirstlane(tid >> 6), l = tid & 63;
  const int n2 = b & 31, g = b >> 5;
  { const int s = n2 + 256 * g + 32 * w;
    glds16((const char*)p.ax + (size_t)s * 1024 + l * 16, lds + w * 1024); }
  const int m0 = 32 * (b >> 2), kq = b & 3;
  { const int c = l & 7, rg = l >> 3, k = 64 * kq + 8 * w + c;
    unsigned d[4];
    #pragma unroll
    for (int i = 0; i < 4; ++i) { const int m = m0 + 4 * rg + i; d[i] = (unsigned)p.wr[m * DF + k] | ((unsigned)p.wi[m * DF + k] << 16); }
    *(u32x4*)(p.ws + WS_WT + (size_t)k * 8192 + (m0 + 4 * rg) * 4) = (u32x4){d[0], d[1], d[2], d[3]};
    if (kq == 3 && w == 0 && c == 0) {
      #pragma unroll
      for (int i = 0; i < 4; ++i) { const int m = m0 + 4 * rg + i; d[i] = (unsigned)p.wr[m * DF + 256] | ((unsigned)p.wi[m * DF + 256] << 16); }
      *(u32x4*)(p.ws + WS_WT + (size_t)256 * 8192 + (m0 + 4 * rg) * 4) = (u32x4){d[0], d[1], d[2], d[3]};
    } }
  { const int slot = 2 * b + (tid >> 8), d0 = (tid & 255) * 2;
    float v[2];
    #pragma unroll
    for (int e = 0; e < 2; ++e) { const int d = d0 + e;
      const int k = slot <= 256 ? slot : slot - 256;
      const float ang = (float)((k * d) & 511) * (1.0f / 512.0f);
      const float sc = (slot == 0 || slot == 256) ? (1.0f / 1048576.0f) : (2.0f / 1048576.0f);
      v[e] = slot <= 256 ? sc * cos_rev(ang) : -sc * sin_rev(ang); }
    *(unsigned*)(p.ws + WS_C + (size_t)slot * 1024 + d0 * 2) = pk2(v[0], v[1]); }
  const int pr = w & 3, t = w >> 2, c = l & 15, q = l >> 4;
  bf16x8 Fc, Fs, Fns;
  { unsigned uc[4], us[4];
    #pragma unroll
    for (int jj = 0; jj < 4; ++jj) {
      float cv[2], sv[2];
      #pragma unroll
      for (int e = 0; e < 2; ++e) { const int j = 2 * jj + e; const int n1 = (j < 4) ? (4 * q + j) : (16 + 4 * q + j - 4);
        const float ang = (float)((n1 * (16 * t + c)) & 31) * (1.0f / 32.0f); cv[e] = cos_rev(ang); sv[e] = sin_rev(ang); }
      uc[jj] = pk2(cv[0], cv[1]); us[jj] = pk2(sv[0], sv[1]); }
    Fc = mk8(uc[0], uc[1], uc[2], uc[3]); Fs = mk8(us[0], us[1], us[2], us[3]); Fns = neg8(Fs); }
  bf16x8 Are, Aim;
  { unsigned ur[4], ui[4];
    #pragma unroll
    for (int r = 0; r < 4; ++r) { const float ang = (float)(((4 * q + r) * c) & 15) * (1.0f / 16.0f); const float cv = cos_rev(ang), sv = sin_rev(ang);
      ur[r] = pk2(cv, sv); ui[r] = pk2(-sv, cv); }
    Are = mk8(ur[0], ur[1], ur[2], ur[3]); Aim = mk8(ui[0], ui[1], ui[2], ui[3]); }
  float twc[4], tws[4];
  #pragma unroll
  for (int r = 0; r < 4; ++r) { const float ang = (float)(((4 * q + r) * (16 * t + c)) & 511) * (1.0f / 512.0f); twc[r] = 0.5f * cos_rev(ang); tws[r] = 0.5f * sin_rev(ang); }

  WAIT_VM(0); __syncthreads();
  { const u32x4 xv = *(const LAS u32x4*)(lds + w * 1024 + l * 16);
    const u32x4 gv = *(const u32x4*)((const char*)p.gw + l * 16);
    float sum = 0.f;
    #pragma unroll
    for (int i = 0; i < 4; ++i) { sum += rbf(bflo(xv[i]) * bflo(gv[i])); sum += rbf(bfhi(xv[i]) * bfhi(gv[i])); }
    #pragma unroll
    for (int m = 32; m >= 1; m >>= 1) sum += __shfl_xor(sum, m);
    const float z = rbf(sum);
    const float gg = rbf(1.0f / (1.0f + __expf(-z)));
    if (l == 0) ((float*)(p.ws + WS_G))[n2 + 256 * g + 32 * w] = gg; }
  const unsigned xa = (unsigned)(size_t)(lds + (2 * pr) * 1024) + l * 8, xb = xa + 1024;
  const bf16x8 A_a = cat4(tr_read(xa), tr_read(xa + 512)), A_b = cat4(tr_read(xb), tr_read(xb + 512));
  f32x4 Tr = {0, 0, 0, 0}, Ti = {0, 0, 0, 0};
  Tr = mfma16(A_a, Fc, Tr); Tr = mfma16(A_b, Fs, Tr);
  Ti = mfma16(A_a, Fns, Ti); Ti = mfma16(A_b, Fc, Ti);
  unsigned tb[4];
  #pragma unroll
  for (int r = 0; r < 4; ++r) {
    const float re = Tr[r] * twc[r] + Ti[r] * tws[r], im = Ti[r] * twc[r] - Tr[r] * tws[r]; tb[r] = pk2(re, im); }
  const bf16x8 Bt = mk8(tb[0], tb[1], tb[2], tb[3]);
  f32x4 Zr = {0, 0, 0, 0}, Zi = {0, 0, 0, 0};
  Zr = mfma16(Are, Bt, Zr); Zi = mfma16(Aim, Bt, Zi);
  LAS f32x2* Zs = (LAS f32x2*)(lds + 8192);
  #pragma unroll
  for (int r = 0; r < 4; ++r) Zs[pr * 512 + 16 * t + c + 32 * (4 * q + r)] = (f32x2){Zr[r], Zi[r]};
  __syncthreads();
  for (int k = tid >> 1; k <= 256; k += 256) {
    const int c2 = tid & 1; unsigned o[4];
    #pragma unroll
    for (int pp = 0; pp < 4; ++pp) { const f32x2 z1 = Zs[pp * 512 + k], z2 = Zs[pp * 512 + ((512 - k) & 511)];
      o[pp] = c2 == 0 ? pk2(z1[0] + z2[0], z1[1] + z2[1]) : pk2(z1[1] - z2[1], z2[0] - z1[0]); }
    *(u32x4*)(p.ws + WS_X + (size_t)b * XJ_STRIDE + k * 32 + c2 * 16) = (u32x4){o[0], o[1], o[2], o[3]};
  }
}

constexpr int RING = 24576 + 8192;
DI void m1_stage(const Params& p, char LAS* lds, int s0, int f0, int st, int w, int l) {
  char LAS* base = lds + RING + (st & 3) * 32768;
  const int d0 = st * 64;
  #pragma unroll
  for (int i = 0; i < 2; ++i) {
    const int row = 8 * (2 * w + i) + (l >> 3), chp = l & 7, ch = chp ^ ((row >> 1) & 7);
    glds16((const char*)p.bx + (size_t)(s0 + row) * 1024 + d0 * 2 + ch * 16, base + (2 * w + i) * 1024);
  }
  #pragma unroll
  for (int i = 0; i < 2; ++i) {
    const int row = 4 * (2 * w + i) + (l >> 4), chp = l & 15, ch = chp ^ ((row & 3) << 2);
    glds16((const char*)p.w1 + (size_t)(d0 + row) * (F_ * 2) + f0 * 2 + ch * 16, base + 16384 + (2 * w + i) * 1024);
  }
}
DI void phase_m1(const Params& p, char LAS* lds, int rt, int ct, int tid) {
  const int w = __builtin_amdgcn_readfirstlane(tid >> 6), l = tid & 63;
  const int s0 = 128 * rt, f0 = 128 * ct;
  const int fw = w & 1, sw = w >> 1;
  m1_stage(p, lds, s0, f0, 0, w, l); m1_stage(p, lds, s0, f0, 1, w, l); m1_stage(p, lds, s0, f0, 2, w, l);
  f32x16 acc[2] = {};
  const int h = l >> 5, blk = (l >> 4) & 1, a = (l & 15) >> 2, pp = l & 3;
  const unsigned ldsb = (unsigned)(size_t)lds + RING;
  const int xrow = 32 * sw + (l & 31);
  unsigned xoff[4];
  #pragma unroll
  for (int ks = 0; ks < 4; ++ks) xoff[ks] = xrow * 128 + 16 * ((2 * ks + h) ^ ((xrow >> 1) & 7));
  unsigned woff[2];
  #pragma unroll
  for (int ft = 0; ft < 2; ++ft) woff[ft] = 16384 + (8 * h + a) * 256 + 16 * (((8 * fw + 4 * ft) ^ (a << 2)) + 2 * blk + (pp >> 1)) + 8 * (pp & 1);
  #pragma unroll 1
  for (int st = 0; st < 8; ++st) {
    if (st < 6) { WAIT_VM(8); } else if (st == 6) { WAIT_VM(4); } else { WAIT_VM(0); }
    BAR();
    if (st + 3 < 8) m1_stage(p, lds, s0, f0, st + 3, w, l);
    const unsigned sb = ldsb + (st & 3) * 32768;
    #pragma unroll
    for (int ks = 0; ks < 4; ++ks) {
      const bf16x8 B = *(const LAS bf16x8*)(sb + xoff[ks]);
      #pragma unroll
      for (int ft = 0; ft < 2; ++ft) {
        const unsigned ad = sb + woff[ft] + ks * 16 * 256;
        const bf16x8 A = cat4(tr_read(ad), tr_read(ad + 4 * 256));
        acc[ft] = mfma32(A, B, acc[ft]);
      }
    }
  }
  const int stile = (s0 >> 5) + sw;
  #pragma unroll
  for (int ft = 0; ft < 2; ++ft)
    #pragma unroll
    for (int u = 0; u < 2; ++u) {
      unsigned o[4];
      #pragma unroll
      for (int jj = 0; jj < 4; ++jj) {
        float v0 = rbf(acc[ft][8 * u + 2 * jj]), v1 = rbf(acc[ft][8 * u + 2 * jj + 1]);
        v0 = v0 / (1.0f + __expf(-v0)); v1 = v1 / (1.0f + __expf(-v1));
        o[jj] = pk2(v0, v1);
      }
      const int fstep = (f0 + 64 * fw + 32 * ft + 16 * u) >> 4;
      *(u32x4*)(p.ws + WS_H + ((size_t)(stile * 112 + fstep) * 64 + l) * 16) = (u32x4){o[0], o[1], o[2], o[3]};
    }
}

__global__ __launch_bounds__(512, 2) void k_a(Params p) {
  extern __shared__ __attribute__((aligned(16))) char smem[];
  char LAS* lds = (char LAS*)smem;
  const int b = blockIdx.x, tid = threadIdx.x;
  phase_p1(p, lds, b, tid);
  const int x = b & 7, j = b >> 3;
  if (j < 28) { const int rt = 4 * (x >> 1) + j / 7, ct = 7 * (x & 1) + j % 7; phase_m1(p, lds, rt, ct, tid); }
}

DI void phase_p2(const Params& p, char LAS* lds, int k, int w, int l) {
  const int c = l & 15, q = l >> 4;
  bf16x8 Zr[2][2], Zi[2][2];
  #pragma unroll
  for (int mt = 0; mt < 2; ++mt)
    #pragma unroll
    for (int u = 0; u < 2; ++u) { const int j = (4 * u + q) * 32 + 16 * mt + c;
      const char* src = p.ws + WS_X + (size_t)j * XJ_STRIDE + k * 32;
      Zr[mt][u] = *(const bf16x8*)src; Zi[mt][u] = *(const bf16x8*)(src + 16); }
  unsigned Wv[8];
  #pragma unroll
  for (int t = 0; t < 2; ++t)
    #pragma unroll
    for (int r = 0; r < 4; ++r) Wv[4 * t + r] = *(const unsigned*)(p.ws + WS_WT + (size_t)k * 8192 + (16 * w + c + 64 * (16 * t + 4 * q + r)) * 4);
  bf16x8 Gc[2], Gs[2];
  #pragma unroll
  for (int u = 0; u < 2; ++u) { unsigned uc[4], us[4];
    #pragma unroll
    for (int jj = 0; jj < 4; ++jj) { float cv[2], sv[2];
      #pragma unroll
      for (int e = 0; e < 2; ++e) { const float ang = (float)(((32 * u + 8 * q + 2 * jj + e) * (16 * w + c)) & 63) * (1.0f / 64.0f); cv[e] = cos_rev(ang); sv[e] = sin_rev(ang); }
      uc[jj] = pk2(cv[0], cv[1]); us[jj] = pk2(sv[0], sv[1]); }
    Gc[u] = mk8(uc[0], uc[1], uc[2], uc[3]); Gs[u] = mk8(us[0], us[1], us[2], us[3]); }
  bf16x8 Hc[2], Hs[2];
  #pragma unroll
  for (int t = 0; t < 2; ++t) { unsigned uc[4], us[4];
    #pragma unroll
    for (int jj = 0; jj < 4; ++jj) { float cv[2], sv[2];
      #pragma unroll
      for (int e = 0; e < 2; ++e) { const int j = 2 * jj + e; const int n = j < 4 ? 4 * q + j : 16 + 4 * q + j - 4;
        const float ang = (float)((n * (16 * t + c)) & 31) * (1.0f / 32.0f); cv[e] = cos_rev(ang); sv[e] = sin_rev(ang); }
      uc[jj] = pk2(cv[0], cv[1]); us[jj] = pk2(sv[0], sv[1]); }
    Hc[t] = mk8(uc[0], uc[1], uc[2], uc[3]); Hs[t] = mk8(us[0], us[1], us[2], us[3]); }
  float twc[8], tws[8];
  #pragma unroll
  for (int i = 0; i < 8; ++i) { const int n2 = 16 * (i >> 2) + 4 * q + (i & 3);
    const float ang = (float)((n2 * (16 * w + c)) & 2047) * (1.0f / 2048.0f); twc[i] = cos_rev(ang); tws[i] = sin_rev(ang); }

  f32x4 Dr[2] = {}, Di[2] = {};
  #pragma unroll
  for (int mt = 0; mt < 2; ++mt)
    #pragma unroll
    for (int u = 0; u < 2; ++u) {
      Dr[mt] = mfma16(Zr[mt][u], Gc[u], Dr[mt]); Dr[mt] = mfma16(Zi[mt][u], Gs[u], Dr[mt]);
      Di[mt] = mfma16(Zi[mt][u], Gc[u], Di[mt]); Di[mt] = mfma16(neg8(Zr[mt][u]), Gs[u], Di[mt]);
    }
  unsigned tr_[4], ti_[4];
  { float re[8], im[8];
    #pragma unroll
    for (int i = 0; i < 8; ++i) { const float x = Dr[i >> 2][i & 3], y = Di[i >> 2][i & 3]; re[i] = x * twc[i] + y * tws[i]; im[i] = y * twc[i] - x * tws[i]; }
    #pragma unroll
    for (int jj = 0; jj < 4; ++jj) { tr_[jj] = pk2(re[2 * jj], re[2 * jj + 1]); ti_[jj] = pk2(im[2 * jj], im[2 * jj + 1]); } }
  bf16x8 Tr = mk8(tr_[0], tr_[1], tr_[2], tr_[3]), Ti = mk8(ti_[0], ti_[1], ti_[2], ti_[3]);
  f32x4 Vr[2] = {}, Vi[2] = {};
  { const bf16x8 nTr = neg8(Tr);
    #pragma unroll
    for (int t = 0; t < 2; ++t) {
      Vr[t] = mfma16(Hc[t], Tr, Vr[t]); Vr[t] = mfma16(Hs[t], Ti, Vr[t]);
      Vi[t] = mfma16(Hc[t], Ti, Vi[t]); Vi[t] = mfma16(Hs[t], nTr, Vi[t]); } }
  { float re[8], im[8];
    #pragma unroll
    for (int i = 0; i < 8; ++i) { const float x = Vr[i >> 2][i & 3], y = Vi[i >> 2][i & 3], wr = bflo(Wv[i]), wi = bfhi(Wv[i]);
      re[i] = x * wr - y * wi; im[i] = x * wi + y * wr; }
    #pragma unroll
    for (int jj = 0; jj < 4; ++jj) { tr_[jj] = pk2(re[2 * jj], re[2 * jj + 1]); ti_[jj] = pk2(im[2 * jj], im[2 * jj + 1]); } }
  Tr = mk8(tr_[0], tr_[1], tr_[2], tr_[3]); Ti = mk8(ti_[0], ti_[1], ti_[2], ti_[3]);
  f32x4 Ur[2] = {}, Ui[2] = {};
  { const bf16x8 nTi = neg8(Ti);
    #pragma unroll
    for (int t = 0; t < 2; ++t) {
      Ur[t] = mfma16(Hc[t], Tr, Ur[t]); Ur[t] = mfma16(Hs[t], nTi, Ur[t]);
      Ui[t] = mfma16(Hc[t], Ti, Ui[t]); Ui[t] = mfma16(Hs[t], Tr, Ui[t]); } }
  #pragma unroll
  for (int t = 0; t < 2; ++t) { float re[4], im[4];
    #pragma unroll
    for (int r = 0; r < 4; ++r) { const float x = Ur[t][r], y = Ui[t][r]; re[r] = x * twc[4 * t + r] - y * tws[4 * t + r]; im[r] = y * twc[4 * t + r] + x * tws[4 * t + r]; }
    char LAS* dst = lds + (16 * w + c) * 72 + (16 * t + 4 * q) * 2;
    *(LAS u32x2*)dst = (u32x2){pk2(re[0], re[1]), pk2(re[2], re[3])};
    *(LAS u32x2*)(dst + 64 * 72) = (u32x2){pk2(im[0], im[1]), pk2(im[2], im[3])}; }
  __syncthreads();
  f32x4 yr[2] = {}, yi[2] = {};
  { const int a = (l & 15) >> 2, pp = l & 3;
    const unsigned base = (unsigned)(size_t)lds;
    #pragma unroll
    for (int t = 0; t < 2; ++t)
      #pragma unroll
      for (int u = 0; u < 2; ++u) {
        const unsigned ad = base + (32 * u + 8 * q + a) * 72 + (8 * pp + 4 * t) * 2;
        const bf16x8 Ar = cat4(tr_read(ad), tr_read(ad + 4 * 72));
        const bf16x8 Ai = cat4(tr_read(ad + 64 * 72), tr_read(ad + 64 * 72 + 4 * 72));
        yr[t] = mfma16(Ar, Gc[u], yr[t]); yr[t] = mfma16(neg8(Ai), Gs[u], yr[t]);
        yi[t] = mfma16(Ai, Gc[u], yi[t]); yi[t] = mfma16(Ar, Gs[u], yi[t]);
      } }
  { const int s = 32 * (16 * w + c) + 8 * q;
    const u32x4 vr = {pk2(yr[0][0], yr[0][1]), pk2(yr[0][2], yr[0][3]), pk2(yr[1][0], yr[1][1]), pk2(yr[1][2], yr[1][3])};
    const u32x4 vi = {pk2(yi[0][0], yi[0][1]), pk2(yi[0][2], yi[0][3]), pk2(yi[1][0], yi[1][1]), pk2(yi[1][2], yi[1][3])};
    *(u32x4*)(p.ws + WS_Y + (size_t)k * 4096 + s * 2) = vr;
    if (k >= 1 && k <= 255) *(u32x4*)(p.ws + WS_Y + (size_t)(256 + k) * 4096 + s * 2) = vi; }
}
__global__ __launch_bounds__(256) void k_b(Params p) {
  __shared__ __attribute__((aligned(16))) char smem[2 * 64 * 72];
  const int b = blockIdx.x;
  const int k = b < 256 ? (b & 7) * 32 + (b >> 3) : 256;
  phase_p2(p, (char LAS*)smem, k, __builtin_amdgcn_readfirstlane(threadIdx.x >> 6), threadIdx.x & 63);
}

DI void m2_stage(const Params& p, char LAS* lds, int pr, int ct, int st, int w, int l) {
  char LAS* base = lds + (st & 3) * 32768;
  const int kh = w >> 2, wi = w & 3;
  char LAS* dA = base + kh * 16384; char LAS* dB = dA + 8192;
  if (st < 14) {
    const int f0 = kh * 896 + st * 64;
    #pragma unroll
    for (int i = 0; i < 2; ++i) { const int row = 8 * (2 * wi + i) + (l >> 3), chp = l & 7, ch = chp ^ (4 * ((row >> 1) & 1));
      glds16((const char*)p.w2 + (size_t)(f0 + row) * 1024 + ct * 128 + ch * 16, dA + (2 * wi + i) * 1024); }
    #pragma unroll
    for (int i = 0; i < 2; ++i) { const int fr = 2 * wi + i, stile = 2 * pr + (fr >> 2), fstep = (f0 >> 4) + (fr & 3);
      glds16(p.ws + WS_H + ((size_t)(stile * 112 + fstep) * 64 + l) * 16, dB + fr * 1024); }
  } else {
    const int sl0 = kh * 256 + (st - 14) * 64;
    #pragma unroll
    for (int i = 0; i < 2; ++i) { const int row = 8 * (2 * wi + i) + (l >> 3), chp = l & 7, ch = chp ^ (4 * ((row >> 1) & 1));
      glds16(p.ws + WS_C + (size_t)(sl0 + row) * 1024 + ct * 128 + ch * 16, dA + (2 * wi + i) * 1024);
      }
    #pragma unroll
    for (int i = 0; i < 2; ++i) { const int row = 8 * (2 * wi + i) + (l >> 3), chp = l & 7, ch = chp ^ (4 * ((row >> 1) & 1));
      glds16(p.ws + WS_Y + (size_t)(sl0 + row) * 4096 + pr * 128 + ch * 16, dB + (2 * wi + i) * 1024); }
  }
}
DI void phase_m2(const Params& p, char LAS* lds, int pr, int ct, int tid) {
  const int w = __builtin_amdgcn_readfirstlane(tid >> 6), l = tid & 63;
  const int kh = w >> 2, nh = (w >> 1) & 1, sh = w & 1;
  const int h = l >> 5, blk = (l >> 4) & 1, a = (l & 15) >> 2, pp = l & 3;
  m2_stage(p, lds, pr, ct, 0, w, l); m2_stage(p, lds, pr, ct, 1, w, l); m2_stage(p, lds, pr, ct, 2, w, l);
  const unsigned ldsb = (unsigned)(size_t)lds + kh * 16384;
  const unsigned aoff = (4 * h + a) * 128 + 16 * ((4 * nh + 2 * (pp & 1) + blk) ^ (4 * (a >> 1))) + 8 * (pp >> 1);
  const unsigned boffH = 8192 + sh * 4096 + l * 16;
  const unsigned boffY = 8192 + (4 * h + a) * 128 + 16 * ((4 * sh + 2 * blk + (pp >> 1)) ^ (4 * (a >> 1))) + 8 * (pp & 1);
  f32x16 accb = {}, acca = {};
  #pragma unroll 1
  for (int st = 0; st < 14; ++st) {
    WAIT_VM(8); BAR();
    m2_stage(p, lds, pr, ct, st + 3, w, l);
    const unsigned sb = ldsb + (st & 3) * 32768;
    #pragma unroll
    for (int ks = 0; ks < 4; ++ks) {
      const bf16x8 B = *(const LAS bf16x8*)(sb + boffH + ks * 1024);
      const unsigned ad = sb + aoff + ks * 16 * 128;
      const bf16x8 A = cat4(tr_read(ad), tr_read(ad + 8 * 128));
      accb = mfma32(A, B, accb);
    }
  }
  #pragma unroll 1
  for (int st = 14; st < 18; ++st) {
    if (st == 14) { WAIT_VM(8); } else if (st == 15) { WAIT_VM(8); } else if (st == 16) { WAIT_VM(4); } else { WAIT_VM(0); }
    BAR();
    if (st + 3 < 18) m2_stage(p, lds, pr, ct, st + 3, w, l);
    const unsigned sb = ldsb + (st & 3) * 32768;
    #pragma unroll
    for (int ks = 0; ks < 4; ++ks) {
      const unsigned bd = sb + boffY + ks * 16 * 128;
      const bf16x8 B = cat4(tr_read(bd), tr_read(bd + 8 * 128));
      const unsigned ad = sb + aoff + ks * 16 * 128;
      const bf16x8 A = cat4(tr_read(ad), tr_read(ad + 8 * 128));
      acca = mfma32(A, B, acca);
    }
  }
  BAR();
  { LAS f32x4* ex = (LAS f32x4*)lds;
    const int o = 8 * (1 - kh);
    ex[(w * 4 + 0) * 64 + l] = (f32x4){kh ? acca[0] : acca[8], kh ? acca[1] : acca[9], kh ? acca[2] : acca[10], kh ? acca[3] : acca[11]};
    ex[(w * 4 + 1) * 64 + l] = (f32x4){kh ? acca[4] : acca[12], kh ? acca[5] : acca[13], kh ? acca[6] : acca[14], kh ? acca[7] : acca[15]};
    ex[(w * 4 + 2) * 64 + l] = (f32x4){kh ? accb[0] : accb[8], kh ? accb[1] : accb[9], kh ? accb[2] : accb[10], kh ? accb[3] : accb[11]};
    ex[(w * 4 + 3) * 64 + l] = (f32x4){kh ? accb[4] : accb[12], kh ? accb[5] : accb[13], kh ? accb[6] : accb[14], kh ? accb[7] : accb[15]};
    (void)o;
    BAR();
    const int pw = w ^ 4;
    const f32x4 a0 = ex[(pw * 4 + 0) * 64 + l], a1 = ex[(pw * 4 + 1) * 64 + l], b0 = ex[(pw * 4 + 2) * 64 + l], b1 = ex[(pw * 4 + 3) * 64 + l];
    float av[8], bv[8];
    #pragma unroll
    for (int i = 0; i < 4; ++i) {
      av[i] = (kh ? acca[8 + i] : acca[i]) + a0[i]; av[4 + i] = (kh ? acca[12 + i] : acca[4 + i]) + a1[i];
      bv[i] = (kh ? accb[8 + i] : accb[i]) + b0[i]; bv[4 + i] = (kh ? accb[12 + i] : accb[4 + i]) + b1[i]; }
    const int s = 64 * pr + 32 * sh + (l & 31), n = 64 * ct + 32 * nh + 16 * h + 8 * kh;
    const float g = ((const float*)(p.ws + WS_G))[s], omg = rbf(1.0f - g);
    unsigned o4[4];
    #pragma unroll
    for (int i = 0; i < 4; ++i) {
      const float v0 = rbf(g * rbf(av[2 * i])) + rbf(omg * rbf(bv[2 * i])), v1 = rbf(g * rbf(av[2 * i + 1])) + rbf(omg * rbf(bv[2 * i + 1]));
      o4[i] = pk2(v0, v1); }
    *(u32x4*)((char*)p.out + (size_t)s * 1024 + n * 2) = (u32x4){o4[0], o4[1], o4[2], o4[3]};
  }
}
__global__ __launch_bounds__(512, 2) void k_c(Params p) {
  extern __shared__ __attribute__((aligned(16))) char smem[];
  const int b = blockIdx.x, x = b & 7, j = b >> 3;
  phase_m2(p, (char LAS*)smem, 4 * x + (j >> 3), j & 7, threadIdx.x);
}

extern "C" void kernel_launch(void* const* d_in, const int* in_sizes, int n_in, void* d_out, int out_size, void* d_ws, size_t ws_size, hipStream_t stream) {
  Params p;
  p.ax = (const uint16_t*)d_in[0]; p.wr = (const uint16_t*)d_in[1]; p.wi = (const uint16_t*)d_in[2];
  p.bx = (const uint16_t*)d_in[0]; p.w1 = (const uint16_t*)d_in[4]; p.w2 = (const uint16_t*)d_in[5]; p.gw = (const uint16_t*)d_in[6];
  p.out = (uint16_t*)d_out; p.ws = (char*)d_ws;
  (void)hipFuncSetAttribute((const void*)k_a, hipFuncAttributeMaxDynamicSharedMemorySize, 163840);
  (void)hipFuncSetAttribute((const void*)k_c, hipFuncAttributeMaxDynamicSharedMemorySize, 131072);
  k_a<<<256, 512, 163840, stream>>>(p);
  k_b<<<257, 256, 0, stream>>>(p);
  k_c<<<256, 512, 131072, stream>>>(p);
}
```

```cpp
#include <hip/hip_runtime.h>
#include <hip/hip_bf16.h>
#include <stdint.h>

typedef short    bf16x8 __attribute__((ext_vector_type(8)));
typedef short    s16x4  __attribute__((ext_vector_type(4)));
typedef float    f32x2  __attribute__((ext_vector_type(2)));
typedef float    f32x4  __attribute__((ext_vector_type(4)));
typedef float    f32x16 __attribute__((ext_vector_type(16)));
typedef unsigned u32x2  __attribute__((ext_vector_type(2)));
typedef unsigned u32x4  __attribute__((ext_vector_type(4)));
typedef __bf16   bf16x2_t __attribute__((ext_vector_type(2)));
#define LAS __attribute__((address_space(3)))
#define GAS __attribute__((address_space(1)))
#define DI __device__ __forceinline__

constexpr int F_ = 1792, DF = 257;

constexpr unsigned XJ_STRIDE = 260 * 32;
constexpr unsigned FL_F1 = 0, FL_F3 = 1024, FL_F2 = 2048, FL_E = 4096, FL_ERR = 4096 + 256;
constexpr unsigned WS_X  = 8192;
constexpr unsigned WS_WT = WS_X + 256 * XJ_STRIDE;
constexpr unsigned WS_Y  = WS_WT + 257 * 8192;
constexpr unsigned WS_G  = WS_Y + 512 * 4096;
constexpr unsigned WS_H  = WS_G + 8192;
constexpr unsigned WS_END = WS_H + 64 * 112 * 1024;

struct Params {
  const uint16_t *ax, *wr, *wi, *w1, *w2, *gw;
  uint16_t* out;
  char* ws;
};

DI unsigned pk2(float lo, float hi) { return __builtin_bit_cast(unsigned, __builtin_convertvector((f32x2){lo, hi}, bf16x2_t)); }
DI float bflo(unsigned u) { return __uint_as_float(u << 16); }
DI float bfhi(unsigned u) { return __uint_as_float(u & 0xffff0000u); }
DI float rbf(float x) { return bflo(pk2(x, x)); }
DI float sin_rev(float x) { return __builtin_amdgcn_sinf(x); }
DI float cos_rev(float x) { return __builtin_amdgcn_cosf(x); }
DI f32x4 mfma16(bf16x8 a, bf16x8 b, f32x4 c) { return __builtin_amdgcn_mfma_f32_16x16x32_bf16(a, b, c, 0, 0, 0); }
DI f32x16 mfma32(bf16x8 a, bf16x8 b, f32x16 c) { return __builtin_amdgcn_mfma_f32_32x32x16_bf16(a, b, c, 0, 0, 0); }
DI bf16x8 mk8(unsigned a, unsigned b, unsigned c, unsigned d) { return __builtin_bit_cast(bf16x8, (u32x4){a, b, c, d}); }
DI bf16x8 as8(u32x4 v) { return __builtin_bit_cast(bf16x8, v); }
DI u32x4 xor4(u32x4 v, unsigned m) { v ^= m; return v; }
DI bf16x8 neg8(bf16x8 v) { return as8(xor4(__builtin_bit_cast(u32x4, v), 0x80008000u)); }
DI s16x4 tr_read(unsigned addr) { return __builtin_amdgcn_ds_read_tr16_b64_v4i16((LAS s16x4*)addr); }
DI bf16x8 lds_read16(unsigned addr) { return *(const LAS bf16x8*)addr; }
DI unsigned lds_read4(unsigned addr) { return *(const LAS unsigned*)addr; }
DI unsigned lds_read2(unsigned addr) { return *(const LAS unsigned short*)addr; }
DI bf16x8 cat4(s16x4 lo, s16x4 hi) { return __builtin_shufflevector(lo, hi, 0, 1, 2, 3, 4, 5, 6, 7); }
#define WAIT_VM(n) asm volatile("s_waitcnt vmcnt(%0)" :: "n"(n) : "memory")
#define WAIT_LGKM0() asm volatile("s_waitcnt lgkmcnt(0)" ::: "memory")
#define SB() __builtin_amdgcn_sched_barrier(0)
#define BARE_BAR() do { SB(); asm volatile("" ::: "memory"); __builtin_amdgcn_s_barrier(); asm volatile("" ::: "memory"); SB(); } while (0)
#define BAR() do { SB(); WAIT_LGKM0(); __builtin_amdgcn_s_barrier(); asm volatile("" ::: "memory"); SB(); } while (0)
#define PIN(x) asm volatile("" : "+v"(x))

DI void glds(unsigned voff, const void* sbase, unsigned dst) {
  asm volatile("s_mov_b32 m0, %2\n\ts_nop 0\n\tglobal_load_lds_dwordx4 %0, %1" :: "v"(voff), "s"(sbase), "s"(dst) : "memory");
}
DI void glds_sc1(unsigned voff, const void* sbase, unsigned dst) {
  asm volatile("s_mov_b32 m0, %2\n\ts_nop 0\n\tglobal_load_lds_dwordx4 %0, %1 sc1" :: "v"(voff), "s"(sbase), "s"(dst) : "memory");
}
DI void gload16(u32x4& d, unsigned voff, const void* sbase) { asm volatile("global_load_dwordx4 %0, %1, %2" : "=v"(d) : "v"(voff), "s"(sbase) : "memory"); }
DI void gload16_sc1(u32x4& d, unsigned voff, const void* sbase) { asm volatile("global_load_dwordx4 %0, %1, %2 sc1" : "=v"(d) : "v"(voff), "s"(sbase) : "memory"); }
DI void gload4_sc1(unsigned& d, unsigned voff, const void* sbase) { asm volatile("global_load_dword %0, %1, %2 sc1" : "=v"(d) : "v"(voff), "s"(sbase) : "memory"); }
typedef __amdgpu_buffer_rsrc_t rsrc_t;
DI void st16_wt(u32x4 v, rsrc_t r, unsigned off) { __builtin_amdgcn_raw_buffer_store_b128(v, r, off, 0, 16); }
DI void st4_wt(unsigned v, rsrc_t r, unsigned off) { __builtin_amdgcn_raw_buffer_store_b32(v, r, off, 0, 16); }
constexpr unsigned OOB = 0x80000000u;
DI void flag_store(char* ws, unsigned off, unsigned epoch) { __hip_atomic_store((GAS unsigned*)(ws + off), epoch, __ATOMIC_RELAXED, __HIP_MEMORY_SCOPE_AGENT); }

#define STAMP(i) do {} while (0)
#define STAMP_DECL
#define STAMP_FLUSH() do {} while (0)

DI void spin16(u32x4 v, char* ws, unsigned voff, unsigned epoch, int l) {
  if (__all(v[0] == epoch && v[1] == epoch && v[2] == epoch && v[3] == epoch)) return;
  const unsigned long long t0 = __builtin_amdgcn_s_memrealtime();
  for (;;) {
    __builtin_amdgcn_s_sleep(1);
    gload16_sc1(v, voff, ws); WAIT_VM(0); PIN(v);
    if (__all(v[0] == epoch && v[1] == epoch && v[2] == epoch && v[3] == epoch)) break;
    if (__builtin_amdgcn_s_memrealtime() - t0 > 2000000ull) { if (l == 0) flag_store(ws, FL_ERR, 1u); break; }
  }
}
DI void spin4(char* ws, unsigned voff, unsigned epoch, int l) {
  const unsigned long long t0 = __builtin_amdgcn_s_memrealtime();
  for (;;) {
    unsigned v; gload4_sc1(v, voff, ws); WAIT_VM(0); PIN(v);
    if (__all(v == epoch)) break;
    __builtin_amdgcn_s_sleep(1);
    if (__builtin_amdgcn_s_memrealtime() - t0 > 2000000ull) { if (l == 0) flag_store(ws, FL_ERR, 1u); break; }
  }
}

constexpr unsigned L_AUX   = 131072;
constexpr unsigned L_XROWS = L_AUX;
constexpr unsigned L_ZS    = L_AUX + 16384;
constexpr unsigned L_WSCR  = L_AUX + 16384;
constexpr unsigned L_XCOL  = L_AUX;
constexpr unsigned L_WCOL  = L_AUX + 8192;
constexpr unsigned L_U     = L_AUX + 16384;
constexpr unsigned L_COL2  = 98304;
constexpr unsigned L_HRING = 98304;
constexpr int NWS = 6, NHS = 3;

template <int N> struct IC { static constexpr int v = N; };

__global__ __launch_bounds__(512, 2) void fused_kernel(Params p) {
  extern __shared__ __attribute__((aligned(16))) char smem[];
  const unsigned ldsb = (unsigned)(size_t)(char LAS*)smem;
  char LAS* lds = (char LAS*)smem;
  const int b = blockIdx.x, tid = threadIdx.x;
  const int w = __builtin_amdgcn_readfirstlane(tid >> 6), l = tid & 63;
  const int xcd = b & 7, jx = b >> 3;
  const bool has_m1 = jx < 28;
  const int rt = 4 * (xcd >> 1) + jx / 7, ct1 = 7 * (xcd & 1) + jx % 7;
  const int s0 = 128 * rt, f0 = 128 * ct1;
  const rsrc_t wsr = __builtin_amdgcn_make_buffer_rsrc(p.ws, 0, WS_END, 0x00020000);
  const int c = l & 15, q = l >> 4;
  STAMP_DECL
  STAMP(0);

  const int n2 = b & 31, gq = b >> 5, pr4 = w & 3, t1 = w >> 2;
  { const char* xr = (const char*)p.ax + (size_t)(n2 + 256 * gq + 64 * pr4) * 1024;
    glds(l * 16, xr, ldsb + L_XROWS + w * 2048); glds(l * 16, xr + 32 * 1024, ldsb + L_XROWS + w * 2048 + 1024); }
  u32x4 gwv; gload16(gwv, l * 16, p.gw);
  unsigned ev; gload4_sc1(ev, FL_E, p.ws);
  unsigned vx[2], vw[2];
  #pragma unroll
  for (int i = 0; i < 2; ++i) { const int row = 8 * (2 * w + i) + (l >> 3); vx[i] = row * 1024 + ((l & 7) ^ ((row >> 1) & 7)) * 16;
    const int r2 = 4 * (2 * w + i) + (l >> 4); vw[i] = r2 * (F_ * 2) + ((l & 15) ^ ((r2 & 3) << 2)) * 16; }
  const char* xbase = (const char*)p.ax + (size_t)s0 * 1024;
  const char* w1base = (const char*)p.w1 + f0 * 2;
  #define M1_ISSUE(s, i) do { if (has_m1) { const unsigned slot_ = ldsb + ((s) & 3) * 32768; \
      if ((i) < 2) glds(vx[(i) & 1], xbase + (s) * 128, slot_ + (2 * w + ((i) & 1)) * 1024); \
      else glds(vw[(i) & 1], w1base + (size_t)(s) * 64 * (F_ * 2), slot_ + 16384 + (2 * w + ((i) & 1)) * 1024); } } while (0)
  bf16x8 Fc, Fs, Fns, Are, Aim; float p1c[4], p1s[4];
  { const float kf = (float)(16 * t1 + c), k32 = kf * (1.0f / 32.0f), q4 = (float)(4 * q);
    unsigned uc[4], us[4];
    #pragma unroll
    for (int jj = 0; jj < 4; ++jj) { float cv[2], sv[2];
      #pragma unroll
      for (int e = 0; e < 2; ++e) { const int j = 2 * jj + e; const float ang = ((float)(j < 4 ? j : 12 + j) + q4) * k32; cv[e] = cos_rev(ang); sv[e] = sin_rev(ang); }
      uc[jj] = pk2(cv[0], cv[1]); us[jj] = pk2(sv[0], sv[1]); PIN(uc[jj]); PIN(us[jj]);
      SB(); M1_ISSUE(0, jj); SB(); }
    Fc = mk8(uc[0], uc[1], uc[2], uc[3]); Fs = mk8(us[0], us[1], us[2], us[3]); Fns = neg8(Fs);
    unsigned ur[4], ui[4];
    #pragma unroll
    for (int r = 0; r < 4; ++r) { const float nr = q4 + (float)r; const float ang = nr * ((float)c * (1.0f / 16.0f)); const float cv = cos_rev(ang), sv = sin_rev(ang);
      ur[r] = pk2(cv, sv); ui[r] = pk2(-sv, cv);
      const float a2 = nr * (kf * (1.0f / 512.0f)); p1c[r] = 0.5f * cos_rev(a2); p1s[r] = 0.5f * sin_rev(a2);
      PIN(ur[r]); PIN(ui[r]); PIN(p1c[r]); PIN(p1s[r]);
      SB(); M1_ISSUE(1, r); SB(); }
    Are = mk8(ur[0], ur[1], ur[2], ur[3]); Aim = mk8(ui[0], ui[1], ui[2], ui[3]); }
  STAMP(1);
  if (has_m1) { WAIT_VM(8); } else { WAIT_VM(0); }
  PIN(gwv); PIN(ev);
  const unsigned epoch = __builtin_amdgcn_readfirstlane(ev) + 1u;
  STAMP(2);
  { const unsigned xa = ldsb + L_XROWS + w * 2048 + l * 8, xb = xa + 1024;
    const bf16x8 A_a = cat4(tr_read(xa), tr_read(xa + 512)), A_b = cat4(tr_read(xb), tr_read(xb + 512));
    const u32x4 xv = __builtin_bit_cast(u32x4, lds_read16(ldsb + L_XROWS + w * 2048 + t1 * 1024 + l * 16));
    SB(); M1_ISSUE(2, 0); SB();
    f32x4 Tr = {0, 0, 0, 0}, Ti = {0, 0, 0, 0};
    Tr = mfma16(A_a, Fc, Tr); Ti = mfma16(A_a, Fns, Ti);
    Tr = mfma16(A_b, Fs, Tr); Ti = mfma16(A_b, Fc, Ti);
    SB(); M1_ISSUE(2, 1); SB();
    float sum = 0.f;
    #pragma unroll
    for (int i = 0; i < 4; ++i) { sum += rbf(bflo(xv[i]) * bflo(gwv[i])); sum += rbf(bfhi(xv[i]) * bfhi(gwv[i])); }
    PIN(sum);
    SB(); M1_ISSUE(2, 2); SB();
    unsigned tb[4];
    #pragma unroll
    for (int r = 0; r < 4; ++r) { const float re = Tr[r] * p1c[r] + Ti[r] * p1s[r], im = Ti[r] * p1c[r] - Tr[r] * p1s[r]; tb[r] = pk2(re, im); }
    const bf16x8 Bt = mk8(tb[0], tb[1], tb[2], tb[3]);
    f32x4 Zr = {0, 0, 0, 0}, Zi = {0, 0, 0, 0};
    Zr = mfma16(Are, Bt, Zr); Zi = mfma16(Aim, Bt, Zi);
    SB(); M1_ISSUE(2, 3); SB();
    sum += __builtin_bit_cast(float, __builtin_amdgcn_update_dpp(0, __builtin_bit_cast(int, sum), 0x111, 0xf, 0xf, true));
    sum += __builtin_bit_cast(float, __builtin_amdgcn_update_dpp(0, __builtin_bit_cast(int, sum), 0x112, 0xf, 0xf, true));
    sum += __builtin_bit_cast(float, __builtin_amdgcn_update_dpp(0, __builtin_bit_cast(int, sum), 0x114, 0xf, 0xf, true));
    sum += __builtin_bit_cast(float, __builtin_amdgcn_update_dpp(0, __builtin_bit_cast(int, sum), 0x118, 0xf, 0xf, true));
    sum += __builtin_bit_cast(float, __builtin_amdgcn_update_dpp(0, __builtin_bit_cast(int, sum), 0x142, 0xa, 0xf, true));
    sum += __builtin_bit_cast(float, __builtin_amdgcn_update_dpp(0, __builtin_bit_cast(int, sum), 0x143, 0xc, 0xf, true));
    LAS f32x2* Zs = (LAS f32x2*)(lds + L_ZS);
    #pragma unroll
    for (int r = 0; r < 4; ++r) Zs[pr4 * 512 + 16 * t1 + c + 32 * (4 * q + r)] = (f32x2){Zr[r], Zi[r]};
    SB(); M1_ISSUE(3, 0); SB();
    const float gg = rbf(__builtin_amdgcn_rcpf(1.0f + __expf(-rbf(sum))));
    BAR();
    STAMP(3);
    { const int k = tid >> 1, c2 = tid & 1; f32x2 z1[4], z2[4];
      #pragma unroll
      for (int pp = 0; pp < 4; ++pp) { z1[pp] = Zs[pp * 512 + k]; z2[pp] = Zs[pp * 512 + ((512 - k) & 511)]; }
      SB(); M1_ISSUE(3, 1); M1_ISSUE(3, 2); SB();
      unsigned o[4];
      #pragma unroll
      for (int pp = 0; pp < 4; ++pp) o[pp] = c2 == 0 ? pk2(z1[pp][0] + z2[pp][0], z1[pp][1] + z2[pp][1]) : pk2(z1[pp][1] - z2[pp][1], z2[pp][0] - z1[pp][0]);
      SB(); M1_ISSUE(3, 3); SB();
      st16_wt((u32x4){o[0], o[1], o[2], o[3]}, wsr, WS_X + b * XJ_STRIDE + k * 32 + c2 * 16);
      st4_wt(__float_as_uint(gg), wsr, l == 63 ? WS_G + (n2 + 256 * gq + 64 * pr4 + 32 * t1) * 4 : OOB);
      if (w == 0) { unsigned o2[4];
        #pragma unroll
        for (int pp = 0; pp < 4; ++pp) { const f32x2 z = Zs[pp * 512 + 256]; o2[pp] = c2 == 0 ? pk2(z[0] + z[0], z[1] + z[1]) : 0u; }
        st16_wt((u32x4){o2[0], o2[1], o2[2], o2[3]}, wsr, tid < 2 ? WS_X + b * XJ_STRIDE + 256 * 32 + c2 * 16 : OOB); }
    } }
  STAMP(4);

  const int ct = jx & 7, pr = 4 * xcd + (jx >> 3);
  const bool p2wave = w < 4 || b == 255;
  const int pw = w & 3;
  const int kcol = w < 4 ? xcd * 32 + jx : 256;
  const unsigned vrow = (l >> 3) * 1024 + ((l & 7) ^ (4 * ((l >> 4) & 1))) * 16;
  const unsigned vrowY = (l >> 3) * 4096 + ((l & 7) ^ (4 * ((l >> 4) & 1))) * 16;
  #define M2_ISSUE_W(u, i) do { const int kh_ = w >> 2, wi_ = w & 3; \
      glds(vrow, (const char*)p.w2 + (size_t)(kh_ * 896 + (u) * 64 + wi_ * 16 + (i) * 8) * 1024 + ct * 128, ldsb + ((u) % NWS) * 16384 + kh_ * 8192 + wi_ * 2048 + (i) * 1024); } while (0)
  #define M2_ISSUE_H(u, i) do { const int kh_ = w >> 2, wi_ = w & 3; const unsigned dst_ = ldsb + L_HRING + ((u) % NHS) * 16384 + kh_ * 8192 + wi_ * 2048 + (i) * 1024; \
      if ((u) < 14) glds_sc1(l * 16, p.ws + WS_H + (size_t)((2 * pr + (wi_ >> 1)) * 112 + kh_ * 56 + (u) * 4 + 2 * (wi_ & 1) + (i)) * 1024, dst_); \
      else glds_sc1(vrowY, p.ws + WS_Y + (size_t)(kh_ * 256 + ((u) - 14) * 64 + wi_ * 16 + (i) * 8) * 4096 + pr * 128, dst_); } while (0)
  const int m0 = 32 * (b >> 2), kq = b & 3;

  unsigned gcu[4], gsu[4], hcu[4], hsu[4]; float twc[8], tws[8];
  const float k1f = (float)(16 * pw + c), q4f = (float)(4 * q);
  auto p2const = [&](auto IDX) {
    constexpr int i = decltype(IDX)::v;
    if constexpr (i < 4) {
      const float k64 = k1f * (1.0f / 64.0f); const float a0 = (2.0f * q4f + (float)(2 * i)) * k64, a1 = (2.0f * q4f + (float)(2 * i + 1)) * k64;
      gcu[i] = pk2(cos_rev(a0), cos_rev(a1)); gsu[i] = pk2(sin_rev(a0), sin_rev(a1)); PIN(gcu[i]); PIN(gsu[i]);
    } else if constexpr (i < 8) {
      constexpr int jj = i - 4; const float c32 = (float)c * (1.0f / 32.0f);
      const float a0 = (q4f + (float)(2 * jj < 4 ? 2 * jj : 12 + 2 * jj)) * c32, a1 = (q4f + (float)(2 * jj + 1 < 4 ? 2 * jj + 1 : 13 + 2 * jj)) * c32;
      hcu[jj] = pk2(cos_rev(a0), cos_rev(a1)); hsu[jj] = pk2(sin_rev(a0), sin_rev(a1)); PIN(hcu[jj]); PIN(hsu[jj]);
    } else {
      constexpr int ii = i - 8; const float a = (q4f + (float)(16 * (ii >> 2) + (ii & 3))) * (k1f * (1.0f / 2048.0f));
      twc[ii] = cos_rev(a); tws[ii] = sin_rev(a); PIN(twc[ii]); PIN(tws[ii]);
    } };

  f32x16 acc1[2] = {};
  u32x4 f1v = {0, 0, 0, 0};
  if (has_m1) {
    const int fw = w & 1, sw = w >> 1;
    const int h = l >> 5, blk = (l >> 4) & 1, a = (l & 15) >> 2, pp = l & 3;
    const int xrow = 32 * sw + (l & 31);
    unsigned xo[2][4], wo0[2], wo1[2];
    #pragma unroll
    for (int hh = 0; hh < 2; ++hh) {
      #pragma unroll
      for (int ks = 0; ks < 4; ++ks) { xo[hh][ks] = ldsb + hh * 65536 + xrow * 128 + 16 * ((2 * ks + h) ^ ((xrow >> 1) & 7)); PIN(xo[hh][ks]); }
      wo0[hh] = ldsb + hh * 65536 + 16384 + (8 * h + a) * 256 + 16 * (((8 * fw + 0) ^ (a << 2)) + 2 * blk + (pp >> 1)) + 8 * (pp & 1); PIN(wo0[hh]);
      wo1[hh] = ldsb + hh * 65536 + 16384 + (8 * h + a) * 256 + 16 * (((8 * fw + 4) ^ (a << 2)) + 2 * blk + (pp >> 1)) + 8 * (pp & 1); PIN(wo1[hh]); }
    struct Fr { bf16x8 B, A0, A1; } fa, fb;
    #define RB(f, g)  f.B  = lds_read16(xo[(((g) >> 2) & 3) >> 1][(g) & 3] + (((g) >> 2) & 1) * 32768)
    #define RA(base_, g) cat4(tr_read(base_[(((g) >> 2) & 3) >> 1] + (((g) >> 2) & 1) * 32768 + ((g) & 3) * 4096), tr_read(base_[(((g) >> 2) & 3) >> 1] + (((g) >> 2) & 1) * 32768 + ((g) & 3) * 4096 + 1024))
    unsigned wd[4], wde = 0;
    WAIT_VM(9); BARE_BAR();
    STAMP(5);
    RB(fa, 0); fa.A0 = RA(wo0, 0); fa.A1 = RA(wo1, 0);
    auto kstep = [&](auto G, Fr& cur, Fr& nxt) {
      constexpr int g = decltype(G)::v, st = g >> 2, ks = g & 3;
      if constexpr (ks == 0 && st > 0) {
        if constexpr (st == 1) { WAIT_VM(6); } else if constexpr (st == 2) { WAIT_VM(4); } else if constexpr (st == 3) { WAIT_VM(5); } else if constexpr (st <= 6) { WAIT_VM(4); }
        BARE_BAR();
        STAMP(20 + st);
        if constexpr (st == 4) { if (tid == 0) flag_store(p.ws, FL_F1 + b * 4, epoch); }
        if constexpr (st == 6) { if (w == 0) gload16_sc1(f1v, FL_F1 + l * 16, p.ws); }
      }
      acc1[0] = mfma32(cur.A0, cur.B, acc1[0]); SB();
      if constexpr (g + 1 < 32) { RB(nxt, g + 1); nxt.A0 = RA(wo0, g + 1); }
      SB();
      acc1[1] = mfma32(cur.A1, cur.B, acc1[1]); SB();
      if constexpr (g + 1 < 32) { nxt.A1 = RA(wo1, g + 1); }
      if constexpr (st >= 1 && st <= 4) { M1_ISSUE(st + 3, ks); }
      if constexpr (st >= 5) { M2_ISSUE_W(2 * (st - 5) + (ks >> 1), ks & 1); }
      if constexpr (g == 0) { const int ra = 8 * w + (l >> 3), row = ra >> 1;
        const unsigned off = (((unsigned)((m0 + row) * (DF * 2) + 128 * kq)) & ~15u) + (l & 7) * 16;
        const char* src = ((ra & 1) ? (const char*)p.wi : (const char*)p.wr) + off;
        asm volatile("s_mov_b32 m0, %1\n\ts_nop 0\n\tglobal_load_lds_dwordx4 %0, off" :: "v"(src), "s"(ldsb + L_WSCR + w * 1024) : "memory"); }
      if constexpr (g == 1) { if (w == 0) { const int row = l >> 1;
        const unsigned off = (((unsigned)((m0 + row) * (DF * 2) + 128 * kq)) & ~15u) + 128;
        const char* src = ((l & 1) ? (const char*)p.wi : (const char*)p.wr) + off;
        asm volatile("s_mov_b32 m0, %1\n\ts_nop 0\n\tglobal_load_lds_dwordx4 %0, off" :: "v"(src), "s"(ldsb + L_WSCR + 8192) : "memory"); } }
      if constexpr (g == 8) {
        const int cc = l & 7, rg = l >> 3, j = 8 * w + cc;
        #pragma unroll
        for (int i = 0; i < 4; ++i) { const int row = 4 * rg + i; const int e = (((m0 + row) * (DF * 2)) & 15) + 2 * j;
          const unsigned ad0 = e < 128 ? ldsb + L_WSCR + (2 * row) * 128 + e : ldsb + L_WSCR + 8192 + (2 * row) * 16 + (e - 128);
          const unsigned ad1 = e < 128 ? ldsb + L_WSCR + (2 * row + 1) * 128 + e : ldsb + L_WSCR + 8192 + (2 * row + 1) * 16 + (e - 128);
          wd[i] = lds_read2(ad0) | (lds_read2(ad1) << 16); }
        { const int row = l & 31; const int e = (((m0 + row) * (DF * 2)) & 15) + 128;
          wde = lds_read2(ldsb + L_WSCR + 8192 + (2 * row) * 16 + (e - 128)) | (lds_read2(ldsb + L_WSCR + 8192 + (2 * row + 1) * 16 + (e - 128)) << 16); } }
      if constexpr (g == 9) { const int cc = l & 7, rg = l >> 3;
        st16_wt((u32x4){wd[0], wd[1], wd[2], wd[3]}, wsr, WS_WT + (64 * kq + 8 * w + cc) * 8192 + (m0 + 4 * rg) * 4);
        if (w == 0) st4_wt(wde, wsr, (kq == 3 && l < 32) ? WS_WT + 256 * 8192 + (m0 + l) * 4 : OOB); }
      if constexpr (g >= 12 && g < 28) { if (p2wave) p2const(IC<g - 12>{}); }
      SB();
    };
    #define K2(g) kstep(IC<g>{}, fa, fb); kstep(IC<g + 1>{}, fb, fa);
    K2(0) K2(2) K2(4) K2(6) K2(8) K2(10) K2(12) K2(14) K2(16) K2(18) K2(20) K2(22) K2(24) K2(26) K2(28) K2(30)
    STAMP(6);
    if (w == 0) { WAIT_VM(8); PIN(f1v); spin16(f1v, p.ws, FL_F1 + l * 16, epoch, l); }
  } else {
    { const int ra = 8 * w + (l >> 3), row = ra >> 1;
      const unsigned off = (((unsigned)((m0 + row) * (DF * 2) + 128 * kq)) & ~15u) + (l & 7) * 16;
      const char* src = ((ra & 1) ? (const char*)p.wi : (const char*)p.wr) + off;
      BAR();
      asm volatile("s_mov_b32 m0, %1\n\ts_nop 0\n\tglobal_load_lds_dwordx4 %0, off" :: "v"(src), "s"(ldsb + L_WSCR + w * 1024) : "memory"); }
    if (w == 0) { const int row = l >> 1;
      const unsigned off = (((unsigned)((m0 + row) * (DF * 2) + 128 * kq)) & ~15u) + 128;
      const char* src = ((l & 1) ? (const char*)p.wi : (const char*)p.wr) + off;
      asm volatile("s_mov_b32 m0, %1\n\ts_nop 0\n\tglobal_load_lds_dwordx4 %0, off" :: "v"(src), "s"(ldsb + L_WSCR + 8192) : "memory"); }
    if (p2wave) { p2const(IC<0>{}); p2const(IC<1>{}); p2const(IC<2>{}); p2const(IC<3>{}); p2const(IC<4>{}); p2const(IC<5>{}); p2const(IC<6>{}); p2const(IC<7>{});
                  p2const(IC<8>{}); p2const(IC<9>{}); p2const(IC<10>{}); p2const(IC<11>{}); p2const(IC<12>{}); p2const(IC<13>{}); p2const(IC<14>{}); p2const(IC<15>{}); }
    WAIT_VM(0); BAR();
    { const int cc = l & 7, rg = l >> 3, j = 8 * w + cc; unsigned wd[4];
      #pragma unroll
      for (int i = 0; i < 4; ++i) { const int row = 4 * rg + i; const int e = (((m0 + row) * (DF * 2)) & 15) + 2 * j;
        const unsigned ad0 = e < 128 ? ldsb + L_WSCR + (2 * row) * 128 + e : ldsb + L_WSCR + 8192 + (2 * row) * 16 + (e - 128);
        const unsigned ad1 = e < 128 ? ldsb + L_WSCR + (2 * row + 1) * 128 + e : ldsb + L_WSCR + 8192 + (2 * row + 1) * 16 + (e - 128);
        wd[i] = lds_read2(ad0) | (lds_read2(ad1) << 16); }
      st16_wt((u32x4){wd[0], wd[1], wd[2], wd[3]}, wsr, WS_WT + (64 * kq + 8 * w + cc) * 8192 + (m0 + 4 * rg) * 4);
      if (w == 0) { const int row = l & 31; const int e = (((m0 + row) * (DF * 2)) & 15) + 128;
        const unsigned wde = lds_read2(ldsb + L_WSCR + 8192 + (2 * row) * 16 + (e - 128)) | (lds_read2(ldsb + L_WSCR + 8192 + (2 * row + 1) * 16 + (e - 128)) << 16);
        st4_wt(wde, wsr, (kq == 3 && l < 32) ? WS_WT + 256 * 8192 + (m0 + l) * 4 : OOB); } }
    WAIT_VM(0); BAR();
    if (tid == 0) flag_store(p.ws, FL_F1 + b * 4, epoch);
    #pragma unroll
    for (int u = 0; u < 6; ++u) { M2_ISSUE_W(u, 0); M2_ISSUE_W(u, 1); }
    STAMP(6);
    if (w == 0) { gload16_sc1(f1v, FL_F1 + l * 16, p.ws); WAIT_VM(0); PIN(f1v); spin16(f1v, p.ws, FL_F1 + l * 16, epoch, l); }
  }
  BAR();
  STAMP(7);
  { const unsigned wofs = __builtin_amdgcn_readfirstlane(ldsb + w * 1024);
    const unsigned vg = (64 * (w & 3) + l) * XJ_STRIDE + (w >> 2) * 16, vt = tid * 16;
    glds_sc1(vg, p.ws + WS_X + (xcd * 32 + jx) * 32, wofs + L_XCOL);
    glds_sc1(vt, p.ws + WS_WT + (size_t)(xcd * 32 + jx) * 8192, wofs + L_WCOL);
    if (b == 255) {
      glds_sc1(vg, p.ws + WS_X + 256 * 32, wofs + L_COL2);
      glds_sc1(vt, p.ws + WS_WT + (size_t)256 * 8192, wofs + L_COL2 + 8192);
    } }
  if (has_m1) {
    const int fw = w & 1, sw = w >> 1, stile = (s0 >> 5) + sw;
    #pragma unroll
    for (int ft = 0; ft < 2; ++ft)
      #pragma unroll
      for (int u = 0; u < 2; ++u) {
        unsigned o[4];
        #pragma unroll
        for (int jj = 0; jj < 4; ++jj) {
          const unsigned hb = pk2(acc1[ft][8 * u + 2 * jj], acc1[ft][8 * u + 2 * jj + 1]);
          const f32x2 v = {bflo(hb), bfhi(hb)};
          const f32x2 t = v * -1.44269504088896340736f;
          const f32x2 e = (f32x2){__builtin_amdgcn_exp2f(t[0]), __builtin_amdgcn_exp2f(t[1])} + 1.0f;
          const f32x2 r = v * (f32x2){__builtin_amdgcn_rcpf(e[0]), __builtin_amdgcn_rcpf(e[1])};
          o[jj] = pk2(r[0], r[1]);
        }
        const int fstep = (f0 + 64 * fw + 32 * ft + 16 * u) >> 4;
        st16_wt((u32x4){o[0], o[1], o[2], o[3]}, wsr, WS_H + ((stile * 112 + fstep) * 64 + l) * 16);
      }
    STAMP(8);
    WAIT_VM(4);
  } else { STAMP(8); WAIT_VM(0); }
  BARE_BAR();
  STAMP(9);
  { const unsigned xc = ldsb + (w < 4 ? L_XCOL : L_COL2), wc = ldsb + (w < 4 ? L_WCOL : L_COL2 + 8192);
    const unsigned ub = ldsb + (w < 4 ? L_U : L_COL2 + 16384);
    bf16x8 Gc[2], Gs[2], Hc[2], Hs[2];
    if (p2wave) {
      const unsigned sgn = (c & 1) ? 0x80008000u : 0u;
      const u32x4 gc0 = {gcu[0], gcu[1], gcu[2], gcu[3]}, gs0 = {gsu[0], gsu[1], gsu[2], gsu[3]};
      const u32x4 hc0 = {hcu[0], hcu[1], hcu[2], hcu[3]}, hs0 = {hsu[0], hsu[1], hsu[2], hsu[3]};
      Gc[0] = as8(gc0); Gs[0] = as8(gs0); Gc[1] = as8(xor4(gc0, sgn)); Gs[1] = as8(xor4(gs0, sgn));
      Hc[0] = as8(hc0); Hs[0] = as8(hs0); Hc[1] = as8(xor4(hc0, 0x80000000u)); Hs[1] = as8(xor4(hs0, 0x80000000u));
      bf16x8 Zr[2][2], Zi[2][2]; unsigned Wv[8];
      #pragma unroll
      for (int u = 0; u < 2; ++u)
        #pragma unroll
        for (int mt = 0; mt < 2; ++mt) { const unsigned ad = xc + ((4 * u + q) * 32 + 16 * mt + c) * 16; Zr[mt][u] = lds_read16(ad); Zi[mt][u] = lds_read16(ad + 4096); }
      #pragma unroll
      for (int i = 0; i < 8; ++i) Wv[i] = lds_read4(wc + (16 * pw + c + 64 * (16 * (i >> 2) + 4 * q + (i & 3))) * 4);
      f32x4 Dr[2] = {}, Di[2] = {};
      #pragma unroll
      for (int u = 0; u < 2; ++u)
        #pragma unroll
        for (int mt = 0; mt < 2; ++mt) {
          Dr[mt] = mfma16(Zr[mt][u], Gc[u], Dr[mt]); Di[mt] = mfma16(Zi[mt][u], Gc[u], Di[mt]);
          Dr[mt] = mfma16(Zi[mt][u], Gs[u], Dr[mt]); Di[mt] = mfma16(neg8(Zr[mt][u]), Gs[u], Di[mt]);
        }
      unsigned tr_[4], ti_[4];
      { float re[8], im[8];
        #pragma unroll
        for (int i = 0; i < 8; ++i) { const float x = Dr[i >> 2][i & 3], y = Di[i >> 2][i & 3]; re[i] = x * twc[i] + y * tws[i]; im[i] = y * twc[i] - x * tws[i]; }
        #pragma unroll
        for (int jj = 0; jj < 4; ++jj) { tr_[jj] = pk2(re[2 * jj], re[2 * jj + 1]); ti_[jj] = pk2(im[2 * jj], im[2 * jj + 1]); } }
      bf16x8 Tr = mk8(tr_[0], tr_[1], tr_[2], tr_[3]), Ti = mk8(ti_[0], ti_[1], ti_[2], ti_[3]);
      f32x4 Vr[2] = {}, Vi[2] = {};
      { const bf16x8 nTr = neg8(Tr);
        #pragma unroll
        for (int t = 0; t < 2; ++t) { Vr[t] = mfma16(Hc[t], Tr, Vr[t]); Vi[t] = mfma16(Hc[t], Ti, Vi[t]); }
        #pragma unroll
        for (int t = 0; t < 2; ++t) { Vr[t] = mfma16(Hs[t], Ti, Vr[t]); Vi[t] = mfma16(Hs[t], nTr, Vi[t]); } }
      { float re[8], im[8];
        #pragma unroll
        for (int i = 0; i < 8; ++i) { const float x = Vr[i >> 2][i & 3], y = Vi[i >> 2][i & 3], wr = bflo(Wv[i]), wi = bfhi(Wv[i]);
          re[i] = x * wr - y * wi; im[i] = x * wi + y * wr; }
        #pragma unroll
        for (int jj = 0; jj < 4; ++jj) { tr_[jj] = pk2(re[2 * jj], re[2 * jj + 1]); ti_[jj] = pk2(im[2 * jj], im[2 * jj + 1]); } }
      Tr = mk8(tr_[0], tr_[1], tr_[2], tr_[3]); Ti = mk8(ti_[0], ti_[1], ti_[2], ti_[3]);
      f32x4 Ur[2] = {}, Ui[2] = {};
      { const bf16x8 nTi = neg8(Ti);
        #pragma unroll
        for (int t = 0; t < 2; ++t) { Ur[t] = mfma16(Hc[t], Tr, Ur[t]); Ui[t] = mfma16(Hc[t], Ti, Ui[t]); }
        #pragma unroll
        for (int t = 0; t < 2; ++t) { Ur[t] = mfma16(Hs[t], nTi, Ur[t]); Ui[t] = mfma16(Hs[t], Tr, Ui[t]); } }
      #pragma unroll
      for (int t = 0; t < 2; ++t) { float re[4], im[4];
        #pragma unroll
        for (int r = 0; r < 4; ++r) { const float x = Ur[t][r], y = Ui[t][r]; re[r] = x * twc[4 * t + r] - y * tws[4 * t + r]; im[r] = y * twc[4 * t + r] + x * tws[4 * t + r]; }
        char LAS* dst = (char LAS*)(size_t)ub + (16 * pw + c) * 72 + (16 * t + 4 * q) * 2;
        *(LAS u32x2*)dst = (u32x2){pk2(re[0], re[1]), pk2(re[2], re[3])};
        *(LAS u32x2*)(dst + 64 * 72) = (u32x2){pk2(im[0], im[1]), pk2(im[2], im[3])}; }
    }
    WAIT_VM(0);
    BAR();
    STAMP(10);
    if (has_m1 && tid == 0) flag_store(p.ws, FL_F2 + rt * 128 + ct1 * 4, epoch);
    if (p2wave) {
      f32x4 yr[2] = {}, yi[2] = {};
      const int a = (l & 15) >> 2, pp = l & 3;
      #pragma unroll
      for (int u = 0; u < 2; ++u)
        #pragma unroll
        for (int t = 0; t < 2; ++t) {
          const unsigned ad = ub + (32 * u + 8 * q + a) * 72 + (8 * pp + 4 * t) * 2;
          const bf16x8 Ar = cat4(tr_read(ad), tr_read(ad + 4 * 72));
          const bf16x8 Ai = cat4(tr_read(ad + 64 * 72), tr_read(ad + 64 * 72 + 4 * 72));
          yr[t] = mfma16(Ar, Gc[u], yr[t]); yi[t] = mfma16(Ai, Gc[u], yi[t]);
          yr[t] = mfma16(neg8(Ai), Gs[u], yr[t]); yi[t] = mfma16(Ar, Gs[u], yi[t]);
        }
      const float ysc = (kcol == 0 || kcol == 256) ? 0.5f : 1.0f;
      const u32x4 yre = {pk2(yr[0][0] * ysc, yr[0][1] * ysc), pk2(yr[0][2] * ysc, yr[0][3] * ysc), pk2(yr[1][0] * ysc, yr[1][1] * ysc), pk2(yr[1][2] * ysc, yr[1][3] * ysc)};
      const u32x4 yim = {pk2(yi[0][0], yi[0][1]), pk2(yi[0][2], yi[0][3]), pk2(yi[1][0], yi[1][1]), pk2(yi[1][2], yi[1][3])};
      const unsigned so = (32 * (16 * pw + c) + 8 * q) * 2;
      st16_wt(yre, wsr, WS_Y + kcol * 4096 + so);
      st16_wt(yim, wsr, (kcol >= 1 && kcol <= 255) ? WS_Y + (256 + kcol) * 4096 + so : OOB);
    }
    STAMP(11);
    if (w == 4) spin4(p.ws, FL_F2 + (pr >> 1) * 128 + (l < 14 ? l : 0) * 4, epoch, l);
  }
  BAR();
  STAMP(12);
  unsigned gv; gload4_sc1(gv, (64 * pr + 32 * (w & 1) + (l & 31)) * 4, p.ws + WS_G);
  #pragma unroll
  for (int u = 0; u < NHS; ++u) { M2_ISSUE_H(u, 0); M2_ISSUE_H(u, 1); }
  f32x16 accb = {}, acca = {};
  {
    const int kh = w >> 2, nh = (w >> 1) & 1, sh = w & 1;
    const int h = l >> 5, blk = (l >> 4) & 1, a = (l & 15) >> 2, pp = l & 3;
    unsigned aoff[2];
    #pragma unroll
    for (int hh = 0; hh < 2; ++hh) { aoff[hh] = ldsb + hh * 65536 + kh * 8192 + (4 * h + a) * 128 + 16 * ((4 * nh + 2 * (pp & 1) + blk) ^ (4 * (a >> 1))) + 8 * (pp >> 1); PIN(aoff[hh]); }
    unsigned boffH = ldsb + L_HRING + kh * 8192 + sh * 4096 + l * 16; PIN(boffH);
    unsigned boffY = ldsb + L_HRING + kh * 8192 + (4 * h + a) * 128 + 16 * ((4 * sh + 2 * blk + (pp >> 1)) ^ (4 * (a >> 1))) + 8 * (pp & 1); PIN(boffY);
    const int i31 = l & 31, dn = 64 * ct + 32 * nh + 16 * ((i31 >> 2) & 1) + 4 * (i31 >> 3) + (i31 & 3);
    const float dsc = (kh ? -1.0f : 1.0f) * (float)dn * (1.0f / 512.0f);
    const float cbase = (float)(4 * h) * dsc + (kh ? 0.0f : 0.25f);
    u32x4 Cf[16];
    auto cgen = [&](auto KS) { constexpr int ks = decltype(KS)::v; unsigned o[4];
      #pragma unroll
      for (int jj = 0; jj < 4; ++jj) { float v[2];
        #pragma unroll
        for (int e = 0; e < 2; ++e) { const int j = 2 * jj + e; v[e] = sin_rev(__builtin_fmaf((float)(16 * ks + 8 * (j >> 2) + (j & 3)), dsc, cbase)); }
        if (ks == 0 && jj == 0) { if (kh && h == 0) v[0] = (dn & 1) ? -1.0f : 1.0f; }
        o[jj] = pk2(v[0], v[1]); }
      Cf[ks] = (u32x4){o[0], o[1], o[2], o[3]}; PIN(Cf[ks]); };
    struct F2 { bf16x8 A[4], B[4]; } fa, fb;
    auto rdA = [&](auto ST, auto KS, F2& f) { constexpr int st = decltype(ST)::v, ks = decltype(KS)::v;
      if constexpr (st < 14) { constexpr unsigned o = (st % NWS) * 16384 + ks * 2048; f.A[ks] = cat4(tr_read(aoff[o >> 16] + (o & 65535)), tr_read(aoff[o >> 16] + (o & 65535) + 1024)); } };
    auto rdB = [&](auto ST, auto KS, F2& f) { constexpr int st = decltype(ST)::v, ks = decltype(KS)::v; constexpr unsigned o = (st % NHS) * 16384;
      if constexpr (st < 14) f.B[ks] = lds_read16(boffH + o + ks * 1024);
      else f.B[ks] = cat4(tr_read(boffY + o + ks * 2048), tr_read(boffY + o + ks * 2048 + 1024)); };
    WAIT_VM(4); PIN(gv);
    BAR();
    if (tid == 0) flag_store(p.ws, FL_F3 + b * 4, epoch);
    STAMP(13);
    rdA(IC<0>{}, IC<0>{}, fa); rdB(IC<0>{}, IC<0>{}, fa); rdA(IC<0>{}, IC<1>{}, fa); rdB(IC<0>{}, IC<1>{}, fa);
    rdA(IC<0>{}, IC<2>{}, fa); rdB(IC<0>{}, IC<2>{}, fa); rdA(IC<0>{}, IC<3>{}, fa); rdB(IC<0>{}, IC<3>{}, fa);
    u32x4 f3v = {0, 0, 0, 0};
    auto step = [&](auto ST, F2& cur, F2& nxt) {
      constexpr int st = decltype(ST)::v;
      if constexpr (st == 0) { WAIT_VM(2); } else if constexpr (st <= 8) { WAIT_VM(4); } else if constexpr (st <= 15) { WAIT_VM(2); } else if constexpr (st == 16) { WAIT_VM(0); }
      if constexpr (st == 10) { if (w == 0) { PIN(f3v); spin16(f3v, p.ws, FL_F3 + l * 16, epoch, l); } }
      BAR();
      STAMP(30 + st);
      if constexpr (st == 9) { if (w == 0) gload16_sc1(f3v, FL_F3 + l * 16, p.ws); }
      auto sub = [&](auto KS) { constexpr int ks = decltype(KS)::v;
        if constexpr (st < 14) accb = mfma32(cur.A[ks], cur.B[ks], accb); else acca = mfma32(as8(Cf[4 * (st - 14) + ks]), cur.B[ks], acca);
        SB();
        if constexpr (st < 17) { rdA(IC<st + 1>{}, KS, nxt); rdB(IC<st + 1>{}, KS, nxt); }
        if constexpr (ks < 2) { if constexpr (st + NWS < 14) M2_ISSUE_W(st + NWS, ks); }
        else                  { if constexpr (st + NHS < 18) M2_ISSUE_H(st + NHS, ks - 2); }
        if constexpr (ks == 1 && st < 16) cgen(IC<(st < 16 ? st : 0)>{});
        SB(); };
      sub(IC<0>{}); sub(IC<1>{}); sub(IC<2>{}); sub(IC<3>{});
    };
    step(IC<0>{}, fa, fb); step(IC<1>{}, fb, fa); step(IC<2>{}, fa, fb); step(IC<3>{}, fb, fa); step(IC<4>{}, fa, fb); step(IC<5>{}, fb, fa);
    step(IC<6>{}, fa, fb); step(IC<7>{}, fb, fa); step(IC<8>{}, fa, fb); step(IC<9>{}, fb, fa); step(IC<10>{}, fa, fb); step(IC<11>{}, fb, fa);
    step(IC<12>{}, fa, fb); step(IC<13>{}, fb, fa); step(IC<14>{}, fa, fb); step(IC<15>{}, fb, fa); step(IC<16>{}, fa, fb); step(IC<17>{}, fb, fa);
    STAMP(15);
    if (b == 0 && tid == 0) flag_store(p.ws, FL_E, epoch);
    BAR();
    LAS f32x4* ex = (LAS f32x4*)lds;
    ex[(w * 4 + 0) * 64 + l] = (f32x4){kh ? acca[0] : acca[8], kh ? acca[1] : acca[9], kh ? acca[2] : acca[10], kh ? acca[3] : acca[11]};
    ex[(w * 4 + 1) * 64 + l] = (f32x4){kh ? acca[4] : acca[12], kh ? acca[5] : acca[13], kh ? acca[6] : acca[14], kh ? acca[7] : acca[15]};
    ex[(w * 4 + 2) * 64 + l] = (f32x4){kh ? accb[0] : accb[8], kh ? accb[1] : accb[9], kh ? accb[2] : accb[10], kh ? accb[3] : accb[11]};
    ex[(w * 4 + 3) * 64 + l] = (f32x4){kh ? accb[4] : accb[12], kh ? accb[5] : accb[13], kh ? accb[6] : accb[14], kh ? accb[7] : accb[15]};
    BAR();
    const int pwv = w ^ 4;
    const f32x4 a0 = ex[(pwv * 4 + 0) * 64 + l], a1 = ex[(pwv * 4 + 1) * 64 + l], b0 = ex[(pwv * 4 + 2) * 64 + l], b1 = ex[(pwv * 4 + 3) * 64 + l];
    float av[8], bv[8];
    #pragma unroll
    for (int i = 0; i < 4; ++i) {
      av[i] = (kh ? acca[8 + i] : acca[i]) + a0[i]; av[4 + i] = (kh ? acca[12 + i] : acca[4 + i]) + a1[i];
      bv[i] = (kh ? accb[8 + i] : accb[i]) + b0[i]; bv[4 + i] = (kh ? accb[12 + i] : accb[4 + i]) + b1[i]; }
    const int s = 64 * pr + 32 * sh + (l & 31), n = 64 * ct + 32 * nh + 16 * h + 8 * kh;
    const float g = __uint_as_float(gv), omg = rbf(1.0f - g);
    constexpr float ASC = 2.0f / 1048576.0f;
    unsigned o4[4];
    #pragma unroll
    for (int i = 0; i < 4; ++i) {
      const float v0 = rbf(g * rbf(av[2 * i] * ASC)) + rbf(omg * rbf(bv[2 * i])), v1 = rbf(g * rbf(av[2 * i + 1] * ASC)) + rbf(omg * rbf(bv[2 * i + 1]));
      o4[i] = pk2(v0, v1); }
    *(GAS u32x4*)((char*)p.out + (size_t)s * 1024 + n * 2) = (u32x4){o4[0], o4[1], o4[2], o4[3]};
  }
  STAMP(16);
  STAMP_FLUSH();
}

extern "C" void kernel_launch(void* const* d_in, const int* in_sizes, int n_in, void* d_out, int out_size, void* d_ws, size_t ws_size, hipStream_t stream) {
  Params p;
  p.ax = (const uint16_t*)d_in[0]; p.wr = (const uint16_t*)d_in[1]; p.wi = (const uint16_t*)d_in[2];
  p.w1 = (const uint16_t*)d_in[4]; p.w2 = (const uint16_t*)d_in[5]; p.gw = (const uint16_t*)d_in[6];
  p.out = (uint16_t*)d_out; p.ws = (char*)d_ws;
  (void)hipFuncSetAttribute((const void*)fused_kernel, hipFuncAttributeMaxDynamicSharedMemorySize, 163840);
  fused_kernel<<<256, 512, 163840, stream>>>(p);
}
```

```cpp
#include <hip/hip_runtime.h>
#include <hip/hip_bf16.h>
#include <stdint.h>

typedef short    bf16x8 __attribute__((ext_vector_type(8)));
typedef short    s16x4  __attribute__((ext_vector_type(4)));
typedef float    f32x2  __attribute__((ext_vector_type(2)));
typedef float    f32x4  __attribute__((ext_vector_type(4)));
typedef float    f32x16 __attribute__((ext_vector_type(16)));
typedef unsigned u32x2  __attribute__((ext_vector_type(2)));
typedef unsigned u32x4  __attribute__((ext_vector_type(4)));
typedef __bf16   bf16x2_t __attribute__((ext_vector_type(2)));
#define LAS __attribute__((address_space(3)))
#define GAS __attribute__((address_space(1)))
#define DI __device__ __forceinline__

constexpr int F_ = 1792, DF = 257;

constexpr unsigned XJ_STRIDE = 260 * 32;
constexpr unsigned FS = 128;
constexpr unsigned FL_F1 = 0, FL_F3 = 256 * FS, FL_F2 = 512 * FS, FL_E = 768 * FS, FL_ERR = 769 * FS;
constexpr unsigned WS_X  = 772 * FS;
constexpr unsigned WS_WT = WS_X + 256 * XJ_STRIDE;
constexpr unsigned WS_Y  = WS_WT + 257 * 8192;
constexpr unsigned WS_G  = WS_Y + 512 * 4096;
constexpr unsigned WS_H  = WS_G + 8192;
constexpr unsigned WS_END = WS_H + 64 * 112 * 1024;

struct Params {
  const uint16_t *ax, *wr, *wi, *w1, *w2, *gw;
  uint16_t* out;
  char* ws;
};

DI unsigned pk2(float lo, float hi) { return __builtin_bit_cast(unsigned, __builtin_convertvector((f32x2){lo, hi}, bf16x2_t)); }
DI float bflo(unsigned u) { return __uint_as_float(u << 16); }
DI float bfhi(unsigned u) { return __uint_as_float(u & 0xffff0000u); }
DI float rbf(float x) { return bflo(pk2(x, x)); }
DI float sin_rev(float x) { return __builtin_amdgcn_sinf(x); }
DI float cos_rev(float x) { return __builtin_amdgcn_cosf(x); }
DI f32x4 mfma16(bf16x8 a, bf16x8 b, f32x4 c) { return __builtin_amdgcn_mfma_f32_16x16x32_bf16(a, b, c, 0, 0, 0); }
DI f32x16 mfma32(bf16x8 a, bf16x8 b, f32x16 c) { return __builtin_amdgcn_mfma_f32_32x32x16_bf16(a, b, c, 0, 0, 0); }
DI bf16x8 mk8(unsigned a, unsigned b, unsigned c, unsigned d) { return __builtin_bit_cast(bf16x8, (u32x4){a, b, c, d}); }
DI bf16x8 as8(u32x4 v) { return __builtin_bit_cast(bf16x8, v); }
DI u32x4 xor4(u32x4 v, unsigned m) { v ^= m; return v; }
DI bf16x8 neg8(bf16x8 v) { return as8(xor4(__builtin_bit_cast(u32x4, v), 0x80008000u)); }
DI s16x4 tr_read(unsigned addr) { return __builtin_amdgcn_ds_read_tr16_b64_v4i16((LAS s16x4*)addr); }
DI bf16x8 lds_read16(unsigned addr) { return *(const LAS bf16x8*)addr; }
DI unsigned lds_read4(unsigned addr) { return *(const LAS unsigned*)addr; }
DI unsigned lds_read2(unsigned addr) { return *(const LAS unsigned short*)addr; }
DI bf16x8 cat4(s16x4 lo, s16x4 hi) { return __builtin_shufflevector(lo, hi, 0, 1, 2, 3, 4, 5, 6, 7); }
#define WAIT_VM(n) asm volatile("s_waitcnt vmcnt(%0)" :: "n"(n) : "memory")
#define WAIT_LGKM0() asm volatile("s_waitcnt lgkmcnt(0)" ::: "memory")
#define SB() __builtin_amdgcn_sched_barrier(0)
#define BARE_BAR() do { SB(); asm volatile("" ::: "memory"); __builtin_amdgcn_s_barrier(); asm volatile("" ::: "memory"); SB(); } while (0)
#define BAR() do { SB(); WAIT_LGKM0(); __builtin_amdgcn_s_barrier(); asm volatile("" ::: "memory"); SB(); } while (0)
#define PIN(x) asm volatile("" : "+v"(x))

DI void glds(unsigned voff, const void* sbase, unsigned dst) {
  asm volatile("s_mov_b32 m0, %2\n\ts_nop 0\n\tglobal_load_lds_dwordx4 %0, %1" :: "v"(voff), "s"(sbase), "s"(dst) : "memory");
}
DI void glds_sc1(unsigned voff, const void* sbase, unsigned dst) {
  asm volatile("s_mov_b32 m0, %2\n\ts_nop 0\n\tglobal_load_lds_dwordx4 %0, %1 sc1" :: "v"(voff), "s"(sbase), "s"(dst) : "memory");
}
DI void gload16(u32x4& d, unsigned voff, const void* sbase) { asm volatile("global_load_dwordx4 %0, %1, %2" : "=v"(d) : "v"(voff), "s"(sbase) : "memory"); }
DI void gload16_sc1(u32x4& d, unsigned voff, const void* sbase) { asm volatile("global_load_dwordx4 %0, %1, %2 sc1" : "=v"(d) : "v"(voff), "s"(sbase) : "memory"); }
DI void gload4_sc1(unsigned& d, unsigned voff, const void* sbase) { asm volatile("global_load_dword %0, %1, %2 sc1" : "=v"(d) : "v"(voff), "s"(sbase) : "memory"); }
typedef __amdgpu_buffer_rsrc_t rsrc_t;
DI void st16_wt(u32x4 v, rsrc_t r, unsigned off) { __builtin_amdgcn_raw_buffer_store_b128(v, r, off, 0, 16); }
DI void st4_wt(unsigned v, rsrc_t r, unsigned off) { __builtin_amdgcn_raw_buffer_store_b32(v, r, off, 0, 16); }
constexpr unsigned OOB = 0x80000000u;
DI void flag_store(char* ws, unsigned off, unsigned epoch) { __hip_atomic_store((GAS unsigned*)(ws + off), epoch, __ATOMIC_RELAXED, __HIP_MEMORY_SCOPE_AGENT); }

#define STAMP(i) do {} while (0)
#define STAMP_DECL
#define STAMP_FLUSH() do {} while (0)

struct Poll { unsigned a, b, c, d; };
DI void poll_issue(Poll& v, unsigned voff, const char* ws) { gload4_sc1(v.a, voff, ws); gload4_sc1(v.b, voff, ws + 64 * FS); gload4_sc1(v.c, voff, ws + 128 * FS); gload4_sc1(v.d, voff, ws + 192 * FS); }
DI void poll_pin(Poll& v) { asm volatile("" : "+v"(v.a), "+v"(v.b), "+v"(v.c), "+v"(v.d)); }
DI void spin_poll(Poll& v, const char* base, char* ws, unsigned epoch, int l) {
  if (__all(v.a == epoch && v.b == epoch && v.c == epoch && v.d == epoch)) return;
  const unsigned long long t0 = __builtin_amdgcn_s_memrealtime();
  for (;;) {
    __builtin_amdgcn_s_sleep(1);
    poll_issue(v, l * FS, base); WAIT_VM(0); poll_pin(v);
    if (__all(v.a == epoch && v.b == epoch && v.c == epoch && v.d == epoch)) break;
    if (__builtin_amdgcn_s_memrealtime() - t0 > 2000000ull) { if (l == 0) flag_store(ws, FL_ERR, 1u); break; }
  }
}
DI void spin4(char* ws, unsigned voff, unsigned epoch, int l) {
  const unsigned long long t0 = __builtin_amdgcn_s_memrealtime();
  for (;;) {
    unsigned v; gload4_sc1(v, voff, ws); WAIT_VM(0); PIN(v);
    if (__all(v == epoch)) break;
    __builtin_amdgcn_s_sleep(1);
    if (__builtin_amdgcn_s_memrealtime() - t0 > 2000000ull) { if (l == 0) flag_store(ws, FL_ERR, 1u); break; }
  }
}

constexpr unsigned L_AUX   = 131072;
constexpr unsigned L_XROWS = L_AUX;
constexpr unsigned L_ZS    = L_AUX + 16384;
constexpr unsigned L_WSCR  = L_AUX + 16384;
constexpr unsigned L_XCOL  = L_AUX;
constexpr unsigned L_WCOL  = L_AUX + 8192;
constexpr unsigned L_U     = L_AUX + 16384;
constexpr unsigned L_COL2  = 81920;
constexpr unsigned L_HRING = 81920;
constexpr int NWS = 5, NHS = 5;

template <int N> struct IC { static constexpr int v = N; };

__global__ __launch_bounds__(512, 2) void fused_kernel(Params p) {
  extern __shared__ __attribute__((aligned(16))) char smem[];
  const unsigned ldsb = (unsigned)(size_t)(char LAS*)smem;
  char LAS* lds = (char LAS*)smem;
  const int b = blockIdx.x, tid = threadIdx.x;
  const int w = __builtin_amdgcn_readfirstlane(tid >> 6), l = tid & 63;
  const int xcd = b & 7, jx = b >> 3;
  const bool has_m1 = jx < 28;
  const int rt = 4 * (xcd >> 1) + jx / 7, ct1 = 7 * (xcd & 1) + jx % 7;
  const int s0 = 128 * rt, f0 = 128 * ct1;
  const rsrc_t wsr = __builtin_amdgcn_make_buffer_rsrc(p.ws, 0, WS_END, 0x00020000);
  const int c = l & 15, q = l >> 4;
  STAMP_DECL
  STAMP(0);

  const int n2 = jx, gq = xcd, pj = xcd * 32 + jx, pr4 = w & 3, t1 = w >> 2;
  { const char* xr = (const char*)p.ax + (size_t)(n2 + 256 * gq + 64 * pr4) * 1024;
    glds(l * 16, xr, ldsb + L_XROWS + w * 2048); glds(l * 16, xr + 32 * 1024, ldsb + L_XROWS + w * 2048 + 1024); }
  u32x4 gwv; gload16(gwv, l * 16, p.gw);
  unsigned ev; gload4_sc1(ev, FL_E, p.ws);
  unsigned vx[2], vw[2];
  #pragma unroll
  for (int i = 0; i < 2; ++i) { const int row = 8 * (2 * w + i) + (l >> 3); vx[i] = row * 1024 + ((l & 7) ^ ((row >> 1) & 7)) * 16;
    const int r2 = 4 * (2 * w + i) + (l >> 4); vw[i] = r2 * (F_ * 2) + ((l & 15) ^ ((r2 & 3) << 2)) * 16; }
  const char* xbase = (const char*)p.ax + (size_t)s0 * 1024;
  const char* w1base = (const char*)p.w1 + f0 * 2;
  const unsigned rot1 = (jx % 7 + 2 * (jx / 7)) & 7;
  const unsigned m1dst = __builtin_amdgcn_readfirstlane(ldsb + w * 2048);
  #define M1_ISSUE(s, i) do { const unsigned ks_ = ((s) + rot1) & 7; \
      if ((i) < 2) glds(vx[(i) & 1] + ks_ * 128, xbase, m1dst + ((s) & 3) * 32768 + ((i) & 1) * 1024); \
      else glds(vw[(i) & 1] + ks_ * (64 * F_ * 2), w1base, m1dst + ((s) & 3) * 32768 + 16384 + ((i) & 1) * 1024); } while (0)
  if (has_m1) { M1_ISSUE(0, 0); M1_ISSUE(0, 2); M1_ISSUE(0, 1); M1_ISSUE(0, 3); }
  bf16x8 Fc, Fs, Fns, Are, Aim; float p1c[4], p1s[4];
  { const float kf = (float)(16 * t1 + c), k32 = kf * (1.0f / 32.0f), q4 = (float)(4 * q);
    unsigned uc[4], us[4];
    #pragma unroll
    for (int jj = 0; jj < 4; ++jj) { float cv[2], sv[2];
      #pragma unroll
      for (int e = 0; e < 2; ++e) { const int j = 2 * jj + e; const float ang = ((float)(j < 4 ? j : 12 + j) + q4) * k32; cv[e] = cos_rev(ang); sv[e] = sin_rev(ang); }
      uc[jj] = pk2(cv[0], cv[1]); us[jj] = pk2(sv[0], sv[1]); PIN(uc[jj]); PIN(us[jj]); }
    Fc = mk8(uc[0], uc[1], uc[2], uc[3]); Fs = mk8(us[0], us[1], us[2], us[3]); Fns = neg8(Fs);
    unsigned ur[4], ui[4];
    #pragma unroll
    for (int r = 0; r < 4; ++r) { const float nr = q4 + (float)r; const float ang = nr * ((float)c * (1.0f / 16.0f)); const float cv = cos_rev(ang), sv = sin_rev(ang);
      ur[r] = pk2(cv, sv); ui[r] = pk2(-sv, cv);
      const float a2 = nr * (kf * (1.0f / 512.0f)); p1c[r] = 0.5f * cos_rev(a2); p1s[r] = 0.5f * sin_rev(a2);
      PIN(ur[r]); PIN(ui[r]); PIN(p1c[r]); PIN(p1s[r]); }
    Are = mk8(ur[0], ur[1], ur[2], ur[3]); Aim = mk8(ui[0], ui[1], ui[2], ui[3]); }
  SB();
  if (has_m1) { M1_ISSUE(1, 0); M1_ISSUE(1, 2); M1_ISSUE(1, 1); M1_ISSUE(1, 3); }
  STAMP(1);
  if (has_m1) { WAIT_VM(8); } else { WAIT_VM(0); }
  PIN(gwv); PIN(ev);
  const unsigned epoch = __builtin_amdgcn_readfirstlane(ev) + 1u;
  STAMP(2);
  { const unsigned xa = ldsb + L_XROWS + w * 2048 + l * 8, xb = xa + 1024;
    const bf16x8 A_a = cat4(tr_read(xa), tr_read(xa + 512)), A_b = cat4(tr_read(xb), tr_read(xb + 512));
    const u32x4 xv = __builtin_bit_cast(u32x4, lds_read16(ldsb + L_XROWS + w * 2048 + t1 * 1024 + l * 16));
    f32x4 Tr = {0, 0, 0, 0}, Ti = {0, 0, 0, 0};
    Tr = mfma16(A_a, Fc, Tr); Ti = mfma16(A_a, Fns, Ti);
    Tr = mfma16(A_b, Fs, Tr); Ti = mfma16(A_b, Fc, Ti);
    float sum = 0.f;
    #pragma unroll
    for (int i = 0; i < 4; ++i) { sum += rbf(bflo(xv[i]) * bflo(gwv[i])); sum += rbf(bfhi(xv[i]) * bfhi(gwv[i])); }
    PIN(sum);
    unsigned tb[4];
    #pragma unroll
    for (int r = 0; r < 4; ++r) { const float re = Tr[r] * p1c[r] + Ti[r] * p1s[r], im = Ti[r] * p1c[r] - Tr[r] * p1s[r]; tb[r] = pk2(re, im); }
    const bf16x8 Bt = mk8(tb[0], tb[1], tb[2], tb[3]);
    f32x4 Zr = {0, 0, 0, 0}, Zi = {0, 0, 0, 0};
    Zr = mfma16(Are, Bt, Zr); Zi = mfma16(Aim, Bt, Zi);
    sum += __builtin_bit_cast(float, __builtin_amdgcn_update_dpp(0, __builtin_bit_cast(int, sum), 0x111, 0xf, 0xf, true));
    sum += __builtin_bit_cast(float, __builtin_amdgcn_update_dpp(0, __builtin_bit_cast(int, sum), 0x112, 0xf, 0xf, true));
    sum += __builtin_bit_cast(float, __builtin_amdgcn_update_dpp(0, __builtin_bit_cast(int, sum), 0x114, 0xf, 0xf, true));
    sum += __builtin_bit_cast(float, __builtin_amdgcn_update_dpp(0, __builtin_bit_cast(int, sum), 0x118, 0xf, 0xf, true));
    sum += __builtin_bit_cast(float, __builtin_amdgcn_update_dpp(0, __builtin_bit_cast(int, sum), 0x142, 0xa, 0xf, true));
    sum += __builtin_bit_cast(float, __builtin_amdgcn_update_dpp(0, __builtin_bit_cast(int, sum), 0x143, 0xc, 0xf, true));
    LAS f32x2* Zs = (LAS f32x2*)(lds + L_ZS);
    #pragma unroll
    for (int r = 0; r < 4; ++r) Zs[pr4 * 512 + 16 * t1 + c + 32 * (4 * q + r)] = (f32x2){Zr[r], Zi[r]};
    const float gg = rbf(__builtin_amdgcn_rcpf(1.0f + __expf(-rbf(sum))));
    BAR();
    STAMP(3);
    { const int k = tid >> 1, c2 = tid & 1; f32x2 z1[4], z2[4];
      #pragma unroll
      for (int pp = 0; pp < 4; ++pp) { z1[pp] = Zs[pp * 512 + k]; z2[pp] = Zs[pp * 512 + ((512 - k) & 511)]; }
      unsigned o[4];
      #pragma unroll
      for (int pp = 0; pp < 4; ++pp) o[pp] = c2 == 0 ? pk2(z1[pp][0] + z2[pp][0], z1[pp][1] + z2[pp][1]) : pk2(z1[pp][1] - z2[pp][1], z2[pp][0] - z1[pp][0]);
      st16_wt((u32x4){o[0], o[1], o[2], o[3]}, wsr, WS_X + pj * XJ_STRIDE + k * 32 + c2 * 16);
      st4_wt(__float_as_uint(gg), wsr, l == 63 ? WS_G + (n2 + 256 * gq + 64 * pr4 + 32 * t1) * 4 : OOB);
      if (w == 0) { unsigned o2[4];
        #pragma unroll
        for (int pp = 0; pp < 4; ++pp) { const f32x2 z = Zs[pp * 512 + 256]; o2[pp] = c2 == 0 ? pk2(z[0] + z[0], z[1] + z[1]) : 0u; }
        st16_wt((u32x4){o2[0], o2[1], o2[2], o2[3]}, wsr, tid < 2 ? WS_X + pj * XJ_STRIDE + 256 * 32 + c2 * 16 : OOB); }
    } }
  if (has_m1) { M1_ISSUE(2, 0); M1_ISSUE(2, 2); M1_ISSUE(2, 1); M1_ISSUE(2, 3); M1_ISSUE(3, 0); M1_ISSUE(3, 2); M1_ISSUE(3, 1); M1_ISSUE(3, 3); }
  STAMP(4);

  const int ct = jx & 7, pr = 4 * xcd + (jx >> 3);
  const bool p2wave = w < 4 || b == 255;
  const int pw = w & 3;
  const int kcol = w < 4 ? xcd * 32 + jx : 256;
  const unsigned vrow = (l >> 3) * 1024 + ((l & 7) ^ (4 * ((l >> 4) & 1))) * 16;
  const unsigned vrowY = (l >> 3) * 4096 + ((l & 7) ^ (4 * ((l >> 4) & 1))) * 16;
  const unsigned rot2 = ((7 * ct) / 4 + (7 * (jx >> 3)) / 2) % 14;
  #define M2_STAGE(u) (((u) + rot2) >= 14 ? ((u) + rot2 - 14) : ((u) + rot2))
  const unsigned vw8 = vrow + ((w >> 2) * 896 + (w & 3) * 16) * 1024 + ct * 128;
  const unsigned w8dst = __builtin_amdgcn_readfirstlane(ldsb + (w >> 2) * 8192 + (w & 3) * 2048);
  #define M2_ISSUE_W8(u, i) glds(vw8 + M2_STAGE(u) * 65536 + (i) * 8192, p.w2, w8dst + ((u) % NWS) * 16384 + (i) * 1024)
  unsigned vst[4];
  #pragma unroll
  for (int i = 0; i < 4; ++i)
    vst[i] = w < 4 ? vrow + ((i >> 1) * 896 + (w & 3) * 16 + (i & 1) * 8) * 1024 + ct * 128
                   : l * 16 + ((2 * pr + ((w & 3) >> 1)) * 112 + (i >> 1) * 56 + 2 * (w & 1) + (i & 1)) * 1024;
  const char* stbase = w < 4 ? (const char*)p.w2 : p.ws + WS_H;
  const unsigned stmul = w < 4 ? 65536u : 4096u;
  const unsigned stdst = __builtin_amdgcn_readfirstlane(ldsb + (w < 4 ? 0u : L_HRING) + (w & 3) * 2048);
  #define M2_ISSUE(u, i) glds_sc1(vst[i] + M2_STAGE(u) * stmul, stbase, stdst + ((u) % 5) * 16384 + ((i) >> 1) * 8192 + ((i) & 1) * 1024)
  #define M2_ISSUE_Y(u, i) glds_sc1(vrowY + (((i) >> 1) * 256 + ((u) - 14) * 64 + ((i) & 1) * 8) * 4096 + (w & 3) * 65536 + pr * 128, p.ws + WS_Y, stdst + ((u) % 5) * 16384 + ((i) >> 1) * 8192 + ((i) & 1) * 1024)
  const int m0 = 32 * (b >> 2), kq = b & 3;

  unsigned gcu[4], gsu[4], hcu[4], hsu[4]; float twc[8], tws[8];
  const float k1f = (float)(16 * pw + c), q4f = (float)(4 * q);
  auto p2const = [&](auto IDX) {
    constexpr int i = decltype(IDX)::v;
    if constexpr (i < 4) {
      const float k64 = k1f * (1.0f / 64.0f); const float a0 = (2.0f * q4f + (float)(2 * i)) * k64, a1 = (2.0f * q4f + (float)(2 * i + 1)) * k64;
      gcu[i] = pk2(cos_rev(a0), cos_rev(a1)); gsu[i] = pk2(sin_rev(a0), sin_rev(a1)); PIN(gcu[i]); PIN(gsu[i]);
    } else if constexpr (i < 8) {
      constexpr int jj = i - 4; const float c32 = (float)c * (1.0f / 32.0f);
      const float a0 = (q4f + (float)(2 * jj < 4 ? 2 * jj : 12 + 2 * jj)) * c32, a1 = (q4f + (float)(2 * jj + 1 < 4 ? 2 * jj + 1 : 13 + 2 * jj)) * c32;
      hcu[jj] = pk2(cos_rev(a0), cos_rev(a1)); hsu[jj] = pk2(sin_rev(a0), sin_rev(a1)); PIN(hcu[jj]); PIN(hsu[jj]);
    } else {
      constexpr int ii = i - 8; const float a = (q4f + (float)(16 * (ii >> 2) + (ii & 3))) * (k1f * (1.0f / 2048.0f));
      twc[ii] = cos_rev(a); tws[ii] = sin_rev(a); PIN(twc[ii]); PIN(tws[ii]);
    } };

  f32x16 acc1[2] = {};
  Poll f1v = {0, 0, 0, 0};
  if (has_m1) {
    const int fw = w & 1, sw = w >> 1;
    const int h = l >> 5, blk = (l >> 4) & 1, a = (l & 15) >> 2, pp = l & 3;
    const int xrow = 32 * sw + (l & 31);
    unsigned xo[2][4], wo0[2], wo1[2];
    #pragma unroll
    for (int hh = 0; hh < 2; ++hh) {
      #pragma unroll
      for (int ks = 0; ks < 4; ++ks) { xo[hh][ks] = ldsb + hh * 65536 + xrow * 128 + 16 * ((2 * ks + h) ^ ((xrow >> 1) & 7)); PIN(xo[hh][ks]); }
      wo0[hh] = ldsb + hh * 65536 + 16384 + (8 * h + a) * 256 + 16 * (((8 * fw + 0) ^ (a << 2)) + 2 * blk + (pp >> 1)) + 8 * (pp & 1); PIN(wo0[hh]);
      wo1[hh] = ldsb + hh * 65536 + 16384 + (8 * h + a) * 256 + 16 * (((8 * fw + 4) ^ (a << 2)) + 2 * blk + (pp >> 1)) + 8 * (pp & 1); PIN(wo1[hh]); }
    struct Fr { bf16x8 B, A0, A1; } fa, fb;
    #define RB(f, g)  f.B  = lds_read16(xo[(((g) >> 2) & 3) >> 1][(g) & 3] + (((g) >> 2) & 1) * 32768)
    #define RA(base_, g) cat4(tr_read(base_[(((g) >> 2) & 3) >> 1] + (((g) >> 2) & 1) * 32768 + ((g) & 3) * 4096), tr_read(base_[(((g) >> 2) & 3) >> 1] + (((g) >> 2) & 1) * 32768 + ((g) & 3) * 4096 + 1024))
    unsigned wd[4], wde = 0;
    WAIT_VM(9); BARE_BAR();
    STAMP(5);
    RB(fa, 0); fa.A0 = RA(wo0, 0); fa.A1 = RA(wo1, 0);
    auto kstep = [&](auto G, Fr& cur, Fr& nxt) {
      constexpr int g = decltype(G)::v, st = g >> 2, ks = g & 3;
      if constexpr (ks == 0 && st > 0) {
        if constexpr (st == 1) { WAIT_VM(5); } else if constexpr (st == 2) { WAIT_VM(4); } else if constexpr (st == 3) { WAIT_VM(5); } else if constexpr (st == 4) { WAIT_VM(4); } else if constexpr (st <= 6) { if (w == 0) { WAIT_VM(5); } else { WAIT_VM(4); } }
        BARE_BAR();
        STAMP(20 + st);
        if constexpr (st == 6) { if (w == 0) poll_issue(f1v, l * FS, p.ws + FL_F1); }
      }
      acc1[0] = mfma32(cur.A0, cur.B, acc1[0]); SB();
      if constexpr (g + 1 < 32) { RB(nxt, g + 1); nxt.A0 = RA(wo0, g + 1); }
      SB();
      acc1[1] = mfma32(cur.A1, cur.B, acc1[1]); SB();
      if constexpr (g + 1 < 32) { nxt.A1 = RA(wo1, g + 1); }
      if constexpr (st >= 1 && st <= 4) { M1_ISSUE(st + 3, ks); }
      if constexpr (st >= 5 && 2 * (st - 5) + (ks >> 1) < NWS) { M2_ISSUE_W8(2 * (st - 5) + (ks >> 1), ks & 1); }
      if constexpr (g == 0) { const int ra = 8 * w + (l >> 3), row = ra >> 1;
        const unsigned off = (((unsigned)((m0 + row) * (DF * 2) + 128 * kq)) & ~15u) + (l & 7) * 16;
        const char* src = ((ra & 1) ? (const char*)p.wi : (const char*)p.wr) + off;
        asm volatile("s_mov_b32 m0, %1\n\ts_nop 0\n\tglobal_load_lds_dwordx4 %0, off" :: "v"(src), "s"(ldsb + L_WSCR + w * 1024) : "memory"); }
      if constexpr (g == 1) { if (w == 0) { const int row = l >> 1;
        const unsigned off = (((unsigned)((m0 + row) * (DF * 2) + 128 * kq)) & ~15u) + 128;
        const char* src = ((l & 1) ? (const char*)p.wi : (const char*)p.wr) + off;
        asm volatile("s_mov_b32 m0, %1\n\ts_nop 0\n\tglobal_load_lds_dwordx4 %0, off" :: "v"(src), "s"(ldsb + L_WSCR + 8192) : "memory"); } }
      if constexpr (g == 8) {
        const int cc = l & 7, rg = l >> 3, j = 8 * w + cc;
        #pragma unroll
        for (int i = 0; i < 4; ++i) { const int row = 4 * rg + i; const int e = (((m0 + row) * (DF * 2)) & 15) + 2 * j;
          const unsigned ad0 = e < 128 ? ldsb + L_WSCR + (2 * row) * 128 + e : ldsb + L_WSCR + 8192 + (2 * row) * 16 + (e - 128);
          const unsigned ad1 = e < 128 ? ldsb + L_WSCR + (2 * row + 1) * 128 + e : ldsb + L_WSCR + 8192 + (2 * row + 1) * 16 + (e - 128);
          wd[i] = lds_read2(ad0) | (lds_read2(ad1) << 16); }
        { const int row = l & 31; const int e = (((m0 + row) * (DF * 2)) & 15) + 128;
          wde = lds_read2(ldsb + L_WSCR + 8192 + (2 * row) * 16 + (e - 128)) | (lds_read2(ldsb + L_WSCR + 8192 + (2 * row + 1) * 16 + (e - 128)) << 16); } }
      if constexpr (g == 9) { const int cc = l & 7, rg = l >> 3;
        st16_wt((u32x4){wd[0], wd[1], wd[2], wd[3]}, wsr, WS_WT + (64 * kq + 8 * w + cc) * 8192 + (m0 + 4 * rg) * 4);
        if (w == 0) st4_wt(wde, wsr, (kq == 3 && l < 32) ? WS_WT + 256 * 8192 + (m0 + l) * 4 : OOB); }
      if constexpr (g == 19) { if (tid == 0) flag_store(p.ws, FL_F1 + b * FS, epoch); }
      if constexpr (g >= 12 && g < 28) { if (p2wave) p2const(IC<g - 12>{}); }
      SB();
    };
    #define K2(g) kstep(IC<g>{}, fa, fb); kstep(IC<g + 1>{}, fb, fa);
    K2(0) K2(2) K2(4) K2(6) K2(8) K2(10) K2(12) K2(14) K2(16) K2(18) K2(20) K2(22) K2(24) K2(26) K2(28) K2(30)
    STAMP(6);
    if (w == 0) { WAIT_VM(6); poll_pin(f1v); spin_poll(f1v, p.ws + FL_F1, p.ws, epoch, l); }
  } else {
    { const int ra = 8 * w + (l >> 3), row = ra >> 1;
      const unsigned off = (((unsigned)((m0 + row) * (DF * 2) + 128 * kq)) & ~15u) + (l & 7) * 16;
      const char* src = ((ra & 1) ? (const char*)p.wi : (const char*)p.wr) + off;
      BAR();
      asm volatile("s_mov_b32 m0, %1\n\ts_nop 0\n\tglobal_load_lds_dwordx4 %0, off" :: "v"(src), "s"(ldsb + L_WSCR + w * 1024) : "memory"); }
    if (w == 0) { const int row = l >> 1;
      const unsigned off = (((unsigned)((m0 + row) * (DF * 2) + 128 * kq)) & ~15u) + 128;
      const char* src = ((l & 1) ? (const char*)p.wi : (const char*)p.wr) + off;
      asm volatile("s_mov_b32 m0, %1\n\ts_nop 0\n\tglobal_load_lds_dwordx4 %0, off" :: "v"(src), "s"(ldsb + L_WSCR + 8192) : "memory"); }
    if (p2wave) { p2const(IC<0>{}); p2const(IC<1>{}); p2const(IC<2>{}); p2const(IC<3>{}); p2const(IC<4>{}); p2const(IC<5>{}); p2const(IC<6>{}); p2const(IC<7>{});
                  p2const(IC<8>{}); p2const(IC<9>{}); p2const(IC<10>{}); p2const(IC<11>{}); p2const(IC<12>{}); p2const(IC<13>{}); p2const(IC<14>{}); p2const(IC<15>{}); }
    WAIT_VM(0); BAR();
    { const int cc = l & 7, rg = l >> 3, j = 8 * w + cc; unsigned wd[4];
      #pragma unroll
      for (int i = 0; i < 4; ++i) { const int row = 4 * rg + i; const int e = (((m0 + row) * (DF * 2)) & 15) + 2 * j;
        const unsigned ad0 = e < 128 ? ldsb + L_WSCR + (2 * row) * 128 + e : ldsb + L_WSCR + 8192 + (2 * row) * 16 + (e - 128);
        const unsigned ad1 = e < 128 ? ldsb + L_WSCR + (2 * row + 1) * 128 + e : ldsb + L_WSCR + 8192 + (2 * row + 1) * 16 + (e - 128);
        wd[i] = lds_read2(ad0) | (lds_read2(ad1) << 16); }
      st16_wt((u32x4){wd[0], wd[1], wd[2], wd[3]}, wsr, WS_WT + (64 * kq + 8 * w + cc) * 8192 + (m0 + 4 * rg) * 4);
      if (w == 0) { const int row = l & 31; const int e = (((m0 + row) * (DF * 2)) & 15) + 128;
        const unsigned wde = lds_read2(ldsb + L_WSCR + 8192 + (2 * row) * 16 + (e - 128)) | (lds_read2(ldsb + L_WSCR + 8192 + (2 * row + 1) * 16 + (e - 128)) << 16);
        st4_wt(wde, wsr, (kq == 3 && l < 32) ? WS_WT + 256 * 8192 + (m0 + l) * 4 : OOB); } }
    WAIT_VM(0); BAR();
    if (tid == 0) flag_store(p.ws, FL_F1 + b * FS, epoch);
    #pragma unroll
    for (int u = 0; u < NWS; ++u) { M2_ISSUE_W8(u, 0); M2_ISSUE_W8(u, 1); }
    STAMP(6);
    if (w == 0) { poll_issue(f1v, l * FS, p.ws + FL_F1); WAIT_VM(0); poll_pin(f1v); spin_poll(f1v, p.ws + FL_F1, p.ws, epoch, l); }
  }
  BAR();
  STAMP(7);
  { const unsigned wofs = __builtin_amdgcn_readfirstlane(ldsb + w * 1024);
    const unsigned vg = (64 * (w & 3) + l) * XJ_STRIDE + (w >> 2) * 16, vt = tid * 16;
    glds_sc1(vg, p.ws + WS_X + (xcd * 32 + jx) * 32, wofs + L_XCOL);
    glds_sc1(vt, p.ws + WS_WT + (size_t)(xcd * 32 + jx) * 8192, wofs + L_WCOL);
    if (b == 255) {
      glds_sc1(vg, p.ws + WS_X + 256 * 32, wofs + L_COL2);
      glds_sc1(vt, p.ws + WS_WT + (size_t)256 * 8192, wofs + L_COL2 + 8192);
    } }
  if (has_m1) {
    const int fw = w & 1, sw = w >> 1, stile = (s0 >> 5) + sw;
    #pragma unroll
    for (int ft = 0; ft < 2; ++ft)
      #pragma unroll
      for (int u = 0; u < 2; ++u) {
        unsigned o[4];
        #pragma unroll
        for (int jj = 0; jj < 4; ++jj) {
          const unsigned hb = pk2(acc1[ft][8 * u + 2 * jj], acc1[ft][8 * u + 2 * jj + 1]);
          const f32x2 v = {bflo(hb), bfhi(hb)};
          const f32x2 t = v * -1.44269504088896340736f;
          const f32x2 e = (f32x2){__builtin_amdgcn_exp2f(t[0]), __builtin_amdgcn_exp2f(t[1])} + 1.0f;
          const f32x2 r = v * (f32x2){__builtin_amdgcn_rcpf(e[0]), __builtin_amdgcn_rcpf(e[1])};
          o[jj] = pk2(r[0], r[1]);
        }
        const int fstep = (f0 + 64 * fw + 32 * ft + 16 * u) >> 4;
        st16_wt((u32x4){o[0], o[1], o[2], o[3]}, wsr, WS_H + ((stile * 112 + fstep) * 64 + l) * 16);
      }
    STAMP(8);
    WAIT_VM(4);
  } else { STAMP(8); WAIT_VM(0); }
  BARE_BAR();
  STAMP(9);
  { const unsigned xc = ldsb + (w < 4 ? L_XCOL : L_COL2), wc = ldsb + (w < 4 ? L_WCOL : L_COL2 + 8192);
    const unsigned ub = ldsb + (w < 4 ? L_U : L_COL2 + 16384);
    bf16x8 Gc[2], Gs[2], Hc[2], Hs[2];
    if (p2wave) {
      const unsigned sgn = (c & 1) ? 0x80008000u : 0u;
      const u32x4 gc0 = {gcu[0], gcu[1], gcu[2], gcu[3]}, gs0 = {gsu[0], gsu[1], gsu[2], gsu[3]};
      const u32x4 hc0 = {hcu[0], hcu[1], hcu[2], hcu[3]}, hs0 = {hsu[0], hsu[1], hsu[2], hsu[3]};
      Gc[0] = as8(gc0); Gs[0] = as8(gs0); Gc[1] = as8(xor4(gc0, sgn)); Gs[1] = as8(xor4(gs0, sgn));
      Hc[0] = as8(hc0); Hs[0] = as8(hs0); Hc[1] = as8(xor4(hc0, 0x80000000u)); Hs[1] = as8(xor4(hs0, 0x80000000u));
      bf16x8 Zr[2][2], Zi[2][2]; unsigned Wv[8];
      #pragma unroll
      for (int u = 0; u < 2; ++u)
        #pragma unroll
        for (int mt = 0; mt < 2; ++mt) { const unsigned ad = xc + ((4 * u + q) * 32 + 16 * mt + c) * 16; Zr[mt][u] = lds_read16(ad); Zi[mt][u] = lds_read16(ad + 4096); }
      #pragma unroll
      for (int i = 0; i < 8; ++i) Wv[i] = lds_read4(wc + (16 * pw + c + 64 * (16 * (i >> 2) + 4 * q + (i & 3))) * 4);
      f32x4 Dr[2] = {}, Di[2] = {};
      #pragma unroll
      for (int u = 0; u < 2; ++u)
        #pragma unroll
        for (int mt = 0; mt < 2; ++mt) {
          Dr[mt] = mfma16(Zr[mt][u], Gc[u], Dr[mt]); Di[mt] = mfma16(Zi[mt][u], Gc[u], Di[mt]);
          Dr[mt] = mfma16(Zi[mt][u], Gs[u], Dr[mt]); Di[mt] = mfma16(neg8(Zr[mt][u]), Gs[u], Di[mt]);
        }
      unsigned tr_[4], ti_[4];
      { float re[8], im[8];
        #pragma unroll
        for (int i = 0; i < 8; ++i) { const float x = Dr[i >> 2][i & 3], y = Di[i >> 2][i & 3]; re[i] = x * twc[i] + y * tws[i]; im[i] = y * twc[i] - x * tws[i]; }
        #pragma unroll
        for (int jj = 0; jj < 4; ++jj) { tr_[jj] = pk2(re[2 * jj], re[2 * jj + 1]); ti_[jj] = pk2(im[2 * jj], im[2 * jj + 1]); } }
      bf16x8 Tr = mk8(tr_[0], tr_[1], tr_[2], tr_[3]), Ti = mk8(ti_[0], ti_[1], ti_[2], ti_[3]);
      f32x4 Vr[2] = {}, Vi[2] = {};
      { const bf16x8 nTr = neg8(Tr);
        #pragma unroll
        for (int t = 0; t < 2; ++t) { Vr[t] = mfma16(Hc[t], Tr, Vr[t]); Vi[t] = mfma16(Hc[t], Ti, Vi[t]); }
        #pragma unroll
        for (int t = 0; t < 2; ++t) { Vr[t] = mfma16(Hs[t], Ti, Vr[t]); Vi[t] = mfma16(Hs[t], nTr, Vi[t]); } }
      { float re[8], im[8];
        #pragma unroll
        for (int i = 0; i < 8; ++i) { const float x = Vr[i >> 2][i & 3], y = Vi[i >> 2][i & 3], wr = bflo(Wv[i]), wi = bfhi(Wv[i]);
          re[i] = x * wr - y * wi; im[i] = x * wi + y * wr; }
        #pragma unroll
        for (int jj = 0; jj < 4; ++jj) { tr_[jj] = pk2(re[2 * jj], re[2 * jj + 1]); ti_[jj] = pk2(im[2 * jj], im[2 * jj + 1]); } }
      Tr = mk8(tr_[0], tr_[1], tr_[2], tr_[3]); Ti = mk8(ti_[0], ti_[1], ti_[2], ti_[3]);
      f32x4 Ur[2] = {}, Ui[2] = {};
      { const bf16x8 nTi = neg8(Ti);
        #pragma unroll
        for (int t = 0; t < 2; ++t) { Ur[t] = mfma16(Hc[t], Tr, Ur[t]); Ui[t] = mfma16(Hc[t], Ti, Ui[t]); }
        #pragma unroll
        for (int t = 0; t < 2; ++t) { Ur[t] = mfma16(Hs[t], nTi, Ur[t]); Ui[t] = mfma16(Hs[t], Tr, Ui[t]); } }
      #pragma unroll
      for (int t = 0; t < 2; ++t) { float re[4], im[4];
        #pragma unroll
        for (int r = 0; r < 4; ++r) { const float x = Ur[t][r], y = Ui[t][r]; re[r] = x * twc[4 * t + r] - y * tws[4 * t + r]; im[r] = y * twc[4 * t + r] + x * tws[4 * t + r]; }
        char LAS* dst = (char LAS*)(size_t)ub + (16 * pw + c) * 72 + (16 * t + 4 * q) * 2;
        *(LAS u32x2*)dst = (u32x2){pk2(re[0], re[1]), pk2(re[2], re[3])};
        *(LAS u32x2*)(dst + 64 * 72) = (u32x2){pk2(im[0], im[1]), pk2(im[2], im[3])}; }
    }
    WAIT_VM(0);
    BAR();
    STAMP(10);
    if (has_m1 && tid == 0) flag_store(p.ws, FL_F2 + (rt * 16 + ct1) * FS, epoch);
    if (p2wave) {
      f32x4 yr[2] = {}, yi[2] = {};
      const int a = (l & 15) >> 2, pp = l & 3;
      #pragma unroll
      for (int u = 0; u < 2; ++u)
        #pragma unroll
        for (int t = 0; t < 2; ++t) {
          const unsigned ad = ub + (32 * u + 8 * q + a) * 72 + (8 * pp + 4 * t) * 2;
          const bf16x8 Ar = cat4(tr_read(ad), tr_read(ad + 4 * 72));
          const bf16x8 Ai = cat4(tr_read(ad + 64 * 72), tr_read(ad + 64 * 72 + 4 * 72));
          yr[t] = mfma16(Ar, Gc[u], yr[t]); yi[t] = mfma16(Ai, Gc[u], yi[t]);
          yr[t] = mfma16(neg8(Ai), Gs[u], yr[t]); yi[t] = mfma16(Ar, Gs[u], yi[t]);
        }
      const float ysc = (kcol == 0 || kcol == 256) ? 0.5f : 1.0f;
      const u32x4 yre = {pk2(yr[0][0] * ysc, yr[0][1] * ysc), pk2(yr[0][2] * ysc, yr[0][3] * ysc), pk2(yr[1][0] * ysc, yr[1][1] * ysc), pk2(yr[1][2] * ysc, yr[1][3] * ysc)};
      const u32x4 yim = {pk2(yi[0][0], yi[0][1]), pk2(yi[0][2], yi[0][3]), pk2(yi[1][0], yi[1][1]), pk2(yi[1][2], yi[1][3])};
      const unsigned so = (32 * (16 * pw + c) + 8 * q) * 2;
      st16_wt(yre, wsr, WS_Y + kcol * 4096 + so);
      st16_wt(yim, wsr, (kcol >= 1 && kcol <= 255) ? WS_Y + (256 + kcol) * 4096 + so : OOB);
    }
    STAMP(11);
    if (w == 4) spin4(p.ws, FL_F2 + ((pr >> 1) * 16 + (l < 14 ? l : 0)) * FS, epoch, l);
  }
  BAR();
  STAMP(12);
  unsigned gv; gload4_sc1(gv, (64 * pr + 32 * (w & 1) + (l & 31)) * 4, p.ws + WS_G);
  if (w >= 4) {
    #pragma unroll
    for (int u = 0; u < NHS; ++u) { M2_ISSUE(u, 0); M2_ISSUE(u, 1); M2_ISSUE(u, 2); M2_ISSUE(u, 3); } }
  f32x16 accb = {}, acca = {};
  {
    const int kh = w >> 2, nh = (w >> 1) & 1, sh = w & 1;
    const int h = l >> 5, blk = (l >> 4) & 1, a = (l & 15) >> 2, pp = l & 3;
    unsigned aoff[2];
    #pragma unroll
    for (int hh = 0; hh < 2; ++hh) { aoff[hh] = ldsb + hh * 65536 + kh * 8192 + (4 * h + a) * 128 + 16 * ((4 * nh + 2 * (pp & 1) + blk) ^ (4 * (a >> 1))) + 8 * (pp >> 1); PIN(aoff[hh]); }
    unsigned boffH[2], boffY[2];
    #pragma unroll
    for (int hh = 0; hh < 2; ++hh) { boffH[hh] = ldsb + L_HRING + hh * 49152 + kh * 8192 + sh * 4096 + l * 16; PIN(boffH[hh]);
      boffY[hh] = ldsb + L_HRING + hh * 49152 + kh * 8192 + (4 * h + a) * 128 + 16 * ((4 * sh + 2 * blk + (pp >> 1)) ^ (4 * (a >> 1))) + 8 * (pp & 1); PIN(boffY[hh]); }
    const int i31 = l & 31, dn = 64 * ct + 32 * nh + 16 * ((i31 >> 2) & 1) + 4 * (i31 >> 3) + (i31 & 3);
    const float dsc = (kh ? -1.0f : 1.0f) * (float)dn * (1.0f / 512.0f);
    const float cbase = (float)(4 * h) * dsc + (kh ? 0.0f : 0.25f);
    u32x4 Cf[16];
    auto cgen = [&](auto KS) { constexpr int ks = decltype(KS)::v; unsigned o[4];
      #pragma unroll
      for (int jj = 0; jj < 4; ++jj) { float v[2];
        #pragma unroll
        for (int e = 0; e < 2; ++e) { const int j = 2 * jj + e; v[e] = sin_rev(__builtin_fmaf((float)(16 * ks + 8 * (j >> 2) + (j & 3)), dsc, cbase)); }
        if (ks == 0 && jj == 0) { if (kh && h == 0) v[0] = (dn & 1) ? -1.0f : 1.0f; }
        o[jj] = pk2(v[0], v[1]); }
      Cf[ks] = (u32x4){o[0], o[1], o[2], o[3]}; PIN(Cf[ks]); };
    struct F2 { bf16x8 A[4], B[4]; } fa, fb;
    auto rdA = [&](auto ST, auto KS, F2& f) { constexpr int st = decltype(ST)::v, ks = decltype(KS)::v;
      if constexpr (st < 14) { constexpr unsigned o = (st % NWS) * 16384 + ks * 2048; f.A[ks] = cat4(tr_read(aoff[o >> 16] + (o & 65535)), tr_read(aoff[o >> 16] + (o & 65535) + 1024)); } };
    auto rdB = [&](auto ST, auto KS, F2& f) { constexpr int st = decltype(ST)::v, ks = decltype(KS)::v; constexpr int sl = st % NHS, hi = sl >= 3; constexpr unsigned o = (sl - 3 * hi) * 16384;
      if constexpr (st < 14) f.B[ks] = lds_read16(boffH[hi] + o + ks * 1024);
      else f.B[ks] = cat4(tr_read(boffY[hi] + o + ks * 2048), tr_read(boffY[hi] + o + ks * 2048 + 1024)); };
    if (w >= 4) { WAIT_VM(16); } else { WAIT_VM(0); }
    PIN(gv);
    BAR();
    if (tid == 0) flag_store(p.ws, FL_F3 + b * FS, epoch);
    STAMP(13);
    rdA(IC<0>{}, IC<0>{}, fa); rdB(IC<0>{}, IC<0>{}, fa); rdA(IC<0>{}, IC<1>{}, fa); rdB(IC<0>{}, IC<1>{}, fa);
    rdA(IC<0>{}, IC<2>{}, fa); rdB(IC<0>{}, IC<2>{}, fa); rdA(IC<0>{}, IC<3>{}, fa); rdB(IC<0>{}, IC<3>{}, fa);
    Poll f3v = {0, 0, 0, 0};
    auto step = [&](auto ST, F2& cur, F2& nxt) {
      constexpr int st = decltype(ST)::v;
      if constexpr (st <= 8) { WAIT_VM(12); }
      else if constexpr (st <= 12) {
        if (w < 4) {
          if constexpr (st <= 10) { WAIT_VM(8); } else if constexpr (st == 11) { WAIT_VM(4); } else { WAIT_VM(0); }
          if constexpr (st == 9) { if (w == 0) { poll_pin(f3v); spin_poll(f3v, p.ws + FL_F3, p.ws, epoch, l); } }
        } else { WAIT_VM(12); }
      }
      else if constexpr (st == 13) { WAIT_VM(12); } else if constexpr (st == 14) { WAIT_VM(8); } else if constexpr (st == 15) { WAIT_VM(4); } else if constexpr (st == 16) { WAIT_VM(0); }
      BAR();
      STAMP(30 + st);
      if constexpr (st == 7) { if (w == 0) poll_issue(f3v, l * FS, p.ws + FL_F3); }
      auto sub = [&](auto KS) { constexpr int ks = decltype(KS)::v;
        if constexpr (st < 14) accb = mfma32(cur.A[ks], cur.B[ks], accb); else acca = mfma32(as8(Cf[4 * (st - 14) + ks]), cur.B[ks], acca);
        SB();
        if constexpr (st < 17) { rdA(IC<st + 1>{}, KS, nxt); rdB(IC<st + 1>{}, KS, nxt); }
        if constexpr (st + 5 < 14) { M2_ISSUE(st + 5, ks); }
        else if constexpr (st + 5 < 18) { if (w >= 4) M2_ISSUE_Y(st + 5, ks); }
        if constexpr (ks == 1 && st < 16) cgen(IC<(st < 16 ? st : 0)>{});
        SB(); };
      sub(IC<0>{}); sub(IC<1>{}); sub(IC<2>{}); sub(IC<3>{});
    };
    step(IC<0>{}, fa, fb); step(IC<1>{}, fb, fa); step(IC<2>{}, fa, fb); step(IC<3>{}, fb, fa); step(IC<4>{}, fa, fb); step(IC<5>{}, fb, fa);
    step(IC<6>{}, fa, fb); step(IC<7>{}, fb, fa); step(IC<8>{}, fa, fb); step(IC<9>{}, fb, fa); step(IC<10>{}, fa, fb); step(IC<11>{}, fb, fa);
    step(IC<12>{}, fa, fb); step(IC<13>{}, fb, fa); step(IC<14>{}, fa, fb); step(IC<15>{}, fb, fa); step(IC<16>{}, fa, fb); step(IC<17>{}, fb, fa);
    STAMP(15);
    if (b == 0 && tid == 0) flag_store(p.ws, FL_E, epoch);
    BAR();
    LAS f32x4* ex = (LAS f32x4*)lds;
    ex[(w * 4 + 0) * 64 + l] = (f32x4){kh ? acca[0] : acca[8], kh ? acca[1] : acca[9], kh ? acca[2] : acca[10], kh ? acca[3] : acca[11]};
    ex[(w * 4 + 1) * 64 + l] = (f32x4){kh ? acca[4] : acca[12], kh ? acca[5] : acca[13], kh ? acca[6] : acca[14], kh ? acca[7] : acca[15]};
    ex[(w * 4 + 2) * 64 + l] = (f32x4){kh ? accb[0] : accb[8], kh ? accb[1] : accb[9], kh ? accb[2] : accb[10], kh ? accb[3] : accb[11]};
    ex[(w * 4 + 3) * 64 + l] = (f32x4){kh ? accb[4] : accb[12], kh ? accb[5] : accb[13], kh ? accb[6] : accb[14], kh ? accb[7] : accb[15]};
    BAR();
    const int pwv = w ^ 4;
    const f32x4 a0 = ex[(pwv * 4 + 0) * 64 + l], a1 = ex[(pwv * 4 + 1) * 64 + l], b0 = ex[(pwv * 4 + 2) * 64 + l], b1 = ex[(pwv * 4 + 3) * 64 + l];
    float av[8], bv[8];
    #pragma unroll
    for (int i = 0; i < 4; ++i) {
      av[i] = (kh ? acca[8 + i] : acca[i]) + a0[i]; av[4 + i] = (kh ? acca[12 + i] : acca[4 + i]) + a1[i];
      bv[i] = (kh ? accb[8 + i] : accb[i]) + b0[i]; bv[4 + i] = (kh ? accb[12 + i] : accb[4 + i]) + b1[i]; }
    const int s = 64 * pr + 32 * sh + (l & 31), n = 64 * ct + 32 * nh + 16 * h + 8 * kh;
    const float g = __uint_as_float(gv), omg = rbf(1.0f - g);
    constexpr float ASC = 2.0f / 1048576.0f;
    unsigned o4[4];
    #pragma unroll
    for (int i = 0; i < 4; ++i) {
      const float v0 = rbf(g * rbf(av[2 * i] * ASC)) + rbf(omg * rbf(bv[2 * i])), v1 = rbf(g * rbf(av[2 * i + 1] * ASC)) + rbf(omg * rbf(bv[2 * i + 1]));
      o4[i] = pk2(v0, v1); }
    *(GAS u32x4*)((char*)p.out + (size_t)s * 1024 + n * 2) = (u32x4){o4[0], o4[1], o4[2], o4[3]};
  }
  STAMP(16);
  STAMP_FLUSH();
}

extern "C" void kernel_launch(void* const* d_in, const int* in_sizes, int n_in, void* d_out, int out_size, void* d_ws, size_t ws_size, hipStream_t stream) {
  Params p;
  p.ax = (const uint16_t*)d_in[0]; p.wr = (const uint16_t*)d_in[1]; p.wi = (const uint16_t*)d_in[2];
  p.w1 = (const uint16_t*)d_in[4]; p.w2 = (const uint16_t*)d_in[5]; p.gw = (const uint16_t*)d_in[6];
  p.out = (uint16_t*)d_out; p.ws = (char*)d_ws;
  (void)hipFuncSetAttribute((const void*)fused_kernel, hipFuncAttributeMaxDynamicSharedMemorySize, 163840);
  fused_kernel<<<256, 512, 163840, stream>>>(p);
}
```

```cpp
#define GAPMODE 4
#include <hip/hip_runtime.h>
#include <hip/hip_bf16.h>
#include <stdint.h>

typedef short    bf16x8 __attribute__((ext_vector_type(8)));
typedef short    s16x4  __attribute__((ext_vector_type(4)));
typedef float    f32x2  __attribute__((ext_vector_type(2)));
typedef float    f32x4  __attribute__((ext_vector_type(4)));
typedef float    f32x16 __attribute__((ext_vector_type(16)));
typedef unsigned u32x2  __attribute__((ext_vector_type(2)));
typedef unsigned u32x4  __attribute__((ext_vector_type(4)));
typedef __bf16   bf16x2_t __attribute__((ext_vector_type(2)));
#define LAS __attribute__((address_space(3)))
#define GAS __attribute__((address_space(1)))
#define DI __device__ __forceinline__

constexpr int F_ = 1792, DF = 257;

constexpr unsigned XJ_STRIDE = 260 * 32;
constexpr unsigned FS = 128;
constexpr unsigned FL_F1 = 0, FL_F3 = 256 * FS, FL_F2 = 512 * FS, FL_E = 768 * FS, FL_ERR = 769 * FS;
constexpr unsigned WS_X  = 772 * FS;
constexpr unsigned WS_WT = WS_X + 256 * XJ_STRIDE;
constexpr unsigned WS_Y  = WS_WT + 257 * 8192;
constexpr unsigned WS_G  = WS_Y + 512 * 4096;
constexpr unsigned WS_H  = WS_G + 8192;
constexpr unsigned WS_END = WS_H + 64 * 112 * 1024;

struct Params {
  const uint16_t *ax, *wr, *wi, *w1, *w2, *gw;
  uint16_t* out;
  char* ws;
};

DI unsigned pk2(float lo, float hi) { return __builtin_bit_cast(unsigned, __builtin_convertvector((f32x2){lo, hi}, bf16x2_t)); }
DI float bflo(unsigned u) { return __uint_as_float(u << 16); }
DI float bfhi(unsigned u) { return __uint_as_float(u & 0xffff0000u); }
DI float rbf(float x) { return bflo(pk2(x, x)); }
DI float sin_rev(float x) { return __builtin_amdgcn_sinf(x); }
DI float cos_rev(float x) { return __builtin_amdgcn_cosf(x); }
DI f32x4 mfma16(bf16x8 a, bf16x8 b, f32x4 c) { return __builtin_amdgcn_mfma_f32_16x16x32_bf16(a, b, c, 0, 0, 0); }
DI f32x16 mfma32(bf16x8 a, bf16x8 b, f32x16 c) { return __builtin_amdgcn_mfma_f32_32x32x16_bf16(a, b, c, 0, 0, 0); }
DI bf16x8 mk8(unsigned a, unsigned b, unsigned c, unsigned d) { return __builtin_bit_cast(bf16x8, (u32x4){a, b, c, d}); }
DI bf16x8 as8(u32x4 v) { return __builtin_bit_cast(bf16x8, v); }
DI u32x4 xor4(u32x4 v, unsigned m) { v ^= m; return v; }
DI bf16x8 neg8(bf16x8 v) { return as8(xor4(__builtin_bit_cast(u32x4, v), 0x80008000u)); }
DI s16x4 tr_read(unsigned addr) { return __builtin_amdgcn_ds_read_tr16_b64_v4i16((LAS s16x4*)addr); }
DI bf16x8 lds_read16(unsigned addr) { return *(const LAS bf16x8*)addr; }
DI unsigned lds_read4(unsigned addr) { return *(const LAS unsigned*)addr; }
DI unsigned lds_read2(unsigned addr) { return *(const LAS unsigned short*)addr; }
DI bf16x8 cat4(s16x4 lo, s16x4 hi) { return __builtin_shufflevector(lo, hi, 0, 1, 2, 3, 4, 5, 6, 7); }
#define WAIT_VM(n) asm volatile("s_waitcnt vmcnt(%0)" :: "n"(n) : "memory")
#define WAIT_LGKM0() asm volatile("s_waitcnt lgkmcnt(0)" ::: "memory")
#define SB() __builtin_amdgcn_sched_barrier(0)
#define BARE_BAR() do { SB(); asm volatile("" ::: "memory"); __builtin_amdgcn_s_barrier(); asm volatile("" ::: "memory"); SB(); } while (0)
#define BAR() do { SB(); WAIT_LGKM0(); __builtin_amdgcn_s_barrier(); asm volatile("" ::: "memory"); SB(); } while (0)
#define PIN(x) asm volatile("" : "+v"(x))

DI void glds(unsigned voff, const void* sbase, unsigned dst) {
  asm volatile("s_mov_b32 m0, %2\n\ts_nop 0\n\tglobal_load_lds_dwordx4 %0, %1" :: "v"(voff), "s"(sbase), "s"(dst) : "memory");
}
DI void glds_sc1(unsigned voff, const void* sbase, unsigned dst) {
  asm volatile("s_mov_b32 m0, %2\n\ts_nop 0\n\tglobal_load_lds_dwordx4 %0, %1 sc1" :: "v"(voff), "s"(sbase), "s"(dst) : "memory");
}
DI void gload16(u32x4& d, unsigned voff, const void* sbase) { asm volatile("global_load_dwordx4 %0, %1, %2" : "=v"(d) : "v"(voff), "s"(sbase) : "memory"); }
DI void gload16_sc1(u32x4& d, unsigned voff, const void* sbase) { asm volatile("global_load_dwordx4 %0, %1, %2 sc1" : "=v"(d) : "v"(voff), "s"(sbase) : "memory"); }
DI void gload4_sc1(unsigned& d, unsigned voff, const void* sbase) { asm volatile("global_load_dword %0, %1, %2 sc1" : "=v"(d) : "v"(voff), "s"(sbase) : "memory"); }
typedef __amdgpu_buffer_rsrc_t rsrc_t;
DI void st16_wt(u32x4 v, rsrc_t r, unsigned off) { __builtin_amdgcn_raw_buffer_store_b128(v, r, off, 0, 16); }
DI void st4_wt(unsigned v, rsrc_t r, unsigned off) { __builtin_amdgcn_raw_buffer_store_b32(v, r, off, 0, 16); }
constexpr unsigned OOB = 0x80000000u;
DI void flag_store(char* ws, unsigned off, unsigned epoch) { __hip_atomic_store((GAS unsigned*)(ws + off), epoch, __ATOMIC_RELAXED, __HIP_MEMORY_SCOPE_AGENT); }

#define STAMP(i) do {} while (0)
#define STAMP_DECL const unsigned long long gm_t0_ = __builtin_amdgcn_s_memrealtime();
#define STAMP_FLUSH() do { const unsigned long long e_ = __builtin_amdgcn_s_memrealtime(); \
  if (GAPMODE == 1) { const unsigned long long k0_ = __hip_atomic_load((GAS unsigned long long*)(p.ws + (128u << 20)), __ATOMIC_RELAXED, __HIP_MEMORY_SCOPE_AGENT); \
    const unsigned long long d_ = 4 * (gm_t0_ - k0_); while (__builtin_amdgcn_s_memrealtime() - e_ < d_) __builtin_amdgcn_s_sleep(1); } \
  if (GAPMODE == 3 || GAPMODE == 4) { if (threadIdx.x == 0) __hip_atomic_store((GAS unsigned long long*)(p.ws + (128u << 20)) + 16 + blockIdx.x, e_, __ATOMIC_RELAXED, __HIP_MEMORY_SCOPE_AGENT); } } while (0)

struct Poll { unsigned a, b, c, d; };
DI void poll_issue(Poll& v, unsigned voff, const char* ws) { gload4_sc1(v.a, voff, ws); gload4_sc1(v.b, voff, ws + 64 * FS); gload4_sc1(v.c, voff, ws + 128 * FS); gload4_sc1(v.d, voff, ws + 192 * FS); }
DI void poll_pin(Poll& v) { asm volatile("" : "+v"(v.a), "+v"(v.b), "+v"(v.c), "+v"(v.d)); }
DI void spin_poll(Poll& v, const char* base, char* ws, unsigned epoch, int l) {
  if (__all(v.a == epoch && v.b == epoch && v.c == epoch && v.d == epoch)) return;
  const unsigned long long t0 = __builtin_amdgcn_s_memrealtime();
  for (;;) {
    __builtin_amdgcn_s_sleep(1);
    poll_issue(v, l * FS, base); WAIT_VM(0); poll_pin(v);
    if (__all(v.a == epoch && v.b == epoch && v.c == epoch && v.d == epoch)) break;
    if (__builtin_amdgcn_s_memrealtime() - t0 > 2000000ull) { if (l == 0) flag_store(ws, FL_ERR, 1u); break; }
  }
}
DI void spin4(char* ws, unsigned voff, unsigned epoch, int l) {
  const unsigned long long t0 = __builtin_amdgcn_s_memrealtime();
  for (;;) {
    unsigned v; gload4_sc1(v, voff, ws); WAIT_VM(0); PIN(v);
    if (__all(v == epoch)) break;
    __builtin_amdgcn_s_sleep(1);
    if (__builtin_amdgcn_s_memrealtime() - t0 > 2000000ull) { if (l == 0) flag_store(ws, FL_ERR, 1u); break; }
  }
}

constexpr unsigned L_AUX   = 131072;
constexpr unsigned L_XROWS = L_AUX;
constexpr unsigned L_ZS    = L_AUX + 16384;
constexpr unsigned L_WSCR  = L_AUX + 16384;
constexpr unsigned L_XCOL  = L_AUX;
constexpr unsigned L_WCOL  = L_AUX + 8192;
constexpr unsigned L_U     = L_AUX + 16384;
constexpr unsigned L_COL2  = 81920;
constexpr unsigned L_HRING = 81920;
constexpr int NWS = 5, NHS = 5;

template <int N> struct IC { static constexpr int v = N; };

__global__ __launch_bounds__(512, 2) void fused_kernel(Params p) {
  extern __shared__ __attribute__((aligned(16))) char smem[];
  const unsigned ldsb = (unsigned)(size_t)(char LAS*)smem;
  char LAS* lds = (char LAS*)smem;
  const int b = blockIdx.x, tid = threadIdx.x;
  const int w = __builtin_amdgcn_readfirstlane(tid >> 6), l = tid & 63;
  const int xcd = b & 7, jx = b >> 3;
  const bool has_m1 = jx < 28;
  const int rt = 4 * (xcd >> 1) + jx / 7, ct1 = 7 * (xcd & 1) + jx % 7;
  const int s0 = 128 * rt, f0 = 128 * ct1;
  const rsrc_t wsr = __builtin_amdgcn_make_buffer_rsrc(p.ws, 0, WS_END, 0x00020000);
  const int c = l & 15, q = l >> 4;
  STAMP_DECL
  STAMP(0);

  const int n2 = jx, gq = xcd, pj = xcd * 32 + jx, pr4 = w & 3, t1 = w >> 2;
  { const char* xr = (const char*)p.ax + (size_t)(n2 + 256 * gq + 64 * pr4) * 1024;
    glds(l * 16, xr, ldsb + L_XROWS + w * 2048); glds(l * 16, xr + 32 * 1024, ldsb + L_XROWS + w * 2048 + 1024); }
  u32x4 gwv; gload16(gwv, l * 16, p.gw);
  unsigned ev; gload4_sc1(ev, FL_E, p.ws);
  unsigned vx[2], vw[2];
  #pragma unroll
  for (int i = 0; i < 2; ++i) { const int row = 8 * (2 * w + i) + (l >> 3); vx[i] = row * 1024 + ((l & 7) ^ ((row >> 1) & 7)) * 16;
    const int r2 = 4 * (2 * w + i) + (l >> 4); vw[i] = r2 * (F_ * 2) + ((l & 15) ^ ((r2 & 3) << 2)) * 16; }
  const char* xbase = (const char*)p.ax + (size_t)s0 * 1024;
  const char* w1base = (const char*)p.w1 + f0 * 2;
  const unsigned rot1 = (jx % 7 + 2 * (jx / 7)) & 7;
  const unsigned m1dst = __builtin_amdgcn_readfirstlane(ldsb + w * 2048);
  #define M1_ISSUE(s, i) do { const unsigned ks_ = ((s) + rot1) & 7; \
      if ((i) < 2) glds(vx[(i) & 1] + ks_ * 128, xbase, m1dst + ((s) & 3) * 32768 + ((i) & 1) * 1024); \
      else glds(vw[(i) & 1] + ks_ * (64 * F_ * 2), w1base, m1dst + ((s) & 3) * 32768 + 16384 + ((i) & 1) * 1024); } while (0)
  if (has_m1) { M1_ISSUE(0, 0); M1_ISSUE(0, 2); M1_ISSUE(0, 1); M1_ISSUE(0, 3); }
  bf16x8 Fc, Fs, Fns, Are, Aim; float p1c[4], p1s[4];
  { const float kf = (float)(16 * t1 + c), k32 = kf * (1.0f / 32.0f), q4 = (float)(4 * q);
    unsigned uc[4], us[4];
    #pragma unroll
    for (int jj = 0; jj < 4; ++jj) { float cv[2], sv[2];
      #pragma unroll
      for (int e = 0; e < 2; ++e) { const int j = 2 * jj + e; const float ang = ((float)(j < 4 ? j : 12 + j) + q4) * k32; cv[e] = cos_rev(ang); sv[e] = sin_rev(ang); }
      uc[jj] = pk2(cv[0], cv[1]); us[jj] = pk2(sv[0], sv[1]); PIN(uc[jj]); PIN(us[jj]); }
    Fc = mk8(uc[0], uc[1], uc[2], uc[3]); Fs = mk8(us[0], us[1], us[2], us[3]); Fns = neg8(Fs);
    unsigned ur[4], ui[4];
    #pragma unroll
    for (int r = 0; r < 4; ++r) { const float nr = q4 + (float)r; const float ang = nr * ((float)c * (1.0f / 16.0f)); const float cv = cos_rev(ang), sv = sin_rev(ang);
      ur[r] = pk2(cv, sv); ui[r] = pk2(-sv, cv);
      const float a2 = nr * (kf * (1.0f / 512.0f)); p1c[r] = 0.5f * cos_rev(a2); p1s[r] = 0.5f * sin_rev(a2);
      PIN(ur[r]); PIN(ui[r]); PIN(p1c[r]); PIN(p1s[r]); }
    Are = mk8(ur[0], ur[1], ur[2], ur[3]); Aim = mk8(ui[0], ui[1], ui[2], ui[3]); }
  SB();
  if (has_m1) { M1_ISSUE(1, 0); M1_ISSUE(1, 2); M1_ISSUE(1, 1); M1_ISSUE(1, 3); }
  STAMP(1);
  if (has_m1) { WAIT_VM(8); } else { WAIT_VM(0); }
  PIN(gwv); PIN(ev);
  const unsigned epoch = __builtin_amdgcn_readfirstlane(ev) + 1u;
  STAMP(2);
  { const unsigned xa = ldsb + L_XROWS + w * 2048 + l * 8, xb = xa + 1024;
    const bf16x8 A_a = cat4(tr_read(xa), tr_read(xa + 512)), A_b = cat4(tr_read(xb), tr_read(xb + 512));
    const u32x4 xv = __builtin_bit_cast(u32x4, lds_read16(ldsb + L_XROWS + w * 2048 + t1 * 1024 + l * 16));
    f32x4 Tr = {0, 0, 0, 0}, Ti = {0, 0, 0, 0};
    Tr = mfma16(A_a, Fc, Tr); Ti = mfma16(A_a, Fns, Ti);
    Tr = mfma16(A_b, Fs, Tr); Ti = mfma16(A_b, Fc, Ti);
    float sum = 0.f;
    #pragma unroll
    for (int i = 0; i < 4; ++i) { sum += rbf(bflo(xv[i]) * bflo(gwv[i])); sum += rbf(bfhi(xv[i]) * bfhi(gwv[i])); }
    PIN(sum);
    unsigned tb[4];
    #pragma unroll
    for (int r = 0; r < 4; ++r) { const float re = Tr[r] * p1c[r] + Ti[r] * p1s[r], im = Ti[r] * p1c[r] - Tr[r] * p1s[r]; tb[r] = pk2(re, im); }
    const bf16x8 Bt = mk8(tb[0], tb[1], tb[2], tb[3]);
    f32x4 Zr = {0, 0, 0, 0}, Zi = {0, 0, 0, 0};
    Zr = mfma16(Are, Bt, Zr); Zi = mfma16(Aim, Bt, Zi);
    sum += __builtin_bit_cast(float, __builtin_amdgcn_update_dpp(0, __builtin_bit_cast(int, sum), 0x111, 0xf, 0xf, true));
    sum += __builtin_bit_cast(float, __builtin_amdgcn_update_dpp(0, __builtin_bit_cast(int, sum), 0x112, 0xf, 0xf, true));
    sum += __builtin_bit_cast(float, __builtin_amdgcn_update_dpp(0, __builtin_bit_cast(int, sum), 0x114, 0xf, 0xf, true));
    sum += __builtin_bit_cast(float, __builtin_amdgcn_update_dpp(0, __builtin_bit_cast(int, sum), 0x118, 0xf, 0xf, true));
    sum += __builtin_bit_cast(float, __builtin_amdgcn_update_dpp(0, __builtin_bit_cast(int, sum), 0x142, 0xa, 0xf, true));
    sum += __builtin_bit_cast(float, __builtin_amdgcn_update_dpp(0, __builtin_bit_cast(int, sum), 0x143, 0xc, 0xf, true));
    LAS f32x2* Zs = (LAS f32x2*)(lds + L_ZS);
    #pragma unroll
    for (int r = 0; r < 4; ++r) Zs[pr4 * 512 + 16 * t1 + c + 32 * (4 * q + r)] = (f32x2){Zr[r], Zi[r]};
    const float gg = rbf(__builtin_amdgcn_rcpf(1.0f + __expf(-rbf(sum))));
    BAR();
    STAMP(3);
    { const int k = tid >> 1, c2 = tid & 1; f32x2 z1[4], z2[4];
      #pragma unroll
      for (int pp = 0; pp < 4; ++pp) { z1[pp] = Zs[pp * 512 + k]; z2[pp] = Zs[pp * 512 + ((512 - k) & 511)]; }
      unsigned o[4];
      #pragma unroll
      for (int pp = 0; pp < 4; ++pp) o[pp] = c2 == 0 ? pk2(z1[pp][0] + z2[pp][0], z1[pp][1] + z2[pp][1]) : pk2(z1[pp][1] - z2[pp][1], z2[pp][0] - z1[pp][0]);
      st16_wt((u32x4){o[0], o[1], o[2], o[3]}, wsr, WS_X + pj * XJ_STRIDE + k * 32 + c2 * 16);
      st4_wt(__float_as_uint(gg), wsr, l == 63 ? WS_G + (n2 + 256 * gq + 64 * pr4 + 32 * t1) * 4 : OOB);
      if (w == 0) { unsigned o2[4];
        #pragma unroll
        for (int pp = 0; pp < 4; ++pp) { const f32x2 z = Zs[pp * 512 + 256]; o2[pp] = c2 == 0 ? pk2(z[0] + z[0], z[1] + z[1]) : 0u; }
        st16_wt((u32x4){o2[0], o2[1], o2[2], o2[3]}, wsr, tid < 2 ? WS_X + pj * XJ_STRIDE + 256 * 32 + c2 * 16 : OOB); }
    } }
  if (has_m1) { M1_ISSUE(2, 0); M1_ISSUE(2, 2); M1_ISSUE(2, 1); M1_ISSUE(2, 3); M1_ISSUE(3, 0); M1_ISSUE(3, 2); M1_ISSUE(3, 1); M1_ISSUE(3, 3); }
  STAMP(4);

  const int ct = jx & 7, pr = 4 * xcd + (jx >> 3);
  const bool p2wave = w < 4 || b == 255;
  const int pw = w & 3;
  const int kcol = w < 4 ? xcd * 32 + jx : 256;
  const unsigned vrow = (l >> 3) * 1024 + ((l & 7) ^ (4 * ((l >> 4) & 1))) * 16;
  const unsigned vrowY = (l >> 3) * 4096 + ((l & 7) ^ (4 * ((l >> 4) & 1))) * 16;
  const unsigned rot2 = ((7 * ct) / 4 + (7 * (jx >> 3)) / 2) % 14;
  #define M2_STAGE(u) (((u) + rot2) >= 14 ? ((u) + rot2 - 14) : ((u) + rot2))
  const unsigned vw8 = vrow + ((w >> 2) * 896 + (w & 3) * 16) * 1024 + ct * 128;
  const unsigned w8dst = __builtin_amdgcn_readfirstlane(ldsb + (w >> 2) * 8192 + (w & 3) * 2048);
  #define M2_ISSUE_W8(u, i) glds(vw8 + M2_STAGE(u) * 65536 + (i) * 8192, p.w2, w8dst + ((u) % NWS) * 16384 + (i) * 1024)
  unsigned vst[4];
  #pragma unroll
  for (int i = 0; i < 4; ++i)
    vst[i] = w < 4 ? vrow + ((i >> 1) * 896 + (w & 3) * 16 + (i & 1) * 8) * 1024 + ct * 128
                   : l * 16 + ((2 * pr + ((w & 3) >> 1)) * 112 + (i >> 1) * 56 + 2 * (w & 1) + (i & 1)) * 1024;
  const char* stbase = w < 4 ? (const char*)p.w2 : p.ws + WS_H;
  const unsigned stmul = w < 4 ? 65536u : 4096u;
  const unsigned stdst = __builtin_amdgcn_readfirstlane(ldsb + (w < 4 ? 0u : L_HRING) + (w & 3) * 2048);
  #define M2_ISSUE(u, i) glds_sc1(vst[i] + M2_STAGE(u) * stmul, stbase, stdst + ((u) % 5) * 16384 + ((i) >> 1) * 8192 + ((i) & 1) * 1024)
  #define M2_ISSUE_Y(u, i) glds_sc1(vrowY + (((i) >> 1) * 256 + ((u) - 14) * 64 + ((i) & 1) * 8) * 4096 + (w & 3) * 65536 + pr * 128, p.ws + WS_Y, stdst + ((u) % 5) * 16384 + ((i) >> 1) * 8192 + ((i) & 1) * 1024)
  const int m0 = 32 * (b >> 2), kq = b & 3;

  unsigned gcu[4], gsu[4], hcu[4], hsu[4]; float twc[8], tws[8];
  const float k1f = (float)(16 * pw + c), q4f = (float)(4 * q);
  auto p2const = [&](auto IDX) {
    constexpr int i = decltype(IDX)::v;
    if constexpr (i < 4) {
      const float k64 = k1f * (1.0f / 64.0f); const float a0 = (2.0f * q4f + (float)(2 * i)) * k64, a1 = (2.0f * q4f + (float)(2 * i + 1)) * k64;
      gcu[i] = pk2(cos_rev(a0), cos_rev(a1)); gsu[i] = pk2(sin_rev(a0), sin_rev(a1)); PIN(gcu[i]); PIN(gsu[i]);
    } else if constexpr (i < 8) {
      constexpr int jj = i - 4; const float c32 = (float)c * (1.0f / 32.0f);
      const float a0 = (q4f + (float)(2 * jj < 4 ? 2 * jj : 12 + 2 * jj)) * c32, a1 = (q4f + (float)(2 * jj + 1 < 4 ? 2 * jj + 1 : 13 + 2 * jj)) * c32;
      hcu[jj] = pk2(cos_rev(a0), cos_rev(a1)); hsu[jj] = pk2(sin_rev(a0), sin_rev(a1)); PIN(hcu[jj]); PIN(hsu[jj]);
    } else {
      constexpr int ii = i - 8; const float a = (q4f + (float)(16 * (ii >> 2) + (ii & 3))) * (k1f * (1.0f / 2048.0f));
      twc[ii] = cos_rev(a); tws[ii] = sin_rev(a); PIN(twc[ii]); PIN(tws[ii]);
    } };

  f32x16 acc1[2] = {};
  Poll f1v = {0, 0, 0, 0};
  if (has_m1) {
    const int fw = w & 1, sw = w >> 1;
    const int h = l >> 5, blk = (l >> 4) & 1, a = (l & 15) >> 2, pp = l & 3;
    const int xrow = 32 * sw + (l & 31);
    unsigned xo[2][4], wo0[2], wo1[2];
    #pragma unroll
    for (int hh = 0; hh < 2; ++hh) {
      #pragma unroll
      for (int ks = 0; ks < 4; ++ks) { xo[hh][ks] = ldsb + hh * 65536 + xrow * 128 + 16 * ((2 * ks + h) ^ ((xrow >> 1) & 7)); PIN(xo[hh][ks]); }
      wo0[hh] = ldsb + hh * 65536 + 16384 + (8 * h + a) * 256 + 16 * (((8 * fw + 0) ^ (a << 2)) + 2 * blk + (pp >> 1)) + 8 * (pp & 1); PIN(wo0[hh]);
      wo1[hh] = ldsb + hh * 65536 + 16384 + (8 * h + a) * 256 + 16 * (((8 * fw + 4) ^ (a << 2)) + 2 * blk + (pp >> 1)) + 8 * (pp & 1); PIN(wo1[hh]); }
    struct Fr { bf16x8 B, A0, A1; } fa, fb;
    #define RB(f, g)  f.B  = lds_read16(xo[(((g) >> 2) & 3) >> 1][(g) & 3] + (((g) >> 2) & 1) * 32768)
    #define RA(base_, g) cat4(tr_read(base_[(((g) >> 2) & 3) >> 1] + (((g) >> 2) & 1) * 32768 + ((g) & 3) * 4096), tr_read(base_[(((g) >> 2) & 3) >> 1] + (((g) >> 2) & 1) * 32768 + ((g) & 3) * 4096 + 1024))
    unsigned wd[4], wde = 0;
    WAIT_VM(9); BARE_BAR();
    STAMP(5);
    RB(fa, 0); fa.A0 = RA(wo0, 0); fa.A1 = RA(wo1, 0);
    auto kstep = [&](auto G, Fr& cur, Fr& nxt) {
      constexpr int g = decltype(G)::v, st = g >> 2, ks = g & 3;
      if constexpr (ks == 0 && st > 0) {
        if constexpr (st == 1) { WAIT_VM(5); } else if constexpr (st == 2) { WAIT_VM(4); } else if constexpr (st == 3) { WAIT_VM(5); } else if constexpr (st == 4) { WAIT_VM(4); } else if constexpr (st <= 6) { if (w == 0) { WAIT_VM(5); } else { WAIT_VM(4); } }
        BARE_BAR();
        STAMP(20 + st);
        if constexpr (st == 6) { if (w == 0) poll_issue(f1v, l * FS, p.ws + FL_F1); }
      }
      acc1[0] = mfma32(cur.A0, cur.B, acc1[0]); SB();
      if constexpr (g + 1 < 32) { RB(nxt, g + 1); nxt.A0 = RA(wo0, g + 1); }
      SB();
      acc1[1] = mfma32(cur.A1, cur.B, acc1[1]); SB();
      if constexpr (g + 1 < 32) { nxt.A1 = RA(wo1, g + 1); }
      if constexpr (st >= 1 && st <= 4) { M1_ISSUE(st + 3, ks); }
      if constexpr (st >= 5 && 2 * (st - 5) + (ks >> 1) < NWS) { M2_ISSUE_W8(2 * (st - 5) + (ks >> 1), ks & 1); }
      if constexpr (g == 0) { const int ra = 8 * w + (l >> 3), row = ra >> 1;
        const unsigned off = (((unsigned)((m0 + row) * (DF * 2) + 128 * kq)) & ~15u) + (l & 7) * 16;
        const char* src = ((ra & 1) ? (const char*)p.wi : (const char*)p.wr) + off;
        asm volatile("s_mov_b32 m0, %1\n\ts_nop 0\n\tglobal_load_lds_dwordx4 %0, off" :: "v"(src), "s"(ldsb + L_WSCR + w * 1024) : "memory"); }
      if constexpr (g == 1) { if (w == 0) { const int row = l >> 1;
        const unsigned off = (((unsigned)((m0 + row) * (DF * 2) + 128 * kq)) & ~15u) + 128;
        const char* src = ((l & 1) ? (const char*)p.wi : (const char*)p.wr) + off;
        asm volatile("s_mov_b32 m0, %1\n\ts_nop 0\n\tglobal_load_lds_dwordx4 %0, off" :: "v"(src), "s"(ldsb + L_WSCR + 8192) : "memory"); } }
      if constexpr (g == 8) {
        const int cc = l & 7, rg = l >> 3, j = 8 * w + cc;
        #pragma unroll
        for (int i = 0; i < 4; ++i) { const int row = 4 * rg + i; const int e = (((m0 + row) * (DF * 2)) & 15) + 2 * j;
          const unsigned ad0 = e < 128 ? ldsb + L_WSCR + (2 * row) * 128 + e : ldsb + L_WSCR + 8192 + (2 * row) * 16 + (e - 128);
          const unsigned ad1 = e < 128 ? ldsb + L_WSCR + (2 * row + 1) * 128 + e : ldsb + L_WSCR + 8192 + (2 * row + 1) * 16 + (e - 128);
          wd[i] = lds_read2(ad0) | (lds_read2(ad1) << 16); }
        { const int row = l & 31; const int e = (((m0 + row) * (DF * 2)) & 15) + 128;
          wde = lds_read2(ldsb + L_WSCR + 8192 + (2 * row) * 16 + (e - 128)) | (lds_read2(ldsb + L_WSCR + 8192 + (2 * row + 1) * 16 + (e - 128)) << 16); } }
      if constexpr (g == 9) { const int cc = l & 7, rg = l >> 3;
        st16_wt((u32x4){wd[0], wd[1], wd[2], wd[3]}, wsr, WS_WT + (64 * kq + 8 * w + cc) * 8192 + (m0 + 4 * rg) * 4);
        if (w == 0) st4_wt(wde, wsr, (kq == 3 && l < 32) ? WS_WT + 256 * 8192 + (m0 + l) * 4 : OOB); }
      if constexpr (g == 19) { if (tid == 0) flag_store(p.ws, FL_F1 + b * FS, epoch); }
      if constexpr (g >= 12 && g < 28) { if (p2wave) p2const(IC<g - 12>{}); }
      SB();
    };
    #define K2(g) kstep(IC<g>{}, fa, fb); kstep(IC<g + 1>{}, fb, fa);
    K2(0) K2(2) K2(4) K2(6) K2(8) K2(10) K2(12) K2(14) K2(16) K2(18) K2(20) K2(22) K2(24) K2(26) K2(28) K2(30)
    STAMP(6);
    if (w == 0) { WAIT_VM(6); poll_pin(f1v); spin_poll(f1v, p.ws + FL_F1, p.ws, epoch, l); }
  } else {
    { const int ra = 8 * w + (l >> 3), row = ra >> 1;
      const unsigned off = (((unsigned)((m0 + row) * (DF * 2) + 128 * kq)) & ~15u) + (l & 7) * 16;
      const char* src = ((ra & 1) ? (const char*)p.wi : (const char*)p.wr) + off;
      BAR();
      asm volatile("s_mov_b32 m0, %1\n\ts_nop 0\n\tglobal_load_lds_dwordx4 %0, off" :: "v"(src), "s"(ldsb + L_WSCR + w * 1024) : "memory"); }
    if (w == 0) { const int row = l >> 1;
      const unsigned off = (((unsigned)((m0 + row) * (DF * 2) + 128 * kq)) & ~15u) + 128;
      const char* src = ((l & 1) ? (const char*)p.wi : (const char*)p.wr) + off;
      asm volatile("s_mov_b32 m0, %1\n\ts_nop 0\n\tglobal_load_lds_dwordx4 %0, off" :: "v"(src), "s"(ldsb + L_WSCR + 8192) : "memory"); }
    if (p2wave) { p2const(IC<0>{}); p2const(IC<1>{}); p2const(IC<2>{}); p2const(IC<3>{}); p2const(IC<4>{}); p2const(IC<5>{}); p2const(IC<6>{}); p2const(IC<7>{});
                  p2const(IC<8>{}); p2const(IC<9>{}); p2const(IC<10>{}); p2const(IC<11>{}); p2const(IC<12>{}); p2const(IC<13>{}); p2const(IC<14>{}); p2const(IC<15>{}); }
    WAIT_VM(0); BAR();
    { const int cc = l & 7, rg = l >> 3, j = 8 * w + cc; unsigned wd[4];
      #pragma unroll
      for (int i = 0; i < 4; ++i) { const int row = 4 * rg + i; const int e = (((m0 + row) * (DF * 2)) & 15) + 2 * j;
        const unsigned ad0 = e < 128 ? ldsb + L_WSCR + (2 * row) * 128 + e : ldsb + L_WSCR + 8192 + (2 * row) * 16 + (e - 128);
        const unsigned ad1 = e < 128 ? ldsb + L_WSCR + (2 * row + 1) * 128 + e : ldsb + L_WSCR + 8192 + (2 * row + 1) * 16 + (e - 128);
        wd[i] = lds_read2(ad0) | (lds_read2(ad1) << 16); }
      st16_wt((u32x4){wd[0], wd[1], wd[2], wd[3]}, wsr, WS_WT + (64 * kq + 8 * w + cc) * 8192 + (m0 + 4 * rg) * 4);
      if (w == 0) { const int row = l & 31; const int e = (((m0 + row) * (DF * 2)) & 15) + 128;
        const unsigned wde = lds_read2(ldsb + L_WSCR + 8192 + (2 * row) * 16 + (e - 128)) | (lds_read2(ldsb + L_WSCR + 8192 + (2 * row + 1) * 16 + (e - 128)) << 16);
        st4_wt(wde, wsr, (kq == 3 && l < 32) ? WS_WT + 256 * 8192 + (m0 + l) * 4 : OOB); } }
    WAIT_VM(0); BAR();
    if (tid == 0) flag_store(p.ws, FL_F1 + b * FS, epoch);
    #pragma unroll
    for (int u = 0; u < NWS; ++u) { M2_ISSUE_W8(u, 0); M2_ISSUE_W8(u, 1); }
    STAMP(6);
    if (w == 0) { poll_issue(f1v, l * FS, p.ws + FL_F1); WAIT_VM(0); poll_pin(f1v); spin_poll(f1v, p.ws + FL_F1, p.ws, epoch, l); }
  }
  BAR();
  STAMP(7);
  { const unsigned wofs = __builtin_amdgcn_readfirstlane(ldsb + w * 1024);
    const unsigned vg = (64 * (w & 3) + l) * XJ_STRIDE + (w >> 2) * 16, vt = tid * 16;
    glds_sc1(vg, p.ws + WS_X + (xcd * 32 + jx) * 32, wofs + L_XCOL);
    glds_sc1(vt, p.ws + WS_WT + (size_t)(xcd * 32 + jx) * 8192, wofs + L_WCOL);
    if (b == 255) {
      glds_sc1(vg, p.ws + WS_X + 256 * 32, wofs + L_COL2);
      glds_sc1(vt, p.ws + WS_WT + (size_t)256 * 8192, wofs + L_COL2 + 8192);
    } }
  if (has_m1) {
    const int fw = w & 1, sw = w >> 1, stile = (s0 >> 5) + sw;
    #pragma unroll
    for (int ft = 0; ft < 2; ++ft)
      #pragma unroll
      for (int u = 0; u < 2; ++u) {
        unsigned o[4];
        #pragma unroll
        for (int jj = 0; jj < 4; ++jj) {
          const unsigned hb = pk2(acc1[ft][8 * u + 2 * jj], acc1[ft][8 * u + 2 * jj + 1]);
          const f32x2 v = {bflo(hb), bfhi(hb)};
          const f32x2 t = v * -1.44269504088896340736f;
          const f32x2 e = (f32x2){__builtin_amdgcn_exp2f(t[0]), __builtin_amdgcn_exp2f(t[1])} + 1.0f;
          const f32x2 r = v * (f32x2){__builtin_amdgcn_rcpf(e[0]), __builtin_amdgcn_rcpf(e[1])};
          o[jj] = pk2(r[0], r[1]);
        }
        const int fstep = (f0 + 64 * fw + 32 * ft + 16 * u) >> 4;
        st16_wt((u32x4){o[0], o[1], o[2], o[3]}, wsr, WS_H + ((stile * 112 + fstep) * 64 + l) * 16);
      }
    STAMP(8);
    WAIT_VM(4);
  } else { STAMP(8); WAIT_VM(0); }
  BARE_BAR();
  STAMP(9);
  { const unsigned xc = ldsb + (w < 4 ? L_XCOL : L_COL2), wc = ldsb + (w < 4 ? L_WCOL : L_COL2 + 8192);
    const unsigned ub = ldsb + (w < 4 ? L_U : L_COL2 + 16384);
    bf16x8 Gc[2], Gs[2], Hc[2], Hs[2];
    if (p2wave) {
      const unsigned sgn = (c & 1) ? 0x80008000u : 0u;
      const u32x4 gc0 = {gcu[0], gcu[1], gcu[2], gcu[3]}, gs0 = {gsu[0], gsu[1], gsu[2], gsu[3]};
      const u32x4 hc0 = {hcu[0], hcu[1], hcu[2], hcu[3]}, hs0 = {hsu[0], hsu[1], hsu[2], hsu[3]};
      Gc[0] = as8(gc0); Gs[0] = as8(gs0); Gc[1] = as8(xor4(gc0, sgn)); Gs[1] = as8(xor4(gs0, sgn));
      Hc[0] = as8(hc0); Hs[0] = as8(hs0); Hc[1] = as8(xor4(hc0, 0x80000000u)); Hs[1] = as8(xor4(hs0, 0x80000000u));
      bf16x8 Zr[2][2], Zi[2][2]; unsigned Wv[8];
      #pragma unroll
      for (int u = 0; u < 2; ++u)
        #pragma unroll
        for (int mt = 0; mt < 2; ++mt) { const unsigned ad = xc + ((4 * u + q) * 32 + 16 * mt + c) * 16; Zr[mt][u] = lds_read16(ad); Zi[mt][u] = lds_read16(ad + 4096); }
      #pragma unroll
      for (int i = 0; i < 8; ++i) Wv[i] = lds_read4(wc + (16 * pw + c + 64 * (16 * (i >> 2) + 4 * q + (i & 3))) * 4);
      f32x4 Dr[2] = {}, Di[2] = {};
      #pragma unroll
      for (int u = 0; u < 2; ++u)
        #pragma unroll
        for (int mt = 0; mt < 2; ++mt) {
          Dr[mt] = mfma16(Zr[mt][u], Gc[u], Dr[mt]); Di[mt] = mfma16(Zi[mt][u], Gc[u], Di[mt]);
          Dr[mt] = mfma16(Zi[mt][u], Gs[u], Dr[mt]); Di[mt] = mfma16(neg8(Zr[mt][u]), Gs[u], Di[mt]);
        }
      unsigned tr_[4], ti_[4];
      { float re[8], im[8];
        #pragma unroll
        for (int i = 0; i < 8; ++i) { const float x = Dr[i >> 2][i & 3], y = Di[i >> 2][i & 3]; re[i] = x * twc[i] + y * tws[i]; im[i] = y * twc[i] - x * tws[i]; }
        #pragma unroll
        for (int jj = 0; jj < 4; ++jj) { tr_[jj] = pk2(re[2 * jj], re[2 * jj + 1]); ti_[jj] = pk2(im[2 * jj], im[2 * jj + 1]); } }
      bf16x8 Tr = mk8(tr_[0], tr_[1], tr_[2], tr_[3]), Ti = mk8(ti_[0], ti_[1], ti_[2], ti_[3]);
      f32x4 Vr[2] = {}, Vi[2] = {};
      { const bf16x8 nTr = neg8(Tr);
        #pragma unroll
        for (int t = 0; t < 2; ++t) { Vr[t] = mfma16(Hc[t], Tr, Vr[t]); Vi[t] = mfma16(Hc[t], Ti, Vi[t]); }
        #pragma unroll
        for (int t = 0; t < 2; ++t) { Vr[t] = mfma16(Hs[t], Ti, Vr[t]); Vi[t] = mfma16(Hs[t], nTr, Vi[t]); } }
      { float re[8], im[8];
        #pragma unroll
        for (int i = 0; i < 8; ++i) { const float x = Vr[i >> 2][i & 3], y = Vi[i >> 2][i & 3], wr = bflo(Wv[i]), wi = bfhi(Wv[i]);
          re[i] = x * wr - y * wi; im[i] = x * wi + y * wr; }
        #pragma unroll
        for (int jj = 0; jj < 4; ++jj) { tr_[jj] = pk2(re[2 * jj], re[2 * jj + 1]); ti_[jj] = pk2(im[2 * jj], im[2 * jj + 1]); } }
      Tr = mk8(tr_[0], tr_[1], tr_[2], tr_[3]); Ti = mk8(ti_[0], ti_[1], ti_[2], ti_[3]);
      f32x4 Ur[2] = {}, Ui[2] = {};
      { const bf16x8 nTi = neg8(Ti);
        #pragma unroll
        for (int t = 0; t < 2; ++t) { Ur[t] = mfma16(Hc[t], Tr, Ur[t]); Ui[t] = mfma16(Hc[t], Ti, Ui[t]); }
        #pragma unroll
        for (int t = 0; t < 2; ++t) { Ur[t] = mfma16(Hs[t], nTi, Ur[t]); Ui[t] = mfma16(Hs[t], Tr, Ui[t]); } }
      #pragma unroll
      for (int t = 0; t < 2; ++t) { float re[4], im[4];
        #pragma unroll
        for (int r = 0; r < 4; ++r) { const float x = Ur[t][r], y = Ui[t][r]; re[r] = x * twc[4 * t + r] - y * tws[4 * t + r]; im[r] = y * twc[4 * t + r] + x * tws[4 * t + r]; }
        char LAS* dst = (char LAS*)(size_t)ub + (16 * pw + c) * 72 + (16 * t + 4 * q) * 2;
        *(LAS u32x2*)dst = (u32x2){pk2(re[0], re[1]), pk2(re[2], re[3])};
        *(LAS u32x2*)(dst + 64 * 72) = (u32x2){pk2(im[0], im[1]), pk2(im[2], im[3])}; }
    }
    WAIT_VM(0);
    BAR();
    STAMP(10);
    if (has_m1 && tid == 0) flag_store(p.ws, FL_F2 + (rt * 16 + ct1) * FS, epoch);
    if (p2wave) {
      f32x4 yr[2] = {}, yi[2] = {};
      const int a = (l & 15) >> 2, pp = l & 3;
      #pragma unroll
      for (int u = 0; u < 2; ++u)
        #pragma unroll
        for (int t = 0; t < 2; ++t) {
          const unsigned ad = ub + (32 * u + 8 * q + a) * 72 + (8 * pp + 4 * t) * 2;
          const bf16x8 Ar = cat4(tr_read(ad), tr_read(ad + 4 * 72));
          const bf16x8 Ai = cat4(tr_read(ad + 64 * 72), tr_read(ad + 64 * 72 + 4 * 72));
          yr[t] = mfma16(Ar, Gc[u], yr[t]); yi[t] = mfma16(Ai, Gc[u], yi[t]);
          yr[t] = mfma16(neg8(Ai), Gs[u], yr[t]); yi[t] = mfma16(Ar, Gs[u], yi[t]);
        }
      const float ysc = (kcol == 0 || kcol == 256) ? 0.5f : 1.0f;
      const u32x4 yre = {pk2(yr[0][0] * ysc, yr[0][1] * ysc), pk2(yr[0][2] * ysc, yr[0][3] * ysc), pk2(yr[1][0] * ysc, yr[1][1] * ysc), pk2(yr[1][2] * ysc, yr[1][3] * ysc)};
      const u32x4 yim = {pk2(yi[0][0], yi[0][1]), pk2(yi[0][2], yi[0][3]), pk2(yi[1][0], yi[1][1]), pk2(yi[1][2], yi[1][3])};
      const unsigned so = (32 * (16 * pw + c) + 8 * q) * 2;
      st16_wt(yre, wsr, WS_Y + kcol * 4096 + so);
      st16_wt(yim, wsr, (kcol >= 1 && kcol <= 255) ? WS_Y + (256 + kcol) * 4096 + so : OOB);
    }
    STAMP(11);
    if (w == 4) spin4(p.ws, FL_F2 + ((pr >> 1) * 16 + (l < 14 ? l : 0)) * FS, epoch, l);
  }
  BAR();
  STAMP(12);
  unsigned gv; gload4_sc1(gv, (64 * pr + 32 * (w & 1) + (l & 31)) * 4, p.ws + WS_G);
  if (w >= 4) {
    #pragma unroll
    for (int u = 0; u < NHS; ++u) { M2_ISSUE(u, 0); M2_ISSUE(u, 1); M2_ISSUE(u, 2); M2_ISSUE(u, 3); } }
  f32x16 accb = {}, acca = {};
  {
    const int kh = w >> 2, nh = (w >> 1) & 1, sh = w & 1;
    const int h = l >> 5, blk = (l >> 4) & 1, a = (l & 15) >> 2, pp = l & 3;
    unsigned aoff[2];
    #pragma unroll
    for (int hh = 0; hh < 2; ++hh) { aoff[hh] = ldsb + hh * 65536 + kh * 8192 + (4 * h + a) * 128 + 16 * ((4 * nh + 2 * (pp & 1) + blk) ^ (4 * (a >> 1))) + 8 * (pp >> 1); PIN(aoff[hh]); }
    unsigned boffH[2], boffY[2];
    #pragma unroll
    for (int hh = 0; hh < 2; ++hh) { boffH[hh] = ldsb + L_HRING + hh * 49152 + kh * 8192 + sh * 4096 + l * 16; PIN(boffH[hh]);
      boffY[hh] = ldsb + L_HRING + hh * 49152 + kh * 8192 + (4 * h + a) * 128 + 16 * ((4 * sh + 2 * blk + (pp >> 1)) ^ (4 * (a >> 1))) + 8 * (pp & 1); PIN(boffY[hh]); }
    const int i31 = l & 31, dn = 64 * ct + 32 * nh + 16 * ((i31 >> 2) & 1) + 4 * (i31 >> 3) + (i31 & 3);
    const float dsc = (kh ? -1.0f : 1.0f) * (float)dn * (1.0f / 512.0f);
    const float cbase = (float)(4 * h) * dsc + (kh ? 0.0f : 0.25f);
    u32x4 Cf[16];
    auto cgen = [&](auto KS) { constexpr int ks = decltype(KS)::v; unsigned o[4];
      #pragma unroll
      for (int jj = 0; jj < 4; ++jj) { float v[2];
        #pragma unroll
        for (int e = 0; e < 2; ++e) { const int j = 2 * jj + e; v[e] = sin_rev(__builtin_fmaf((float)(16 * ks + 8 * (j >> 2) + (j & 3)), dsc, cbase)); }
        if (ks == 0 && jj == 0) { if (kh && h == 0) v[0] = (dn & 1) ? -1.0f : 1.0f; }
        o[jj] = pk2(v[0], v[1]); }
      Cf[ks] = (u32x4){o[0], o[1], o[2], o[3]}; PIN(Cf[ks]); };
    struct F2 { bf16x8 A[4], B[4]; } fa, fb;
    auto rdA = [&](auto ST, auto KS, F2& f) { constexpr int st = decltype(ST)::v, ks = decltype(KS)::v;
      if constexpr (st < 14) { constexpr unsigned o = (st % NWS) * 16384 + ks * 2048; f.A[ks] = cat4(tr_read(aoff[o >> 16] + (o & 65535)), tr_read(aoff[o >> 16] + (o & 65535) + 1024)); } };
    auto rdB = [&](auto ST, auto KS, F2& f) { constexpr int st = decltype(ST)::v, ks = decltype(KS)::v; constexpr int sl = st % NHS, hi = sl >= 3; constexpr unsigned o = (sl - 3 * hi) * 16384;
      if constexpr (st < 14) f.B[ks] = lds_read16(boffH[hi] + o + ks * 1024);
      else f.B[ks] = cat4(tr_read(boffY[hi] + o + ks * 2048), tr_read(boffY[hi] + o + ks * 2048 + 1024)); };
    if (w >= 4) { WAIT_VM(16); } else { WAIT_VM(0); }
    PIN(gv);
    BAR();
    if (tid == 0) flag_store(p.ws, FL_F3 + b * FS, epoch);
    STAMP(13);
    rdA(IC<0>{}, IC<0>{}, fa); rdB(IC<0>{}, IC<0>{}, fa); rdA(IC<0>{}, IC<1>{}, fa); rdB(IC<0>{}, IC<1>{}, fa);
    rdA(IC<0>{}, IC<2>{}, fa); rdB(IC<0>{}, IC<2>{}, fa); rdA(IC<0>{}, IC<3>{}, fa); rdB(IC<0>{}, IC<3>{}, fa);
    Poll f3v = {0, 0, 0, 0};
    auto step = [&](auto ST, F2& cur, F2& nxt) {
      constexpr int st = decltype(ST)::v;
      if constexpr (st <= 8) { WAIT_VM(12); }
      else if constexpr (st <= 12) {
        if (w < 4) {
          if constexpr (st <= 10) { WAIT_VM(8); } else if constexpr (st == 11) { WAIT_VM(4); } else { WAIT_VM(0); }
          if constexpr (st == 9) { if (w == 0) { poll_pin(f3v); spin_poll(f3v, p.ws + FL_F3, p.ws, epoch, l); } }
        } else { WAIT_VM(12); }
      }
      else if constexpr (st == 13) { WAIT_VM(12); } else if constexpr (st == 14) { WAIT_VM(8); } else if constexpr (st == 15) { WAIT_VM(4); } else if constexpr (st == 16) { WAIT_VM(0); }
      BAR();
      STAMP(30 + st);
      if constexpr (st == 7) { if (w == 0) poll_issue(f3v, l * FS, p.ws + FL_F3); }
      auto sub = [&](auto KS) { constexpr int ks = decltype(KS)::v;
        if constexpr (st < 14) accb = mfma32(cur.A[ks], cur.B[ks], accb); else acca = mfma32(as8(Cf[4 * (st - 14) + ks]), cur.B[ks], acca);
        SB();
        if constexpr (st < 17) { rdA(IC<st + 1>{}, KS, nxt); rdB(IC<st + 1>{}, KS, nxt); }
        if constexpr (st + 5 < 14) { M2_ISSUE(st + 5, ks); }
        else if constexpr (st + 5 < 18) { if (w >= 4) M2_ISSUE_Y(st + 5, ks); }
        if constexpr (ks == 1 && st < 16) cgen(IC<(st < 16 ? st : 0)>{});
        SB(); };
      sub(IC<0>{}); sub(IC<1>{}); sub(IC<2>{}); sub(IC<3>{});
    };
    step(IC<0>{}, fa, fb); step(IC<1>{}, fb, fa); step(IC<2>{}, fa, fb); step(IC<3>{}, fb, fa); step(IC<4>{}, fa, fb); step(IC<5>{}, fb, fa);
    step(IC<6>{}, fa, fb); step(IC<7>{}, fb, fa); step(IC<8>{}, fa, fb); step(IC<9>{}, fb, fa); step(IC<10>{}, fa, fb); step(IC<11>{}, fb, fa);
    step(IC<12>{}, fa, fb); step(IC<13>{}, fb, fa); step(IC<14>{}, fa, fb); step(IC<15>{}, fb, fa); step(IC<16>{}, fa, fb); step(IC<17>{}, fb, fa);
    STAMP(15);
    if (b == 0 && tid == 0) flag_store(p.ws, FL_E, epoch);
    BAR();
    LAS f32x4* ex = (LAS f32x4*)lds;
    ex[(w * 4 + 0) * 64 + l] = (f32x4){kh ? acca[0] : acca[8], kh ? acca[1] : acca[9], kh ? acca[2] : acca[10], kh ? acca[3] : acca[11]};
    ex[(w * 4 + 1) * 64 + l] = (f32x4){kh ? acca[4] : acca[12], kh ? acca[5] : acca[13], kh ? acca[6] : acca[14], kh ? acca[7] : acca[15]};
    ex[(w * 4 + 2) * 64 + l] = (f32x4){kh ? accb[0] : accb[8], kh ? accb[1] : accb[9], kh ? accb[2] : accb[10], kh ? accb[3] : accb[11]};
    ex[(w * 4 + 3) * 64 + l] = (f32x4){kh ? accb[4] : accb[12], kh ? accb[5] : accb[13], kh ? accb[6] : accb[14], kh ? accb[7] : accb[15]};
    BAR();
    const int pwv = w ^ 4;
    const f32x4 a0 = ex[(pwv * 4 + 0) * 64 + l], a1 = ex[(pwv * 4 + 1) * 64 + l], b0 = ex[(pwv * 4 + 2) * 64 + l], b1 = ex[(pwv * 4 + 3) * 64 + l];
    float av[8], bv[8];
    #pragma unroll
    for (int i = 0; i < 4; ++i) {
      av[i] = (kh ? acca[8 + i] : acca[i]) + a0[i]; av[4 + i] = (kh ? acca[12 + i] : acca[4 + i]) + a1[i];
      bv[i] = (kh ? accb[8 + i] : accb[i]) + b0[i]; bv[4 + i] = (kh ? accb[12 + i] : accb[4 + i]) + b1[i]; }
    const int s = 64 * pr + 32 * sh + (l & 31), n = 64 * ct + 32 * nh + 16 * h + 8 * kh;
    const float g = __uint_as_float(gv), omg = rbf(1.0f - g);
    constexpr float ASC = 2.0f / 1048576.0f;
    unsigned o4[4];
    #pragma unroll
    for (int i = 0; i < 4; ++i) {
      const float v0 = rbf(g * rbf(av[2 * i] * ASC)) + rbf(omg * rbf(bv[2 * i])), v1 = rbf(g * rbf(av[2 * i + 1] * ASC)) + rbf(omg * rbf(bv[2 * i + 1]));
      o4[i] = pk2(v0, v1); }
    *(GAS u32x4*)((char*)p.out + (size_t)s * 1024 + n * 2) = (u32x4){o4[0], o4[1], o4[2], o4[3]};
  }
  STAMP(16);
  STAMP_FLUSH();
}

__global__ void k_pre(char* ws) { if (threadIdx.x == 0) __hip_atomic_store((GAS unsigned long long*)(ws + (128u << 20)), (unsigned long long)__builtin_amdgcn_s_memrealtime(), __ATOMIC_RELAXED, __HIP_MEMORY_SCOPE_AGENT); }
__global__ void k_post(char* ws, int spin) { const unsigned long long t0 = __builtin_amdgcn_s_memrealtime();
  unsigned long long v = ((GAS unsigned long long*)(ws + (128u << 20)))[16 + threadIdx.x], m = v;
  for (int o = 32; o >= 1; o >>= 1) { unsigned long long x = __shfl_xor(m, o); m = x > m ? x : m; }
  __shared__ unsigned long long sm[4]; if ((threadIdx.x & 63) == 0) sm[threadIdx.x >> 6] = m; __syncthreads();
  m = sm[0]; for (int i = 1; i < 4; ++i) m = sm[i] > m ? sm[i] : m;
  if (spin) { const unsigned long long e = __builtin_amdgcn_s_memrealtime(), d = 4 * (t0 - m); while (__builtin_amdgcn_s_memrealtime() - e < d) __builtin_amdgcn_s_sleep(1); } }
extern "C" void kernel_launch(void* const* d_in, const int* in_sizes, int n_in, void* d_out, int out_size, void* d_ws, size_t ws_size, hipStream_t stream) {
  Params p;
  p.ax = (const uint16_t*)d_in[0]; p.wr = (const uint16_t*)d_in[1]; p.wi = (const uint16_t*)d_in[2];
  p.w1 = (const uint16_t*)d_in[4]; p.w2 = (const uint16_t*)d_in[5]; p.gw = (const uint16_t*)d_in[6];
  p.out = (uint16_t*)d_out; p.ws = (char*)d_ws;
  (void)hipFuncSetAttribute((const void*)fused_kernel, hipFuncAttributeMaxDynamicSharedMemorySize, 163840);
  fused_kernel<<<256, 512, 163840, stream>>>(p);
  k_post<<<1, 256, 0, stream>>>(p.ws, 0);
}
```

```cpp
#include <hip/hip_runtime.h>
#include <hip/hip_bf16.h>
#include <stdint.h>

typedef short    bf16x8 __attribute__((ext_vector_type(8)));
typedef short    s16x4  __attribute__((ext_vector_type(4)));
typedef float    f32x2  __attribute__((ext_vector_type(2)));
typedef float    f32x4  __attribute__((ext_vector_type(4)));
typedef float    f32x16 __attribute__((ext_vector_type(16)));
typedef unsigned u32x2  __attribute__((ext_vector_type(2)));
typedef unsigned u32x4  __attribute__((ext_vector_type(4)));
typedef __bf16   bf16x2_t __attribute__((ext_vector_type(2)));
#define LAS __attribute__((address_space(3)))
#define GAS __attribute__((address_space(1)))
#define DI __device__ __forceinline__

constexpr int F_ = 1792, DF = 257;

constexpr unsigned XJ_STRIDE = 260 * 32;
constexpr unsigned FS = 128;
constexpr unsigned FL_F1 = 0, FL_F3 = 256 * FS, FL_F2 = 512 * FS, FL_E = 768 * FS, FL_ERR = 769 * FS;
constexpr unsigned WS_X  = 772 * FS;
constexpr unsigned WS_WT = WS_X + 256 * XJ_STRIDE;
constexpr unsigned YROW = 4096 + 128;
constexpr unsigned WS_Y  = WS_WT + 257 * 8192;
constexpr unsigned WS_G  = WS_Y + 512 * YROW;
constexpr unsigned WS_H  = WS_G + 8192;
constexpr unsigned WS_END = WS_H + 64 * 112 * 1024;

struct Params {
  const uint16_t *ax, *wr, *wi, *w1, *w2, *gw;
  uint16_t* out;
  char* ws;
};

DI unsigned pk2(float lo, float hi) { return __builtin_bit_cast(unsigned, __builtin_convertvector((f32x2){lo, hi}, bf16x2_t)); }
DI float bflo(unsigned u) { return __uint_as_float(u << 16); }
DI float bfhi(unsigned u) { return __uint_as_float(u & 0xffff0000u); }
DI float rbf(float x) { return bflo(pk2(x, x)); }
DI float sin_rev(float x) { return __builtin_amdgcn_sinf(x); }
DI float cos_rev(float x) { return __builtin_amdgcn_cosf(x); }
DI f32x4 mfma16(bf16x8 a, bf16x8 b, f32x4 c) { return __builtin_amdgcn_mfma_f32_16x16x32_bf16(a, b, c, 0, 0, 0); }
DI f32x16 mfma32(bf16x8 a, bf16x8 b, f32x16 c) { return __builtin_amdgcn_mfma_f32_32x32x16_bf16(a, b, c, 0, 0, 0); }
DI bf16x8 mk8(unsigned a, unsigned b, unsigned c, unsigned d) { return __builtin_bit_cast(bf16x8, (u32x4){a, b, c, d}); }
DI bf16x8 as8(u32x4 v) { return __builtin_bit_cast(bf16x8, v); }
DI u32x4 xor4(u32x4 v, unsigned m) { v ^= m; return v; }
DI bf16x8 neg8(bf16x8 v) { return as8(xor4(__builtin_bit_cast(u32x4, v), 0x80008000u)); }
DI s16x4 tr_read(unsigned addr) { return __builtin_amdgcn_ds_read_tr16_b64_v4i16((LAS s16x4*)addr); }
DI bf16x8 lds_read16(unsigned addr) { return *(const LAS bf16x8*)addr; }
DI unsigned lds_read4(unsigned addr) { return *(const LAS unsigned*)addr; }
DI unsigned lds_read2(unsigned addr) { return *(const LAS unsigned short*)addr; }
DI bf16x8 cat4(s16x4 lo, s16x4 hi) { return __builtin_shufflevector(lo, hi, 0, 1, 2, 3, 4, 5, 6, 7); }
#define WAIT_VM(n) asm volatile("s_waitcnt vmcnt(%0)" :: "n"(n) : "memory")
#define WAIT_LGKM0() asm volatile("s_waitcnt lgkmcnt(0)" ::: "memory")
#define SB() __builtin_amdgcn_sched_barrier(0)
#define BARE_BAR() do { SB(); asm volatile("" ::: "memory"); __builtin_amdgcn_s_barrier(); asm volatile("" ::: "memory"); SB(); } while (0)
#define BAR() do { SB(); WAIT_LGKM0(); __builtin_amdgcn_s_barrier(); asm volatile("" ::: "memory"); SB(); } while (0)
#define PIN(x) asm volatile("" : "+v"(x))

DI void glds(unsigned voff, const void* sbase, unsigned dst) {
  asm volatile("s_mov_b32 m0, %2\n\ts_nop 0\n\tglobal_load_lds_dwordx4 %0, %1" :: "v"(voff), "s"(sbase), "s"(dst) : "memory");
}
DI void glds_sc1(unsigned voff, const void* sbase, unsigned dst) {
  asm volatile("s_mov_b32 m0, %2\n\ts_nop 0\n\tglobal_load_lds_dwordx4 %0, %1 sc1" :: "v"(voff), "s"(sbase), "s"(dst) : "memory");
}
DI void gload16(u32x4& d, unsigned voff, const void* sbase) { asm volatile("global_load_dwordx4 %0, %1, %2" : "=v"(d) : "v"(voff), "s"(sbase) : "memory"); }
DI void gload16_sc1(u32x4& d, unsigned voff, const void* sbase) { asm volatile("global_load_dwordx4 %0, %1, %2 sc1" : "=v"(d) : "v"(voff), "s"(sbase) : "memory"); }
DI void gload4_sc1(unsigned& d, unsigned voff, const void* sbase) { asm volatile("global_load_dword %0, %1, %2 sc1" : "=v"(d) : "v"(voff), "s"(sbase) : "memory"); }
typedef __amdgpu_buffer_rsrc_t rsrc_t;
DI void st16_wt(u32x4 v, rsrc_t r, unsigned off) { __builtin_amdgcn_raw_buffer_store_b128(v, r, off, 0, 16); }
DI void st4_wt(unsigned v, rsrc_t r, unsigned off) { __builtin_amdgcn_raw_buffer_store_b32(v, r, off, 0, 16); }
constexpr unsigned OOB = 0x80000000u;
DI void flag_store(char* ws, unsigned off, unsigned epoch) { __hip_atomic_store((GAS unsigned*)(ws + off), epoch, __ATOMIC_RELAXED, __HIP_MEMORY_SCOPE_AGENT); }
DI void flag_line(char* ws, unsigned off, unsigned epoch, int l) { if (l < 32) __hip_atomic_store((GAS unsigned*)(ws + off + l * 4), epoch, __ATOMIC_RELAXED, __HIP_MEMORY_SCOPE_AGENT); }

#define STAMP(i) do {} while (0)
#define STAMP_DECL
#define STAMP_FLUSH() do {} while (0)

struct Poll { unsigned a, b, c, d; };
DI void poll_issue(Poll& v, unsigned voff, const char* ws) { gload4_sc1(v.a, voff, ws); gload4_sc1(v.b, voff, ws + 64 * FS); gload4_sc1(v.c, voff, ws + 128 * FS); gload4_sc1(v.d, voff, ws + 192 * FS); }
DI void poll_pin(Poll& v) { asm volatile("" : "+v"(v.a), "+v"(v.b), "+v"(v.c), "+v"(v.d)); }
DI void spin_poll(Poll& v, const char* base, char* ws, unsigned epoch, int l) {
  if (__all(v.a == epoch && v.b == epoch && v.c == epoch && v.d == epoch)) return;
  const unsigned long long t0 = __builtin_amdgcn_s_memrealtime();
  for (;;) {
    __builtin_amdgcn_s_sleep(1);
    poll_issue(v, l * FS, base); WAIT_VM(0); poll_pin(v);
    if (__all(v.a == epoch && v.b == epoch && v.c == epoch && v.d == epoch)) break;
    if (__builtin_amdgcn_s_memrealtime() - t0 > 2000000ull) { if (l == 0) flag_store(ws, FL_ERR, 1u); break; }
  }
}
DI void spin4(char* ws, unsigned voff, unsigned epoch, int l) {
  const unsigned long long t0 = __builtin_amdgcn_s_memrealtime();
  for (;;) {
    unsigned v; gload4_sc1(v, voff, ws); WAIT_VM(0); PIN(v);
    if (__all(v == epoch)) break;
    __builtin_amdgcn_s_sleep(1);
    if (__builtin_amdgcn_s_memrealtime() - t0 > 2000000ull) { if (l == 0) flag_store(ws, FL_ERR, 1u); break; }
  }
}

constexpr unsigned L_AUX   = 131072;
constexpr unsigned L_XROWS = L_AUX;
constexpr unsigned L_WSCR  = 32768;
constexpr unsigned L_XCOL  = L_AUX;
constexpr unsigned L_WCOL  = L_AUX + 8192;
constexpr unsigned L_U     = L_AUX + 16384;
constexpr unsigned L_GW    = L_AUX + 25600;
constexpr unsigned L_CNT   = 163840 - 32;
constexpr unsigned L_COL2  = 81920;
constexpr unsigned L_HRING = 81920;
constexpr int NWS = 5, NHS = 5;

template <int N> struct IC { static constexpr int v = N; };
DI unsigned lds_inc(unsigned addr, int l) {
  unsigned old = 0;
  if (l == 0) asm volatile("ds_add_rtn_u32 %0, %1, %2\n\ts_waitcnt lgkmcnt(0)" : "=&v"(old) : "v"(addr), "v"(1u) : "memory");
  return __builtin_amdgcn_readfirstlane(old);
}
DI void lds_spin(unsigned addr, unsigned n) { while (__builtin_amdgcn_readfirstlane(__hip_atomic_load((LAS unsigned*)addr, __ATOMIC_RELAXED, __HIP_MEMORY_SCOPE_WORKGROUP)) < n) __builtin_amdgcn_s_sleep(1); asm volatile("" ::: "memory"); }

__global__ __launch_bounds__(512, 2) void fused_kernel(Params p) {
  extern __shared__ __attribute__((aligned(16))) char smem[];
  const unsigned ldsb = (unsigned)(size_t)(char LAS*)smem;
  char LAS* lds = (char LAS*)smem;
  const int b = blockIdx.x, tid = threadIdx.x;
  const int w = __builtin_amdgcn_readfirstlane(tid >> 6), l = tid & 63;
  const int xcd = b & 7, jx = b >> 3;
  const bool has_m1 = jx < 28;
  const int rt = 4 * (xcd >> 1) + jx / 7, ct1 = 7 * (xcd & 1) + jx % 7;
  const int s0 = 128 * rt, f0 = 128 * ct1;
  const int c = l & 15, q = l >> 4;
  STAMP_DECL
  STAMP(0);

  const int n2 = jx, gq = xcd, pj = xcd * 32 + jx, pr4 = w & 3, t1 = w >> 2;
  const unsigned xrw = __builtin_amdgcn_readfirstlane(ldsb + L_XROWS + w * 2048);
  { const char* xr = (const char*)p.ax + (size_t)(n2 + 256 * gq + 64 * pr4) * 1024;
    glds(l * 16, xr, xrw); glds(l * 16, xr + 32 * 1024, xrw + 1024); }
  if (w == 7) glds(l * 16, p.gw, ldsb + L_GW);
  if (w == 6) asm volatile("s_mov_b32 m0, %1\n\ts_mov_b64 exec, 1\n\tglobal_load_lds_dword %0, %2 sc1\n\ts_mov_b64 exec, -1" :: "v"(FL_E), "s"(ldsb + L_CNT + 12), "s"(p.ws) : "memory");
  unsigned vx[2], vw[2];
  #pragma unroll
  for (int i = 0; i < 2; ++i) { const int row = 8 * (2 * w + i) + (l >> 3); vx[i] = row * 1024 + ((l & 7) ^ ((row >> 1) & 7)) * 16;
    const int r2 = 4 * (2 * w + i) + (l >> 4); vw[i] = r2 * (F_ * 2) + ((l & 15) ^ ((r2 & 3) << 2)) * 16; }
  const char* xbase = (const char*)p.ax + (size_t)s0 * 1024;
  const char* w1base = (const char*)p.w1 + f0 * 2;
  const unsigned rot1 = (jx % 7 + 2 * (jx / 7)) & 7;
  const unsigned m1dst = __builtin_amdgcn_readfirstlane(ldsb + w * 2048);
  #define M1_ISSUE(s, i) do { const unsigned ks_ = ((s) + rot1) & 7; \
      if ((i) < 2) glds(vx[(i) & 1] + ks_ * 128, xbase, m1dst + ((s) & 3) * 32768 + ((i) & 1) * 1024); \
      else glds(vw[(i) & 1] + ks_ * (64 * F_ * 2), w1base, m1dst + ((s) & 3) * 32768 + 16384 + ((i) & 1) * 1024); } while (0)
  #define M1_STAGE(s) do { M1_ISSUE(s, 0); M1_ISSUE(s, 2); M1_ISSUE(s, 1); M1_ISSUE(s, 3); } while (0)
  const int wsl = ((xcd + 8 * (jx - 28)) * 8 + w) & 255, m0 = 32 * (wsl >> 2), kq = wsl & 3;
  const unsigned wscr = __builtin_amdgcn_readfirstlane(ldsb + L_WSCR + w * 8192);
  unsigned er = 0, ei = 0;
  if (!has_m1 && kq == 3) { const unsigned vo = (m0 + (l & 31)) * (DF * 2) + 512;
    asm volatile("global_load_ushort %0, %2, %3\n\tglobal_load_ushort %1, %2, %4" : "=&v"(er), "=&v"(ei) : "v"(vo), "s"(p.wr), "s"(p.wi) : "memory"); }
  const rsrc_t wsr = __builtin_amdgcn_make_buffer_rsrc(p.ws, 0, WS_END, 0x00020000);
  if (tid < 5 && tid != 3) { *(LAS unsigned*)(lds + L_CNT + tid * 4) = 0u; }
  bf16x8 Fc, Fs, Fns, Are, Aim; float p1c[4], p1s[4];
  { const float kf = (float)(16 * t1 + c), k32 = kf * (1.0f / 32.0f), q4 = (float)(4 * q);
    u32x4 uc, us, ur, ui;
    auto wl = [&](int i) { const int row = 8 * (i & 3) + (l >> 3), ch = (l & 7) ^ (((row >> 1) & 3) << 1);
      glds((m0 + row) * (DF * 2) + 128 * kq + ch * 16, i < 4 ? (const char*)p.wr : (const char*)p.wi, wscr + i * 1024); };
    #pragma unroll
    for (int jj = 0; jj < 4; ++jj) {
      SB(); if (has_m1) M1_ISSUE(0, (jj == 1 ? 2 : jj == 2 ? 1 : jj)); else { wl(2 * jj); wl(2 * jj + 1); } SB();
      float cv[2], sv[2];
      #pragma unroll
      for (int e = 0; e < 2; ++e) { const int j = 2 * jj + e; const float ang = ((float)(j < 4 ? j : 12 + j) + q4) * k32; cv[e] = cos_rev(ang); sv[e] = sin_rev(ang); }
      uc[jj] = pk2(cv[0], cv[1]); us[jj] = pk2(sv[0], sv[1]); PIN(uc[jj]); PIN(us[jj]); }
    u32x4 un = xor4(us, 0x80008000u); PIN(un);
    Fc = as8(uc); Fs = as8(us); Fns = as8(un);
    #pragma unroll
    for (int r = 0; r < 4; ++r) {
      if (r < 2) { SB(); if (has_m1) M1_ISSUE(1, 2 + r); SB(); }
      const float nr = q4 + (float)r; const float ang = nr * ((float)c * (1.0f / 16.0f)); const float cv = cos_rev(ang), sv = sin_rev(ang);
      ur[r] = pk2(cv, sv); ui[r] = pk2(-sv, cv);
      const float a2 = nr * (kf * (1.0f / 512.0f)); p1c[r] = 0.5f * cos_rev(a2); p1s[r] = 0.5f * sin_rev(a2); PIN(ur[r]); PIN(ui[r]); PIN(p1c[r]); PIN(p1s[r]); }
    Are = as8(ur); Aim = as8(ui); }
  STAMP(1);
  if (has_m1) { WAIT_VM(6); } else { WAIT_VM(8); }
  STAMP(2);
  u32x4 xv;
  { const unsigned xa = xrw + l * 8, xb = xa + 1024;
    const bf16x8 A_a = cat4(tr_read(xa), tr_read(xa + 512)), A_b = cat4(tr_read(xb), tr_read(xb + 512));
    xv = __builtin_bit_cast(u32x4, lds_read16(xrw + t1 * 1024 + l * 16));
    f32x4 Tr = {0, 0, 0, 0}, Ti = {0, 0, 0, 0};
    Tr = mfma16(A_a, Fc, Tr); Ti = mfma16(A_a, Fns, Ti);
    Tr = mfma16(A_b, Fs, Tr); Ti = mfma16(A_b, Fc, Ti);
    PIN(xv); SB();
    unsigned tb[4];
    #pragma unroll
    for (int r = 0; r < 4; ++r) { const float re = Tr[r] * p1c[r] + Ti[r] * p1s[r], im = Ti[r] * p1c[r] - Tr[r] * p1s[r]; tb[r] = pk2(re, im); }
    const bf16x8 Bt = mk8(tb[0], tb[1], tb[2], tb[3]);
    f32x4 Zr = {0, 0, 0, 0}, Zi = {0, 0, 0, 0};
    Zr = mfma16(Are, Bt, Zr); Zi = mfma16(Aim, Bt, Zi);
    #pragma unroll
    for (int r = 0; r < 4; ++r) *(LAS f32x2*)(size_t)(xrw + ((4 * q + r) * 16 + c) * 8) = (f32x2){Zr[r], Zi[r]};
    BAR();
    STAMP(3);
    if (has_m1) { M1_ISSUE(1, 0); M1_ISSUE(1, 1); }
    { const int k = tid >> 1, c2 = tid & 1, km = (512 - k) & 511; f32x2 z1[4], z2[4];
      const unsigned a1 = ldsb + L_XROWS + ((k >> 4) & 1) * 8192 + ((k >> 5) * 16 + (k & 15)) * 8, a2 = ldsb + L_XROWS + ((km >> 4) & 1) * 8192 + ((km >> 5) * 16 + (km & 15)) * 8;
      #pragma unroll
      for (int pp = 0; pp < 4; ++pp) { z1[pp] = *(const LAS f32x2*)(size_t)(a1 + pp * 2048); z2[pp] = *(const LAS f32x2*)(size_t)(a2 + pp * 2048); }
      unsigned o[4];
      #pragma unroll
      for (int pp = 0; pp < 4; ++pp) o[pp] = c2 == 0 ? pk2(z1[pp][0] + z2[pp][0], z1[pp][1] + z2[pp][1]) : pk2(z1[pp][1] - z2[pp][1], z2[pp][0] - z1[pp][0]);
      st16_wt((u32x4){o[0], o[1], o[2], o[3]}, wsr, WS_X + pj * XJ_STRIDE + k * 32 + c2 * 16);
      if (w == 0) { unsigned o2[4];
        #pragma unroll
        for (int pp = 0; pp < 4; ++pp) { const f32x2 z = *(const LAS f32x2*)(size_t)(ldsb + L_XROWS + pp * 2048 + 8 * 16 * 8); o2[pp] = tid == 0 ? pk2(z[0] + z[0], z[1] + z[1]) : 0u; }
        st16_wt((u32x4){o2[0], o2[1], o2[2], o2[3]}, wsr, tid < 8 ? WS_X + pj * XJ_STRIDE + 256 * 32 + tid * 16 : OOB); }
    }
  }
  const unsigned epoch = __builtin_amdgcn_readfirstlane(lds_read4(ldsb + L_CNT + 12)) + 1u;
  STAMP(4);

  const bool p2wave = w < 4 || b == 255;
  const int pw = w & 3;
  const int kcol = w < 4 ? xcd * 32 + jx : 256;
  const int ct = jx & 7, pr = 4 * xcd + (jx >> 3);
  u32x4 gc0, gs0, hc0, hs0; float twc[8], tws[8];
  auto p2consts = [&]() {
    const float k1f = (float)(16 * pw + c), q4f = (float)(4 * q), k64 = k1f * (1.0f / 64.0f), c32 = (float)c * (1.0f / 32.0f), k2k = k1f * (1.0f / 2048.0f);
    #pragma unroll
    for (int i = 0; i < 4; ++i) { const float a0 = (2.0f * q4f + (float)(2 * i)) * k64, a1 = (2.0f * q4f + (float)(2 * i + 1)) * k64;
      gc0[i] = pk2(cos_rev(a0), cos_rev(a1)); gs0[i] = pk2(sin_rev(a0), sin_rev(a1));
      const float b0 = (q4f + (float)(2 * i < 4 ? 2 * i : 12 + 2 * i)) * c32, b1 = (q4f + (float)(2 * i + 1 < 4 ? 2 * i + 1 : 13 + 2 * i)) * c32;
      hc0[i] = pk2(cos_rev(b0), cos_rev(b1)); hs0[i] = pk2(sin_rev(b0), sin_rev(b1)); }
    #pragma unroll
    for (int i = 0; i < 8; ++i) { const float a = (q4f + (float)(16 * (i >> 2) + (i & 3))) * k2k; twc[i] = cos_rev(a); tws[i] = sin_rev(a); }
    PIN(gc0); PIN(gs0); PIN(hc0); PIN(hs0);
    #pragma unroll
    for (int i = 0; i < 8; ++i) { PIN(twc[i]); PIN(tws[i]); } };
  auto gather = [&]() {
    const unsigned wofs = __builtin_amdgcn_readfirstlane(ldsb + w * 1024);
    const unsigned vg = (64 * (w & 3) + l) * XJ_STRIDE + (w >> 2) * 16, vt = tid * 16, kc = xcd * 32 + jx;
    glds_sc1(vg, p.ws + WS_X + kc * 32, wofs + L_XCOL);
    { const int piece = 16 * w + (l >> 2);
      glds_sc1((piece >> 1) * 8192 + (piece & 1) * 4096 + (l & 3) * 16, p.ws + WS_WT + (size_t)(kc >> 6) * (64 * 8192) + (kc & 63) * 64, wofs + L_WCOL); }
    if (b == 255) {
      glds_sc1(vg, p.ws + WS_X + 256 * 32, wofs + L_COL2);
      glds_sc1(vt, p.ws + WS_WT + (size_t)256 * 8192, wofs + L_COL2 + 8192);
    } };
  auto gate = [&]() {
    const u32x4 gwv = __builtin_bit_cast(u32x4, lds_read16(ldsb + L_GW + l * 16));
    float sum = 0.f;
    #pragma unroll
    for (int i = 0; i < 4; ++i) { sum += rbf(bflo(xv[i]) * bflo(gwv[i])); sum += rbf(bfhi(xv[i]) * bfhi(gwv[i])); }
    sum += __builtin_bit_cast(float, __builtin_amdgcn_update_dpp(0, __builtin_bit_cast(int, sum), 0x111, 0xf, 0xf, true));
    sum += __builtin_bit_cast(float, __builtin_amdgcn_update_dpp(0, __builtin_bit_cast(int, sum), 0x112, 0xf, 0xf, true));
    sum += __builtin_bit_cast(float, __builtin_amdgcn_update_dpp(0, __builtin_bit_cast(int, sum), 0x114, 0xf, 0xf, true));
    sum += __builtin_bit_cast(float, __builtin_amdgcn_update_dpp(0, __builtin_bit_cast(int, sum), 0x118, 0xf, 0xf, true));
    sum += __builtin_bit_cast(float, __builtin_amdgcn_update_dpp(0, __builtin_bit_cast(int, sum), 0x142, 0xa, 0xf, true));
    sum += __builtin_bit_cast(float, __builtin_amdgcn_update_dpp(0, __builtin_bit_cast(int, sum), 0x143, 0xc, 0xf, true));
    const float gg = rbf(__builtin_amdgcn_rcpf(1.0f + __expf(-rbf(sum))));
    st4_wt(__float_as_uint(gg), wsr, l == 63 ? WS_G + (n2 + 256 * gq + 64 * pr4 + 32 * t1) * 4 : OOB); };

  const unsigned vrow = (l >> 3) * 1024 + ((l & 7) ^ (4 * ((l >> 4) & 1))) * 16;
  const unsigned vrowY = (l >> 3) * YROW + ((l & 7) ^ (4 * ((l >> 4) & 1))) * 16;
  const unsigned rot2 = ((7 * ct) / 4 + (7 * (jx >> 3)) / 2) % 14;
  #define M2_STAGE(u) (((u) + rot2) >= 14 ? ((u) + rot2 - 14) : ((u) + rot2))
  const unsigned w2dst = __builtin_amdgcn_readfirstlane(ldsb + (w & 1) * 8192), vw2 = vrow + (w & 1) * 896 * 1024 + ct * 128;
  #define M2_ISSUE_W2(u, j) glds(vw2 + (j) * 8192 + M2_STAGE(u) * 65536, p.w2, w2dst + ((u) % NWS) * 16384 + (j) * 1024)

  if (has_m1) {
    f32x16 acc1[2] = {};
    const int fw = w & 1, sw = w >> 1;
    unsigned xo[2][4], wo0[2], wo1[2];
    { const int h = l >> 5, blk = (l >> 4) & 1, a = (l & 15) >> 2, pp = l & 3, xrow = 32 * sw + (l & 31);
      #pragma unroll
      for (int hh = 0; hh < 2; ++hh) {
        #pragma unroll
        for (int ks = 0; ks < 4; ++ks) { xo[hh][ks] = ldsb + hh * 65536 + xrow * 128 + 16 * ((2 * ks + h) ^ ((xrow >> 1) & 7)); PIN(xo[hh][ks]); }
        wo0[hh] = ldsb + hh * 65536 + 16384 + (8 * h + a) * 256 + 16 * (((8 * fw + 0) ^ (a << 2)) + 2 * blk + (pp >> 1)) + 8 * (pp & 1); PIN(wo0[hh]);
        wo1[hh] = ldsb + hh * 65536 + 16384 + (8 * h + a) * 256 + 16 * (((8 * fw + 4) ^ (a << 2)) + 2 * blk + (pp >> 1)) + 8 * (pp & 1); PIN(wo1[hh]); } }
    struct Fr { bf16x8 B, A0, A1; } fa, fb;
    #define RB(f, g)  f.B  = lds_read16(xo[(((g) >> 2) & 3) >> 1][(g) & 3] + (((g) >> 2) & 1) * 32768)
    #define RA(base_, g) cat4(tr_read(base_[(((g) >> 2) & 3) >> 1] + (((g) >> 2) & 1) * 32768 + ((g) & 3) * 4096), tr_read(base_[(((g) >> 2) & 3) >> 1] + (((g) >> 2) & 1) * 32768 + ((g) & 3) * 4096 + 1024))
    SB();
    M1_STAGE(2);
    if (p2wave) p2consts();
    if (w == 0) { WAIT_VM(6); } else { WAIT_VM(5); }
    BARE_BAR();
    STAMP(5);
    RB(fa, 0); fa.A0 = RA(wo0, 0); fa.A1 = RA(wo1, 0);
    unsigned f1v = 0;
    auto kstep = [&](auto G, Fr& cur, Fr& nxt) {
      constexpr int g = decltype(G)::v, st = g >> 2, ks = g & 3;
      if constexpr (ks == 0 && st > 0) {
        if constexpr (st == 1) { WAIT_VM(4); }
        else if constexpr (st <= 3) { if (w == 0) { WAIT_VM(5); } else { WAIT_VM(4); } }
        else if constexpr (st <= 5) { WAIT_VM(4); }
        else if constexpr (st == 6) { if (w < 4) { WAIT_VM(0); } else { WAIT_VM(2); } }
        BARE_BAR();
        STAMP(20 + st);
        if constexpr (st == 6) { if (w < 4) gload4_sc1(f1v, (64 * w + l) * FS, p.ws + FL_F1); }
      }
      acc1[0] = mfma32(cur.A0, cur.B, acc1[0]); SB();
      if constexpr (g + 1 < 32) { RB(nxt, g + 1); nxt.A0 = RA(wo0, g + 1); }
      SB();
      acc1[1] = mfma32(cur.A1, cur.B, acc1[1]); SB();
      if constexpr (g + 1 < 32) { nxt.A1 = RA(wo1, g + 1); }
      if constexpr (st <= 4) { M1_ISSUE(st + 3, ks); }
      if constexpr (g == 7) { if (w == 0) flag_line(p.ws, FL_F1 + b * FS, epoch, l); }
      if constexpr (g == 20 || g == 21) { if (w >= 4) glds(l * 16, (const char*)p.w2 + (size_t)((xcd * 28 + jx) * 8 + (w & 3) * 2 + (g & 1)) * 1024, m1dst + (g & 1) * 1024); }
      SB();
    };
    #define K2(g) kstep(IC<g>{}, fa, fb); kstep(IC<g + 1>{}, fb, fa);
    K2(0) K2(2) K2(4) K2(6) K2(8) K2(10) K2(12) K2(14) K2(16) K2(18) K2(20) K2(22) K2(24) K2(26) K2(28) K2(30)
    STAMP(6);
    if (w < 4) { WAIT_VM(0); PIN(f1v); if (!__all(f1v == epoch)) spin4(p.ws, FL_F1 + (64 * w + l) * FS, epoch, l); }
    BARE_BAR();
    gather();
    STAMP(7);
    const int stile = (s0 >> 5) + sw;
    #pragma unroll
    for (int ft = 0; ft < 2; ++ft)
      #pragma unroll
      for (int u = 0; u < 2; ++u) {
        unsigned o[4];
        #pragma unroll
        for (int jj = 0; jj < 4; ++jj) {
          const unsigned hb = pk2(acc1[ft][8 * u + 2 * jj], acc1[ft][8 * u + 2 * jj + 1]);
          const f32x2 v = {bflo(hb), bfhi(hb)};
          const f32x2 t = v * -1.44269504088896340736f;
          const f32x2 e = (f32x2){__builtin_amdgcn_exp2f(t[0]), __builtin_amdgcn_exp2f(t[1])} + 1.0f;
          const f32x2 r = v * (f32x2){__builtin_amdgcn_rcpf(e[0]), __builtin_amdgcn_rcpf(e[1])};
          o[jj] = pk2(r[0], r[1]);
        }
        const int fstep = (f0 + 64 * fw + 32 * ft + 16 * u) >> 4;
        st16_wt((u32x4){o[0], o[1], o[2], o[3]}, wsr, WS_H + ((stile * 112 + fstep) * 64 + l) * 16);
      }
    SB();
    STAMP(8);
    WAIT_VM(4);
  } else {
    if (w == 0) { WAIT_VM(2); } else { WAIT_VM(1); }
    STAMP(18);
    { PIN(er); PIN(ei);
      const int a = (l & 15) >> 2, pp = l & 3;
      #pragma unroll
      for (int rh = 0; rh < 2; ++rh)
        #pragma unroll
        for (int cb = 0; cb < 4; ++cb) { const int row = 16 * rh + 4 * q + a;
          const unsigned ad = wscr + row * 128 + ((cb ^ ((row >> 1) & 3)) * 32) + pp * 8;
          const u32x2 vr = __builtin_bit_cast(u32x2, tr_read(ad)), vi = __builtin_bit_cast(u32x2, tr_read(ad + 4096));
          const u32x4 o = {__builtin_amdgcn_perm(vi[0], vr[0], 0x05040100u), __builtin_amdgcn_perm(vi[0], vr[0], 0x07060302u), __builtin_amdgcn_perm(vi[1], vr[1], 0x05040100u), __builtin_amdgcn_perm(vi[1], vr[1], 0x07060302u)};
          st16_wt(o, wsr, WS_WT + (kq * 64 + (m0 >> 5)) * 8192 + rh * 4096 + cb * 1024 + c * 64 + q * 16); }
      st4_wt(er | (ei << 16), wsr, (kq == 3 && l < 32) ? WS_WT + 256 * 8192 + (m0 + l) * 4 : OOB); }
    STAMP(19);
    WAIT_VM(0); BAR();
    STAMP(14);
    if (w == 0) flag_line(p.ws, FL_F1 + b * FS, epoch, l);
    if (w >= 4) {
      unsigned long long pc; unsigned rest;
      asm volatile("s_getpc_b64 %0\nfused_pc_label:\n\ts_mov_b32 %1, fused_end_label-fused_pc_label" : "=s"(pc), "=s"(rest));
      const char* code = (const char*)(pc & ~127ull);
      #pragma unroll
      for (int i = 0; i < 6; ++i) { const unsigned o = (unsigned)(i * 4 + (w & 3)) * 1024u; if (o + 1024u <= rest) glds(l * 16, code + o, ldsb + (i * 4 + (w & 3)) * 1024); }
    }
    if (p2wave) p2consts();
    if (w < 4) spin4(p.ws, FL_F1 + (64 * w + l) * FS, epoch, l);
    BAR();
    gather();
    STAMP(8);
    WAIT_VM(0);
  }
  BARE_BAR();
  STAMP(9);
  const unsigned cnt = ldsb + L_CNT;
  if (p2wave) {
    const unsigned xc = ldsb + (w < 4 ? L_XCOL : L_COL2), wc = ldsb + (w < 4 ? L_WCOL : L_COL2 + 8192);
    const unsigned ub = ldsb + (w < 4 ? L_U : L_COL2 + 16384);
    bf16x8 Zr[2][2], Zi[2][2]; unsigned Wv[8];
    #pragma unroll
    for (int u = 0; u < 2; ++u)
      #pragma unroll
      for (int mt = 0; mt < 2; ++mt) { const unsigned ad = xc + ((4 * u + q) * 32 + 16 * mt + c) * 16; Zr[mt][u] = lds_read16(ad); Zi[mt][u] = lds_read16(ad + 4096); }
    #pragma unroll
    for (int i = 0; i < 8; ++i) Wv[i] = lds_read4(wc + (16 * pw + c + 64 * (16 * (i >> 2) + 4 * q + (i & 3))) * 4);
    const unsigned sgn = (c & 1) ? 0x80008000u : 0u;
    u32x4 gc1 = xor4(gc0, sgn), gs1 = xor4(gs0, sgn), ngs0 = xor4(gs0, 0x80008000u), ngs1 = xor4(gs0, sgn ^ 0x80008000u);
    u32x4 hc1 = xor4(hc0, 0x80000000u), hs1 = xor4(hs0, 0x80000000u), nhs0 = xor4(hs0, 0x80008000u), nhs1 = xor4(hs0, 0x00008000u);
    PIN(gc1); PIN(gs1); PIN(ngs0); PIN(ngs1); PIN(hc1); PIN(hs1); PIN(nhs0); PIN(nhs1);
    const bf16x8 Gc[2] = {as8(gc0), as8(gc1)}, Gs[2] = {as8(gs0), as8(gs1)}, nGs[2] = {as8(ngs0), as8(ngs1)};
    const bf16x8 Hc[2] = {as8(hc0), as8(hc1)}, Hs[2] = {as8(hs0), as8(hs1)}, nHs[2] = {as8(nhs0), as8(nhs1)};
    f32x4 Dr[2] = {}, Di[2] = {};
    #pragma unroll
    for (int u = 0; u < 2; ++u)
      #pragma unroll
      for (int mt = 0; mt < 2; ++mt) {
        Dr[mt] = mfma16(Zr[mt][u], Gc[u], Dr[mt]); Di[mt] = mfma16(Zi[mt][u], Gc[u], Di[mt]);
        Dr[mt] = mfma16(Zi[mt][u], Gs[u], Dr[mt]); Di[mt] = mfma16(Zr[mt][u], nGs[u], Di[mt]);
      }
    #pragma unroll
    for (int i = 0; i < 8; ++i) PIN(Wv[i]);
    SB(); WAIT_VM(0);
    if (lds_inc(cnt, l) == 7u && has_m1) flag_line(p.ws, FL_F2 + (rt * 16 + ct1) * FS, epoch, l);
    SB();
    u32x4 tr_, ti_;
    { float re[8], im[8];
      #pragma unroll
      for (int i = 0; i < 8; ++i) { const float x = Dr[i >> 2][i & 3], y = Di[i >> 2][i & 3]; re[i] = x * twc[i] + y * tws[i]; im[i] = y * twc[i] - x * tws[i]; }
      #pragma unroll
      for (int jj = 0; jj < 4; ++jj) { tr_[jj] = pk2(re[2 * jj], re[2 * jj + 1]); ti_[jj] = pk2(im[2 * jj], im[2 * jj + 1]); } }
    f32x4 Vr[2] = {}, Vi[2] = {};
    { const bf16x8 Tr = as8(tr_), Ti = as8(ti_);
      #pragma unroll
      for (int t = 0; t < 2; ++t) { Vr[t] = mfma16(Hc[t], Tr, Vr[t]); Vi[t] = mfma16(Hc[t], Ti, Vi[t]); }
      #pragma unroll
      for (int t = 0; t < 2; ++t) { Vr[t] = mfma16(Hs[t], Ti, Vr[t]); Vi[t] = mfma16(nHs[t], Tr, Vi[t]); } }
    { float re[8], im[8];
      #pragma unroll
      for (int i = 0; i < 8; ++i) { const float x = Vr[i >> 2][i & 3], y = Vi[i >> 2][i & 3], wr = bflo(Wv[i]), wi = bfhi(Wv[i]);
        re[i] = x * wr - y * wi; im[i] = x * wi + y * wr; }
      #pragma unroll
      for (int jj = 0; jj < 4; ++jj) { tr_[jj] = pk2(re[2 * jj], re[2 * jj + 1]); ti_[jj] = pk2(im[2 * jj], im[2 * jj + 1]); } }
    f32x4 Ur[2] = {}, Ui[2] = {};
    { const bf16x8 Tr = as8(tr_), Ti = as8(ti_);
      #pragma unroll
      for (int t = 0; t < 2; ++t) { Ur[t] = mfma16(Hc[t], Tr, Ur[t]); Ui[t] = mfma16(Hc[t], Ti, Ui[t]); }
      #pragma unroll
      for (int t = 0; t < 2; ++t) { Ur[t] = mfma16(nHs[t], Ti, Ur[t]); Ui[t] = mfma16(Hs[t], Tr, Ui[t]); } }
    #pragma unroll
    for (int t = 0; t < 2; ++t) { float re[4], im[4];
      #pragma unroll
      for (int r = 0; r < 4; ++r) { const float x = Ur[t][r], y = Ui[t][r]; re[r] = x * twc[4 * t + r] - y * tws[4 * t + r]; im[r] = y * twc[4 * t + r] + x * tws[4 * t + r]; }
      char LAS* dst = (char LAS*)(size_t)ub + (16 * pw + c) * 72 + (16 * t + 4 * q) * 2;
      *(LAS u32x2*)dst = (u32x2){pk2(re[0], re[1]), pk2(re[2], re[3])};
      *(LAS u32x2*)(dst + 64 * 72) = (u32x2){pk2(im[0], im[1]), pk2(im[2], im[3])}; }
    WAIT_LGKM0();
    { const unsigned uc = cnt + (w < 4 ? 4 : 8); lds_inc(uc, l); lds_spin(uc, 4u); }
    STAMP(10);
    f32x4 yr[2] = {}, yi[2] = {};
    const int a = (l & 15) >> 2, pp = l & 3;
    #pragma unroll
    for (int u = 0; u < 2; ++u)
      #pragma unroll
      for (int t = 0; t < 2; ++t) {
        const unsigned ad = ub + (32 * u + 8 * q + a) * 72 + (8 * pp + 4 * t) * 2;
        const bf16x8 Ar = cat4(tr_read(ad), tr_read(ad + 4 * 72));
        const bf16x8 Ai = cat4(tr_read(ad + 64 * 72), tr_read(ad + 64 * 72 + 4 * 72));
        yr[t] = mfma16(Ar, Gc[u], yr[t]); yi[t] = mfma16(Ai, Gc[u], yi[t]);
        yr[t] = mfma16(Ai, nGs[u], yr[t]); yi[t] = mfma16(Ar, Gs[u], yi[t]);
      }
    const float ysc = (kcol == 0 || kcol == 256) ? 0.5f : 1.0f;
    const u32x4 yre = {pk2(yr[0][0] * ysc, yr[0][1] * ysc), pk2(yr[0][2] * ysc, yr[0][3] * ysc), pk2(yr[1][0] * ysc, yr[1][1] * ysc), pk2(yr[1][2] * ysc, yr[1][3] * ysc)};
    const u32x4 yim = {pk2(yi[0][0], yi[0][1]), pk2(yi[0][2], yi[0][3]), pk2(yi[1][0], yi[1][1]), pk2(yi[1][2], yi[1][3])};
    const unsigned so = (32 * (16 * pw + c) + 8 * q) * 2;
    st16_wt(yre, wsr, WS_Y + kcol * YROW + so);
    st16_wt(yim, wsr, (kcol >= 1 && kcol <= 255) ? WS_Y + (256 + kcol) * YROW + so : OOB);
    gate();
    STAMP(11);
  } else {
    WAIT_VM(0);
    if (lds_inc(cnt, l) == 7u && has_m1) flag_line(p.ws, FL_F2 + (rt * 16 + ct1) * FS, epoch, l);
  }
  if (w == 4 || w == 5) {
    #pragma unroll
    for (int u = 0; u < NWS; ++u)
      #pragma unroll
      for (int j = 0; j < 8; ++j) M2_ISSUE_W2(u, j);
  }
  if (!p2wave) gate();
  unsigned vst[4];
  #pragma unroll
  for (int i = 0; i < 4; ++i)
    vst[i] = w < 4 ? vrow + ((i >> 1) * 896 + (w & 3) * 16 + (i & 1) * 8) * 1024 + ct * 128
                   : l * 16 + ((2 * pr + ((w & 3) >> 1)) * 112 + (i >> 1) * 56 + 2 * (w & 1) + (i & 1)) * 1024;
  const char* stbase = w < 4 ? (const char*)p.w2 : p.ws + WS_H;
  const unsigned stmul = w < 4 ? 65536u : 4096u;
  const unsigned stdst = __builtin_amdgcn_readfirstlane(ldsb + (w < 4 ? 0u : L_HRING) + (w & 3) * 2048);
  #define M2_ISSUE(u, i) glds_sc1(vst[i] + M2_STAGE(u) * stmul, stbase, stdst + ((u) % 5) * 16384 + ((i) >> 1) * 8192 + ((i) & 1) * 1024)
  #define M2_ISSUE_Y(u, i) glds_sc1(vrowY + (((i) >> 1) * 256 + ((u) - 14) * 64 + ((i) & 1) * 8) * YROW + (w & 3) * (16 * YROW) + pr * 128, p.ws + WS_Y, stdst + ((u) % 5) * 16384 + ((i) >> 1) * 8192 + ((i) & 1) * 1024)
  if (w >= 4) {
    if (w == 6) { spin4(p.ws, FL_F2 + ((pr >> 1) * 16 + (l < 14 ? l : 0)) * FS, epoch, l); lds_inc(cnt + 16, l); }
    lds_spin(cnt + 16, 1u);
    lds_spin(cnt, 8u);
    STAMP(12);
    #pragma unroll
    for (int u = 0; u < 4; ++u) { M2_ISSUE(u, 0); M2_ISSUE(u, 1); M2_ISSUE(u, 2); M2_ISSUE(u, 3); }
    WAIT_VM(12);
  } else { WAIT_VM(0); }
  BAR();
  if (w == 0) flag_line(p.ws, FL_F3 + b * FS, epoch, l);
  if (w >= 4) { M2_ISSUE(4, 0); M2_ISSUE(4, 1); M2_ISSUE(4, 2); M2_ISSUE(4, 3); }
  STAMP(13);
  f32x16 accb = {}, acca = {};
  {
    const int kh = w >> 2, nh = (w >> 1) & 1, sh = w & 1;
    const int h = l >> 5, blk = (l >> 4) & 1, a = (l & 15) >> 2, pp = l & 3;
    unsigned aoff[2];
    #pragma unroll
    for (int hh = 0; hh < 2; ++hh) { aoff[hh] = ldsb + hh * 65536 + kh * 8192 + (4 * h + a) * 128 + 16 * ((4 * nh + 2 * (pp & 1) + blk) ^ (4 * (a >> 1))) + 8 * (pp >> 1); PIN(aoff[hh]); }
    unsigned boffH[2], boffY[2];
    #pragma unroll
    for (int hh = 0; hh < 2; ++hh) { boffH[hh] = ldsb + L_HRING + hh * 49152 + kh * 8192 + sh * 4096 + l * 16; PIN(boffH[hh]);
      boffY[hh] = ldsb + L_HRING + hh * 49152 + kh * 8192 + (4 * h + a) * 128 + 16 * ((4 * sh + 2 * blk + (pp >> 1)) ^ (4 * (a >> 1))) + 8 * (pp & 1); PIN(boffY[hh]); }
    const int i31 = l & 31, dn = 64 * ct + 32 * nh + 16 * ((i31 >> 2) & 1) + 4 * (i31 >> 3) + (i31 & 3);
    const float dsc = (kh ? -1.0f : 1.0f) * (float)dn * (1.0f / 512.0f);
    const float cbase = (float)(4 * h) * dsc + (kh ? 0.0f : 0.25f);
    const float c256 = (dn & 1) ? -1.0f : 1.0f;
    u32x4 Cf[16];
    auto cgen = [&](auto KS, auto HALF) { constexpr int ks = decltype(KS)::v, hf = decltype(HALF)::v;
      #pragma unroll
      for (int jj = 2 * hf; jj < 2 * hf + 2; ++jj) { float v[2];
        #pragma unroll
        for (int e = 0; e < 2; ++e) { const int j = 2 * jj + e; v[e] = sin_rev(__builtin_fmaf((float)(16 * ks + 8 * (j >> 2) + (j & 3)), dsc, cbase)); }
        if (ks == 0 && jj == 0) v[0] = (kh && h == 0) ? c256 : v[0];
        Cf[ks][jj] = pk2(v[0], v[1]); }
      if (hf == 1) PIN(Cf[ks]); };
    struct F2 { bf16x8 A[4], B[4]; } fa, fb;
    auto rdA = [&](auto ST, auto KS, F2& f) { constexpr int st = decltype(ST)::v, ks = decltype(KS)::v;
      if constexpr (st < 14) { constexpr unsigned o = (st % NWS) * 16384 + ks * 2048; f.A[ks] = cat4(tr_read(aoff[o >> 16] + (o & 65535)), tr_read(aoff[o >> 16] + (o & 65535) + 1024)); } };
    auto rdB = [&](auto ST, auto KS, F2& f) { constexpr int st = decltype(ST)::v, ks = decltype(KS)::v; constexpr int sl = st % NHS, hi = sl >= 3; constexpr unsigned o = (sl - 3 * hi) * 16384;
      if constexpr (st < 14) f.B[ks] = lds_read16(boffH[hi] + o + ks * 1024);
      else f.B[ks] = cat4(tr_read(boffY[hi] + o + ks * 2048), tr_read(boffY[hi] + o + ks * 2048 + 1024)); };
    rdA(IC<0>{}, IC<0>{}, fa); rdB(IC<0>{}, IC<0>{}, fa); rdA(IC<0>{}, IC<1>{}, fa); rdB(IC<0>{}, IC<1>{}, fa);
    rdA(IC<0>{}, IC<2>{}, fa); rdB(IC<0>{}, IC<2>{}, fa); rdA(IC<0>{}, IC<3>{}, fa); rdB(IC<0>{}, IC<3>{}, fa);
    Poll f3v = {0, 0, 0, 0};
    unsigned gv = 0;
    auto step = [&](auto ST, F2& cur, F2& nxt) {
      constexpr int st = decltype(ST)::v;
      if constexpr (st <= 8) { WAIT_VM(12); }
      else if constexpr (st <= 12) {
        if (w < 4) {
          if constexpr (st <= 10) { WAIT_VM(8); } else if constexpr (st == 11) { WAIT_VM(4); } else { WAIT_VM(0); }
          if constexpr (st == 9) { if (w == 0) { poll_pin(f3v); spin_poll(f3v, p.ws + FL_F3, p.ws, epoch, l); } }
        } else { WAIT_VM(12); }
      }
      else if constexpr (st == 13) { WAIT_VM(12); } else if constexpr (st == 14) { WAIT_VM(8); } else if constexpr (st == 15) { WAIT_VM(4); } else if constexpr (st == 16) { WAIT_VM(0); }
      BAR();
      STAMP(30 + st);
      if constexpr (st == 7) { if (w == 0) poll_issue(f3v, l * FS, p.ws + FL_F3); }
      if constexpr (st == 16) gload4_sc1(gv, (64 * pr + 32 * sh + (l & 31)) * 4, p.ws + WS_G);
      auto mm = [&](auto KS) { constexpr int ks = decltype(KS)::v;
        if constexpr (st < 14) accb = mfma32(cur.A[ks], cur.B[ks], accb); else acca = mfma32(as8(Cf[4 * (st - 14) + ks]), cur.B[ks], acca); };
      auto iss = [&](auto KS) { constexpr int ks = decltype(KS)::v;
        if constexpr (st + 5 < 14) { M2_ISSUE(st + 5, ks); }
        else if constexpr (st + 5 < 18) { if (w >= 4) M2_ISSUE_Y(st + 5, ks); } };
      mm(IC<0>{}); SB();
      if constexpr (st < 17) { rdA(IC<st + 1>{}, IC<0>{}, nxt); rdB(IC<st + 1>{}, IC<0>{}, nxt); rdA(IC<st + 1>{}, IC<1>{}, nxt); rdB(IC<st + 1>{}, IC<1>{}, nxt); }
      iss(IC<0>{}); SB();
      mm(IC<1>{}); SB();
      if constexpr (st < 17) { rdA(IC<st + 1>{}, IC<2>{}, nxt); rdB(IC<st + 1>{}, IC<2>{}, nxt); rdA(IC<st + 1>{}, IC<3>{}, nxt); rdB(IC<st + 1>{}, IC<3>{}, nxt); }
      iss(IC<1>{}); SB();
      mm(IC<2>{}); SB();
      if constexpr (st < 16) cgen(IC<(st < 16 ? st : 0)>{}, IC<0>{});
      iss(IC<2>{}); SB();
      mm(IC<3>{}); SB();
      if constexpr (st < 16) cgen(IC<(st < 16 ? st : 0)>{}, IC<1>{});
      iss(IC<3>{}); SB();
    };
    step(IC<0>{}, fa, fb); step(IC<1>{}, fb, fa); step(IC<2>{}, fa, fb); step(IC<3>{}, fb, fa); step(IC<4>{}, fa, fb); step(IC<5>{}, fb, fa);
    step(IC<6>{}, fa, fb); step(IC<7>{}, fb, fa); step(IC<8>{}, fa, fb); step(IC<9>{}, fb, fa); step(IC<10>{}, fa, fb); step(IC<11>{}, fb, fa);
    step(IC<12>{}, fa, fb); step(IC<13>{}, fb, fa); step(IC<14>{}, fa, fb); step(IC<15>{}, fb, fa); step(IC<16>{}, fa, fb); step(IC<17>{}, fb, fa);
    STAMP(15);
    WAIT_VM(0); PIN(gv);
    if (b == 0 && w == 0) flag_line(p.ws, FL_E, epoch, l);
    LAS f32x4* ex = (LAS f32x4*)lds;
    float av[8], bv[8];
    auto fin = [&](auto KH) { constexpr int o = 8 * (1 - decltype(KH)::v), m = 8 * decltype(KH)::v;
      ex[(w * 4 + 0) * 64 + l] = (f32x4){accb[o], accb[o + 1], accb[o + 2], accb[o + 3]};
      ex[(w * 4 + 1) * 64 + l] = (f32x4){accb[o + 4], accb[o + 5], accb[o + 6], accb[o + 7]};
      ex[(w * 4 + 2) * 64 + l] = (f32x4){acca[o], acca[o + 1], acca[o + 2], acca[o + 3]};
      ex[(w * 4 + 3) * 64 + l] = (f32x4){acca[o + 4], acca[o + 5], acca[o + 6], acca[o + 7]};
      #pragma unroll
      for (int i = 0; i < 8; ++i) { av[i] = acca[m + i]; bv[i] = accb[m + i]; } };
    if (kh) fin(IC<1>{}); else fin(IC<0>{});
    BAR();
    const int pwv = w ^ 4;
    const f32x4 b0 = ex[(pwv * 4 + 0) * 64 + l], b1 = ex[(pwv * 4 + 1) * 64 + l], a0 = ex[(pwv * 4 + 2) * 64 + l], a1 = ex[(pwv * 4 + 3) * 64 + l];
    const int s = 64 * pr + 32 * sh + (l & 31), n = 64 * ct + 32 * nh + 16 * h + 8 * kh;
    const float g = __uint_as_float(gv), omg = rbf(1.0f - g);
    constexpr float ASC = 2.0f / 1048576.0f;
    unsigned o4[4];
    #pragma unroll
    for (int i = 0; i < 4; ++i) {
      const float a_0 = av[2 * i] + (i < 2 ? a0[2 * i] : a1[2 * i - 4]), a_1 = av[2 * i + 1] + (i < 2 ? a0[2 * i + 1] : a1[2 * i - 3]);
      const float b_0 = bv[2 * i] + (i < 2 ? b0[2 * i] : b1[2 * i - 4]), b_1 = bv[2 * i + 1] + (i < 2 ? b0[2 * i + 1] : b1[2 * i - 3]);
      const unsigned ab = pk2(a_0 * ASC, a_1 * ASC), bb = pk2(b_0, b_1);
      const unsigned t1_ = pk2(g * bflo(ab), g * bfhi(ab)), t2_ = pk2(omg * bflo(bb), omg * bfhi(bb));
      o4[i] = pk2(bflo(t1_) + bflo(t2_), bfhi(t1_) + bfhi(t2_)); }
    *(GAS u32x4*)((char*)p.out + (size_t)s * 1024 + n * 2) = (u32x4){o4[0], o4[1], o4[2], o4[3]};
  }
  STAMP(16);
  STAMP_FLUSH();
  asm volatile("fused_end_label:");
}

extern "C" void kernel_launch(void* const* d_in, const int* in_sizes, int n_in, void* d_out, int out_size, void* d_ws, size_t ws_size, hipStream_t stream) {
  Params p;
  p.ax = (const uint16_t*)d_in[0]; p.wr = (const uint16_t*)d_in[1]; p.wi = (const uint16_t*)d_in[2];
  p.w1 = (const uint16_t*)d_in[4]; p.w2 = (const uint16_t*)d_in[5]; p.gw = (const uint16_t*)d_in[6];
  p.out = (uint16_t*)d_out; p.ws = (char*)d_ws;
  (void)hipFuncSetAttribute((const void*)fused_kernel, hipFuncAttributeMaxDynamicSharedMemorySize, 163840);
  fused_kernel<<<256, 512, 163840, stream>>>(p);
}
```

```cpp
#include <hip/hip_runtime.h>
#include <hip/hip_bf16.h>
#include <stdint.h>

typedef short    bf16x8 __attribute__((ext_vector_type(8)));
typedef short    s16x4  __attribute__((ext_vector_type(4)));
typedef float    f32x2  __attribute__((ext_vector_type(2)));
typedef float    f32x4  __attribute__((ext_vector_type(4)));
typedef float    f32x16 __attribute__((ext_vector_type(16)));
typedef unsigned u32x2  __attribute__((ext_vector_type(2)));
typedef unsigned u32x4  __attribute__((ext_vector_type(4)));
typedef __bf16   bf16x2_t __attribute__((ext_vector_type(2)));
#define LAS __attribute__((address_space(3)))
#define GAS __attribute__((address_space(1)))
#define DI __device__ __forceinline__

constexpr int F_ = 1792, DF = 257;

constexpr unsigned XJ_STRIDE = 260 * 32;
constexpr unsigned FS = 128;
constexpr unsigned FL_F1 = 0, FL_F3 = 256 * FS, FL_F2 = 512 * FS, FL_E = 768 * FS, FL_ERR = 769 * FS;
constexpr unsigned WS_X  = 772 * FS;
constexpr unsigned WS_WT = WS_X + 256 * XJ_STRIDE;
constexpr unsigned YROW = 4096 + 128;
constexpr unsigned WS_Y  = WS_WT + 257 * 8192;
constexpr unsigned WS_G  = WS_Y + 512 * YROW;
constexpr unsigned WS_H  = WS_G + 8192;
constexpr unsigned WS_END = WS_H + 64 * 112 * 1024;

struct Params {
  const uint16_t *ax, *wr, *wi, *w1, *w2, *gw;
  uint16_t* out;
  char* ws;
};

DI unsigned pk2(float lo, float hi) { return __builtin_bit_cast(unsigned, __builtin_convertvector((f32x2){lo, hi}, bf16x2_t)); }
DI float bflo(unsigned u) { return __uint_as_float(u << 16); }
DI float bfhi(unsigned u) { return __uint_as_float(u & 0xffff0000u); }
DI float rbf(float x) { return bflo(pk2(x, x)); }
DI float sin_rev(float x) { return __builtin_amdgcn_sinf(x); }
DI float cos_rev(float x) { return __builtin_amdgcn_cosf(x); }
DI f32x4 mfma16(bf16x8 a, bf16x8 b, f32x4 c) { return __builtin_amdgcn_mfma_f32_16x16x32_bf16(a, b, c, 0, 0, 0); }
DI f32x16 mfma32(bf16x8 a, bf16x8 b, f32x16 c) { return __builtin_amdgcn_mfma_f32_32x32x16_bf16(a, b, c, 0, 0, 0); }
DI bf16x8 mk8(unsigned a, unsigned b, unsigned c, unsigned d) { return __builtin_bit_cast(bf16x8, (u32x4){a, b, c, d}); }
DI bf16x8 as8(u32x4 v) { return __builtin_bit_cast(bf16x8, v); }
DI u32x4 xor4(u32x4 v, unsigned m) { v ^= m; return v; }
DI bf16x8 neg8(bf16x8 v) { return as8(xor4(__builtin_bit_cast(u32x4, v), 0x80008000u)); }
DI s16x4 tr_read(unsigned addr) { return __builtin_amdgcn_ds_read_tr16_b64_v4i16((LAS s16x4*)addr); }
DI bf16x8 lds_read16(unsigned addr) { return *(const LAS bf16x8*)addr; }
DI unsigned lds_read4(unsigned addr) { return *(const LAS unsigned*)addr; }
DI unsigned lds_read2(unsigned addr) { return *(const LAS unsigned short*)addr; }
DI bf16x8 cat4(s16x4 lo, s16x4 hi) { return __builtin_shufflevector(lo, hi, 0, 1, 2, 3, 4, 5, 6, 7); }
#define WAIT_VM(n) asm volatile("s_waitcnt vmcnt(%0)" :: "n"(n) : "memory")
#define WAIT_LGKM0() asm volatile("s_waitcnt lgkmcnt(0)" ::: "memory")
#define SB() __builtin_amdgcn_sched_barrier(0)
#define BARE_BAR() do { SB(); asm volatile("" ::: "memory"); __builtin_amdgcn_s_barrier(); asm volatile("" ::: "memory"); SB(); } while (0)
#define BAR() do { SB(); WAIT_LGKM0(); __builtin_amdgcn_s_barrier(); asm volatile("" ::: "memory"); SB(); } while (0)
#define PIN(x) asm volatile("" : "+v"(x))

DI void glds(unsigned voff, const void* sbase, unsigned dst) {
  asm volatile("s_mov_b32 m0, %2\n\ts_nop 0\n\tglobal_load_lds_dwordx4 %0, %1" :: "v"(voff), "s"(sbase), "s"(dst) : "memory");
}
DI void glds_sc1(unsigned voff, const void* sbase, unsigned dst) {
  asm volatile("s_mov_b32 m0, %2\n\ts_nop 0\n\tglobal_load_lds_dwordx4 %0, %1 sc1" :: "v"(voff), "s"(sbase), "s"(dst) : "memory");
}
DI void gload16(u32x4& d, unsigned voff, const void* sbase) { asm volatile("global_load_dwordx4 %0, %1, %2" : "=v"(d) : "v"(voff), "s"(sbase) : "memory"); }
DI void gload16_sc1(u32x4& d, unsigned voff, const void* sbase) { asm volatile("global_load_dwordx4 %0, %1, %2 sc1" : "=v"(d) : "v"(voff), "s"(sbase) : "memory"); }
DI void gload4_sc1(unsigned& d, unsigned voff, const void* sbase) { asm volatile("global_load_dword %0, %1, %2 sc1" : "=v"(d) : "v"(voff), "s"(sbase) : "memory"); }
typedef __amdgpu_buffer_rsrc_t rsrc_t;
DI void st16_wt(u32x4 v, rsrc_t r, unsigned off) { __builtin_amdgcn_raw_buffer_store_b128(v, r, off, 0, 16); }
DI void st4_wt(unsigned v, rsrc_t r, unsigned off) { __builtin_amdgcn_raw_buffer_store_b32(v, r, off, 0, 16); }
constexpr unsigned OOB = 0x80000000u;
DI void flag_store(char* ws, unsigned off, unsigned epoch) { __hip_atomic_store((GAS unsigned*)(ws + off), epoch, __ATOMIC_RELAXED, __HIP_MEMORY_SCOPE_AGENT); }
DI void flag_line(char* ws, unsigned off, unsigned epoch, int l) { if (l < 32) __hip_atomic_store((GAS unsigned*)(ws + off + l * 4), epoch, __ATOMIC_RELAXED, __HIP_MEMORY_SCOPE_AGENT); }

#define STAMP(i) do {} while (0)
#define STAMP_DECL
#define STAMP_FLUSH() do {} while (0)

struct Poll { unsigned a, b, c, d; };
DI void poll_issue(Poll& v, unsigned voff, const char* ws) { gload4_sc1(v.a, voff, ws); gload4_sc1(v.b, voff, ws + 64 * FS); gload4_sc1(v.c, voff, ws + 128 * FS); gload4_sc1(v.d, voff, ws + 192 * FS); }
DI void poll_pin(Poll& v) { asm volatile("" : "+v"(v.a), "+v"(v.b), "+v"(v.c), "+v"(v.d)); }
DI void spin_poll(Poll& v, const char* base, char* ws, unsigned epoch, int l) {
  if (__all(v.a == epoch && v.b == epoch && v.c == epoch && v.d == epoch)) return;
  const unsigned long long t0 = __builtin_amdgcn_s_memrealtime();
  for (;;) {
    __builtin_amdgcn_s_sleep(1);
    poll_issue(v, l * FS, base); WAIT_VM(0); poll_pin(v);
    if (__all(v.a == epoch && v.b == epoch && v.c == epoch && v.d == epoch)) break;
    if (__builtin_amdgcn_s_memrealtime() - t0 > 2000000ull) { if (l == 0) flag_store(ws, FL_ERR, 1u); break; }
  }
}
DI void spin4(char* ws, unsigned voff, unsigned epoch, int l) {
  const unsigned long long t0 = __builtin_amdgcn_s_memrealtime();
  for (;;) {
    unsigned v; gload4_sc1(v, voff, ws); WAIT_VM(0); PIN(v);
    if (__all(v == epoch)) break;
    __builtin_amdgcn_s_sleep(1);
    if (__builtin_amdgcn_s_memrealtime() - t0 > 2000000ull) { if (l == 0) flag_store(ws, FL_ERR, 1u); break; }
  }
}

constexpr unsigned L_AUX   = 131072;
constexpr unsigned L_XROWS = L_AUX;
constexpr unsigned L_WSCR  = 32768;
constexpr unsigned L_XCOL  = L_AUX;
constexpr unsigned L_WCOL  = L_AUX + 8192;
constexpr unsigned L_U     = L_AUX + 16384;
constexpr unsigned L_GW    = L_AUX + 25600;
constexpr unsigned L_CNT   = 163840 - 32;
constexpr unsigned L_VX    = 81920;
constexpr unsigned L_W256  = 81920 + 16384;
constexpr unsigned L_HRING = 81920;
constexpr int NWS = 5, NHS = 5;

template <int N> struct IC { static constexpr int v = N; };
DI unsigned lds_inc(unsigned addr, int l) {
  unsigned old = 0;
  if (l == 0) asm volatile("ds_add_rtn_u32 %0, %1, %2\n\ts_waitcnt lgkmcnt(0)" : "=&v"(old) : "v"(addr), "v"(1u) : "memory");
  return __builtin_amdgcn_readfirstlane(old);
}
DI void lds_spin(unsigned addr, unsigned n) { while (__builtin_amdgcn_readfirstlane(__hip_atomic_load((LAS unsigned*)addr, __ATOMIC_RELAXED, __HIP_MEMORY_SCOPE_WORKGROUP)) < n) __builtin_amdgcn_s_sleep(1); asm volatile("" ::: "memory"); }

__global__ __launch_bounds__(512, 2) void fused_kernel(Params p) {
  extern __shared__ __attribute__((aligned(16))) char smem[];
  const unsigned ldsb = (unsigned)(size_t)(char LAS*)smem;
  char LAS* lds = (char LAS*)smem;
  const int b = blockIdx.x, tid = threadIdx.x;
  const int w = __builtin_amdgcn_readfirstlane(tid >> 6), l = tid & 63;
  const int xcd = b & 7, jx = b >> 3;
  const bool has_m1 = jx < 28;
  const int rt = 4 * (xcd >> 1) + jx / 7, ct1 = 7 * (xcd & 1) + jx % 7;
  const int s0 = 128 * rt, f0 = 128 * ct1;
  const int c = l & 15, q = l >> 4;
  STAMP_DECL
  STAMP(0);

  const int n2 = jx, gq = xcd, pj = xcd * 32 + jx, pr4 = w & 3, t1 = w >> 2;
  const unsigned xrw = __builtin_amdgcn_readfirstlane(ldsb + L_XROWS + w * 2048);
  { const char* xr = (const char*)p.ax + (size_t)(n2 + 256 * gq + 64 * pr4) * 1024;
    glds(l * 16, xr, xrw); glds(l * 16, xr + 32 * 1024, xrw + 1024); }
  if (w == 7) glds(l * 16, p.gw, ldsb + L_GW);
  if (w == 6) asm volatile("s_mov_b32 m0, %1\n\ts_mov_b64 exec, 1\n\tglobal_load_lds_dword %0, %2 sc1\n\ts_mov_b64 exec, -1" :: "v"(FL_E), "s"(ldsb + L_CNT + 12), "s"(p.ws) : "memory");
  unsigned vx[2], vw[2];
  #pragma unroll
  for (int i = 0; i < 2; ++i) { const int row = 8 * (2 * w + i) + (l >> 3); vx[i] = row * 1024 + ((l & 7) ^ ((row >> 1) & 7)) * 16;
    const int r2 = 4 * (2 * w + i) + (l >> 4); vw[i] = r2 * (F_ * 2) + ((l & 15) ^ ((r2 & 3) << 2)) * 16; }
  const char* xbase = (const char*)p.ax + (size_t)s0 * 1024;
  const char* w1base = (const char*)p.w1 + f0 * 2;
  const unsigned rot1 = (jx % 7 + 2 * (jx / 7)) & 7;
  const unsigned m1dst = __builtin_amdgcn_readfirstlane(ldsb + w * 2048);
  #define M1_ISSUE(s, i) do { const unsigned ks_ = ((s) + rot1) & 7; \
      if ((i) < 2) glds(vx[(i) & 1] + ks_ * 128, xbase, m1dst + ((s) & 3) * 32768 + ((i) & 1) * 1024); \
      else glds(vw[(i) & 1] + ks_ * (64 * F_ * 2), w1base, m1dst + ((s) & 3) * 32768 + 16384 + ((i) & 1) * 1024); } while (0)
  #define M1_STAGE(s) do { M1_ISSUE(s, 0); M1_ISSUE(s, 2); M1_ISSUE(s, 1); M1_ISSUE(s, 3); } while (0)
  const int wsl = ((xcd + 8 * (jx - 28)) * 8 + w) & 255, m0 = 32 * (wsl >> 2), kq = wsl & 3;
  const unsigned wscr = __builtin_amdgcn_readfirstlane(ldsb + L_WSCR + w * 8192);
  unsigned er = 0, ei = 0;
  if (!has_m1 && kq == 3) { const unsigned vo = (m0 + (l & 31)) * (DF * 2) + 512;
    asm volatile("global_load_ushort %0, %2, %3\n\tglobal_load_ushort %1, %2, %4" : "=&v"(er), "=&v"(ei) : "v"(vo), "s"(p.wr), "s"(p.wi) : "memory"); }
  const rsrc_t wsr = __builtin_amdgcn_make_buffer_rsrc(p.ws, 0, WS_END, 0x00020000);
  if (tid < 5 && tid != 3) { *(LAS unsigned*)(lds + L_CNT + tid * 4) = 0u; }
  bf16x8 Fc, Fs, Fns, Are, Aim; float p1c[4], p1s[4];
  { const float kf = (float)(16 * t1 + c), k32 = kf * (1.0f / 32.0f), q4 = (float)(4 * q);
    u32x4 uc, us, ur, ui;
    auto wl = [&](int i) { const int row = 8 * (i & 3) + (l >> 3), ch = (l & 7) ^ (((row >> 1) & 3) << 1);
      glds((m0 + row) * (DF * 2) + 128 * kq + ch * 16, i < 4 ? (const char*)p.wr : (const char*)p.wi, wscr + i * 1024); };
    #pragma unroll
    for (int jj = 0; jj < 4; ++jj) {
      SB(); if (has_m1) M1_ISSUE(0, (jj == 1 ? 2 : jj == 2 ? 1 : jj)); else { wl(2 * jj); wl(2 * jj + 1); } SB();
      float cv[2], sv[2];
      #pragma unroll
      for (int e = 0; e < 2; ++e) { const int j = 2 * jj + e; const float ang = ((float)(j < 4 ? j : 12 + j) + q4) * k32; cv[e] = cos_rev(ang); sv[e] = sin_rev(ang); }
      uc[jj] = pk2(cv[0], cv[1]); us[jj] = pk2(sv[0], sv[1]); PIN(uc[jj]); PIN(us[jj]); }
    u32x4 un = xor4(us, 0x80008000u); PIN(un);
    Fc = as8(uc); Fs = as8(us); Fns = as8(un);
    #pragma unroll
    for (int r = 0; r < 4; ++r) {
      if (r < 2) { SB(); if (has_m1) M1_ISSUE(1, 2 + r); SB(); }
      const float nr = q4 + (float)r; const float ang = nr * ((float)c * (1.0f / 16.0f)); const float cv = cos_rev(ang), sv = sin_rev(ang);
      ur[r] = pk2(cv, sv); ui[r] = pk2(-sv, cv);
      const float a2 = nr * (kf * (1.0f / 512.0f)); p1c[r] = 0.5f * cos_rev(a2); p1s[r] = 0.5f * sin_rev(a2); PIN(ur[r]); PIN(ui[r]); PIN(p1c[r]); PIN(p1s[r]); }
    Are = as8(ur); Aim = as8(ui); }
  STAMP(1);
  if (has_m1) { WAIT_VM(6); } else { WAIT_VM(8); }
  STAMP(2);
  u32x4 xv;
  { const unsigned xa = xrw + l * 8, xb = xa + 1024;
    const bf16x8 A_a = cat4(tr_read(xa), tr_read(xa + 512)), A_b = cat4(tr_read(xb), tr_read(xb + 512));
    xv = __builtin_bit_cast(u32x4, lds_read16(xrw + t1 * 1024 + l * 16));
    f32x4 Tr = {0, 0, 0, 0}, Ti = {0, 0, 0, 0};
    Tr = mfma16(A_a, Fc, Tr); Ti = mfma16(A_a, Fns, Ti);
    Tr = mfma16(A_b, Fs, Tr); Ti = mfma16(A_b, Fc, Ti);
    PIN(xv); SB();
    unsigned tb[4];
    #pragma unroll
    for (int r = 0; r < 4; ++r) { const float re = Tr[r] * p1c[r] + Ti[r] * p1s[r], im = Ti[r] * p1c[r] - Tr[r] * p1s[r]; tb[r] = pk2(re, im); }
    const bf16x8 Bt = mk8(tb[0], tb[1], tb[2], tb[3]);
    f32x4 Zr = {0, 0, 0, 0}, Zi = {0, 0, 0, 0};
    Zr = mfma16(Are, Bt, Zr); Zi = mfma16(Aim, Bt, Zi);
    #pragma unroll
    for (int r = 0; r < 4; ++r) *(LAS f32x2*)(size_t)(xrw + ((4 * q + r) * 16 + c) * 8) = (f32x2){Zr[r], Zi[r]};
    BAR();
    STAMP(3);
    if (has_m1) { M1_ISSUE(1, 0); M1_ISSUE(1, 1); }
    {
      const bool sp = tid == 1;
      const int k = tid >> 1, c2 = tid & 1, k1 = sp ? 256 : k, k2 = sp ? 256 : (512 - k) & 511; f32x2 z1[4], z2[4];
      const unsigned a1 = ldsb + L_XROWS + ((k1 >> 4) & 1) * 8192 + ((k1 >> 5) * 16 + (k1 & 15)) * 8, a2 = ldsb + L_XROWS + ((k2 >> 4) & 1) * 8192 + ((k2 >> 5) * 16 + (k2 & 15)) * 8;
      #pragma unroll
      for (int pp = 0; pp < 4; ++pp) { z1[pp] = *(const LAS f32x2*)(size_t)(a1 + pp * 2048); z2[pp] = *(const LAS f32x2*)(size_t)(a2 + pp * 2048); }
      unsigned o[4];
      #pragma unroll
      for (int pp = 0; pp < 4; ++pp) o[pp] = (c2 == 0 || sp) ? pk2(z1[pp][0] + z2[pp][0], z1[pp][1] + z2[pp][1]) : pk2(z1[pp][1] - z2[pp][1], z2[pp][0] - z1[pp][0]);
      st16_wt((u32x4){o[0], o[1], o[2], o[3]}, wsr, WS_X + pj * XJ_STRIDE + k * 32 + c2 * 16);
    }
  }
  const unsigned epoch = __builtin_amdgcn_readfirstlane(lds_read4(ldsb + L_CNT + 12)) + 1u;
  STAMP(4);

  const bool p2wave = w < 4;
  const int pw = w & 3;
  const int kcol = xcd * 32 + jx;
  const int ct = jx & 7, pr = 4 * xcd + (jx >> 3);
  u32x4 gc0, gs0, hc0, hs0; float twc[8], tws[8];
  auto p2consts = [&]() {
    const float k1f = (float)(16 * pw + c), q4f = (float)(4 * q), k64 = k1f * (1.0f / 64.0f), c32 = (float)c * (1.0f / 32.0f), k2k = k1f * (1.0f / 2048.0f);
    #pragma unroll
    for (int i = 0; i < 4; ++i) { const float a0 = (2.0f * q4f + (float)(2 * i)) * k64, a1 = (2.0f * q4f + (float)(2 * i + 1)) * k64;
      gc0[i] = pk2(cos_rev(a0), cos_rev(a1)); gs0[i] = pk2(sin_rev(a0), sin_rev(a1));
      const float b0 = (q4f + (float)(2 * i < 4 ? 2 * i : 12 + 2 * i)) * c32, b1 = (q4f + (float)(2 * i + 1 < 4 ? 2 * i + 1 : 13 + 2 * i)) * c32;
      hc0[i] = pk2(cos_rev(b0), cos_rev(b1)); hs0[i] = pk2(sin_rev(b0), sin_rev(b1)); }
    #pragma unroll
    for (int i = 0; i < 8; ++i) { const float a = (q4f + (float)(16 * (i >> 2) + (i & 3))) * k2k; twc[i] = cos_rev(a); tws[i] = sin_rev(a); }
    PIN(gc0); PIN(gs0); PIN(hc0); PIN(hs0);
    #pragma unroll
    for (int i = 0; i < 8; ++i) { PIN(twc[i]); PIN(tws[i]); } };
  auto gather = [&]() {
    const unsigned wofs = __builtin_amdgcn_readfirstlane(ldsb + w * 1024);
    const unsigned vg = (64 * (w & 3) + l) * XJ_STRIDE + (w >> 2) * 16, vt = tid * 16, kc = xcd * 32 + jx;
    glds_sc1(vg, p.ws + WS_X + kc * 32, wofs + L_XCOL);
    { const int piece = 16 * w + (l >> 2);
      glds_sc1((piece >> 1) * 8192 + (piece & 1) * 4096 + (l & 3) * 16, p.ws + WS_WT + (size_t)(kc >> 6) * (64 * 8192) + (kc & 63) * 64, wofs + L_WCOL); }
    if (b == 0) glds_sc1(vt, p.ws + WS_WT + (size_t)256 * 8192, wofs + L_W256); };
  auto gate = [&]() {
    const u32x4 gwv = __builtin_bit_cast(u32x4, lds_read16(ldsb + L_GW + l * 16));
    float sum = 0.f;
    #pragma unroll
    for (int i = 0; i < 4; ++i) { sum += rbf(bflo(xv[i]) * bflo(gwv[i])); sum += rbf(bfhi(xv[i]) * bfhi(gwv[i])); }
    sum += __builtin_bit_cast(float, __builtin_amdgcn_update_dpp(0, __builtin_bit_cast(int, sum), 0x111, 0xf, 0xf, true));
    sum += __builtin_bit_cast(float, __builtin_amdgcn_update_dpp(0, __builtin_bit_cast(int, sum), 0x112, 0xf, 0xf, true));
    sum += __builtin_bit_cast(float, __builtin_amdgcn_update_dpp(0, __builtin_bit_cast(int, sum), 0x114, 0xf, 0xf, true));
    sum += __builtin_bit_cast(float, __builtin_amdgcn_update_dpp(0, __builtin_bit_cast(int, sum), 0x118, 0xf, 0xf, true));
    sum += __builtin_bit_cast(float, __builtin_amdgcn_update_dpp(0, __builtin_bit_cast(int, sum), 0x142, 0xa, 0xf, true));
    sum += __builtin_bit_cast(float, __builtin_amdgcn_update_dpp(0, __builtin_bit_cast(int, sum), 0x143, 0xc, 0xf, true));
    const float gg = rbf(__builtin_amdgcn_rcpf(1.0f + __expf(-rbf(sum))));
    st4_wt(__float_as_uint(gg), wsr, l == 63 ? WS_G + (n2 + 256 * gq + 64 * pr4 + 32 * t1) * 4 : OOB); };

  const unsigned vrow = (l >> 3) * 1024 + ((l & 7) ^ (4 * ((l >> 4) & 1))) * 16;
  const unsigned vrowY = (l >> 3) * YROW + ((l & 7) ^ (4 * ((l >> 4) & 1))) * 16;
  #define M2_STAGE(u) (u)
  const unsigned w2dst = __builtin_amdgcn_readfirstlane(ldsb + (w & 1) * 8192), vw2 = vrow + (w & 1) * 896 * 1024 + ct * 128;
  #define M2_ISSUE_W2(u, j) glds(vw2 + (j) * 8192 + M2_STAGE(u) * 65536, p.w2, w2dst + ((u) % NWS) * 16384 + (j) * 1024)

  if (has_m1) {
    f32x16 acc1[2] = {};
    const int fw = w & 1, sw = w >> 1;
    unsigned xo[2][4], wo0[2], wo1[2];
    { const int h = l >> 5, blk = (l >> 4) & 1, a = (l & 15) >> 2, pp = l & 3, xrow = 32 * sw + (l & 31);
      #pragma unroll
      for (int hh = 0; hh < 2; ++hh) {
        #pragma unroll
        for (int ks = 0; ks < 4; ++ks) { xo[hh][ks] = ldsb + hh * 65536 + xrow * 128 + 16 * ((2 * ks + h) ^ ((xrow >> 1) & 7)); PIN(xo[hh][ks]); }
        wo0[hh] = ldsb + hh * 65536 + 16384 + (8 * h + a) * 256 + 16 * (((8 * fw + 0) ^ (a << 2)) + 2 * blk + (pp >> 1)) + 8 * (pp & 1); PIN(wo0[hh]);
        wo1[hh] = ldsb + hh * 65536 + 16384 + (8 * h + a) * 256 + 16 * (((8 * fw + 4) ^ (a << 2)) + 2 * blk + (pp >> 1)) + 8 * (pp & 1); PIN(wo1[hh]); } }
    struct Fr { bf16x8 B, A0, A1; } fa, fb;
    #define RB(f, g)  f.B  = lds_read16(xo[(((g) >> 2) & 3) >> 1][(g) & 3] + (((g) >> 2) & 1) * 32768)
    #define RA(base_, g) cat4(tr_read(base_[(((g) >> 2) & 3) >> 1] + (((g) >> 2) & 1) * 32768 + ((g) & 3) * 4096), tr_read(base_[(((g) >> 2) & 3) >> 1] + (((g) >> 2) & 1) * 32768 + ((g) & 3) * 4096 + 1024))
    SB();
    M1_STAGE(2);
    if (p2wave) p2consts();
    WAIT_VM(5);
    BARE_BAR();
    STAMP(5);
    RB(fa, 0); fa.A0 = RA(wo0, 0); fa.A1 = RA(wo1, 0);
    unsigned f1v = 0;
    auto kstep = [&](auto G, Fr& cur, Fr& nxt) {
      constexpr int g = decltype(G)::v, st = g >> 2, ks = g & 3;
      if constexpr (ks == 0 && st > 0) {
        if constexpr (st == 1) { WAIT_VM(4); }
        else if constexpr (st <= 3) { if (w == 0) { WAIT_VM(5); } else { WAIT_VM(4); } }
        else if constexpr (st <= 5) { WAIT_VM(4); }
        else if constexpr (st == 6) { if (w < 4) { WAIT_VM(0); } else { WAIT_VM(2); } }
        BARE_BAR();
        STAMP(20 + st);
        if constexpr (st == 6) { if (w < 4) gload4_sc1(f1v, (64 * w + l) * FS, p.ws + FL_F1); }
      }
      acc1[0] = mfma32(cur.A0, cur.B, acc1[0]); SB();
      if constexpr (g + 1 < 32) { RB(nxt, g + 1); nxt.A0 = RA(wo0, g + 1); }
      SB();
      acc1[1] = mfma32(cur.A1, cur.B, acc1[1]); SB();
      if constexpr (g + 1 < 32) { nxt.A1 = RA(wo1, g + 1); }
      if constexpr (st <= 4) { M1_ISSUE(st + 3, ks); }
      if constexpr (g == 7) { if (w == 0) flag_line(p.ws, FL_F1 + b * FS, epoch, l); }
      if constexpr (g == 20 || g == 21) { if (w >= 4) glds(l * 16, (const char*)p.w2 + (size_t)((xcd * 28 + jx) * 8 + (w & 3) * 2 + (g & 1)) * 1024, m1dst + (g & 1) * 1024); }
      SB();
    };
    #define K2(g) kstep(IC<g>{}, fa, fb); kstep(IC<g + 1>{}, fb, fa);
    K2(0) K2(2) K2(4) K2(6) K2(8) K2(10) K2(12) K2(14) K2(16) K2(18) K2(20) K2(22) K2(24) K2(26) K2(28) K2(30)
    STAMP(6);
    if (w < 4) { WAIT_VM(0); PIN(f1v); if (!__all(f1v == epoch)) spin4(p.ws, FL_F1 + (64 * w + l) * FS, epoch, l); }
    BARE_BAR();
    gather();
    STAMP(7);
    const int stile = (s0 >> 5) + sw;
    #pragma unroll
    for (int ft = 0; ft < 2; ++ft)
      #pragma unroll
      for (int u = 0; u < 2; ++u) {
        unsigned o[4];
        #pragma unroll
        for (int jj = 0; jj < 4; ++jj) {
          const unsigned hb = pk2(acc1[ft][8 * u + 2 * jj], acc1[ft][8 * u + 2 * jj + 1]);
          const f32x2 v = {bflo(hb), bfhi(hb)};
          const f32x2 t = v * -1.44269504088896340736f;
          const f32x2 e = (f32x2){__builtin_amdgcn_exp2f(t[0]), __builtin_amdgcn_exp2f(t[1])} + 1.0f;
          const f32x2 r = v * (f32x2){__builtin_amdgcn_rcpf(e[0]), __builtin_amdgcn_rcpf(e[1])};
          o[jj] = pk2(r[0], r[1]);
        }
        const int fstep = (f0 + 64 * fw + 32 * ft + 16 * u) >> 4;
        st16_wt((u32x4){o[0], o[1], o[2], o[3]}, wsr, WS_H + ((stile * 112 + fstep) * 64 + l) * 16);
      }
    SB();
    STAMP(8);
    WAIT_VM(4);
  } else {
    WAIT_VM(1);
    STAMP(18);
    { PIN(er); PIN(ei);
      const int a = (l & 15) >> 2, pp = l & 3;
      #pragma unroll
      for (int rh = 0; rh < 2; ++rh)
        #pragma unroll
        for (int cb = 0; cb < 4; ++cb) { const int row = 16 * rh + 4 * q + a;
          const unsigned ad = wscr + row * 128 + ((cb ^ ((row >> 1) & 3)) * 32) + pp * 8;
          const u32x2 vr = __builtin_bit_cast(u32x2, tr_read(ad)), vi = __builtin_bit_cast(u32x2, tr_read(ad + 4096));
          const u32x4 o = {__builtin_amdgcn_perm(vi[0], vr[0], 0x05040100u), __builtin_amdgcn_perm(vi[0], vr[0], 0x07060302u), __builtin_amdgcn_perm(vi[1], vr[1], 0x05040100u), __builtin_amdgcn_perm(vi[1], vr[1], 0x07060302u)};
          st16_wt(o, wsr, WS_WT + (kq * 64 + (m0 >> 5)) * 8192 + rh * 4096 + cb * 1024 + c * 64 + q * 16); }
      st4_wt(er | (ei << 16), wsr, (kq == 3 && l < 32) ? WS_WT + 256 * 8192 + (m0 + l) * 4 : OOB); }
    if (w == 1 && jx == 28 && l < 32) { __hip_atomic_store((GAS unsigned*)(p.ws + FL_F3 + (xcd * 2 + 0) * FS + l * 4), 0u, __ATOMIC_RELAXED, __HIP_MEMORY_SCOPE_AGENT);
                                        __hip_atomic_store((GAS unsigned*)(p.ws + FL_F3 + (xcd * 2 + 1) * FS + l * 4), 0u, __ATOMIC_RELAXED, __HIP_MEMORY_SCOPE_AGENT); }
    STAMP(19);
    WAIT_VM(0); BAR();
    STAMP(14);
    if (w == 0) flag_line(p.ws, FL_F1 + b * FS, epoch, l);
    if (w >= 4) {
      unsigned long long pc; unsigned rest;
      asm volatile("s_getpc_b64 %0\nfused_pc_label:\n\ts_mov_b32 %1, fused_end_label-fused_pc_label" : "=s"(pc), "=s"(rest));
      const char* code = (const char*)(pc & ~127ull);
      #pragma unroll
      for (int i = 0; i < 6; ++i) { const unsigned o = (unsigned)(i * 4 + (w & 3)) * 1024u; if (o + 1024u <= rest) glds(l * 16, code + o, ldsb + (i * 4 + (w & 3)) * 1024); }
    }
    if (p2wave) p2consts();
    if (w < 4) spin4(p.ws, FL_F1 + (64 * w + l) * FS, epoch, l);
    BAR();
    gather();
    STAMP(8);
    WAIT_VM(0);
  }
  BARE_BAR();
  STAMP(9);
  const unsigned cnt = ldsb + L_CNT;
  if (p2wave) {
    const unsigned xc = ldsb + L_XCOL, wc = ldsb + L_WCOL, ub = ldsb + L_U;
    bf16x8 Zr[2][2], Zi[2][2]; unsigned Wv[8];
    #pragma unroll
    for (int u = 0; u < 2; ++u)
      #pragma unroll
      for (int mt = 0; mt < 2; ++mt) { const unsigned ad = xc + ((4 * u + q) * 32 + 16 * mt + c) * 16; Zr[mt][u] = lds_read16(ad); Zi[mt][u] = lds_read16(ad + 4096); }
    #pragma unroll
    for (int i = 0; i < 8; ++i) Wv[i] = lds_read4(wc + (16 * pw + c + 64 * (16 * (i >> 2) + 4 * q + (i & 3))) * 4);
    const unsigned sgn = (c & 1) ? 0x80008000u : 0u;
    u32x4 gc1 = xor4(gc0, sgn), gs1 = xor4(gs0, sgn), ngs0 = xor4(gs0, 0x80008000u), ngs1 = xor4(gs0, sgn ^ 0x80008000u);
    u32x4 hc1 = xor4(hc0, 0x80000000u), hs1 = xor4(hs0, 0x80000000u), nhs0 = xor4(hs0, 0x80008000u), nhs1 = xor4(hs0, 0x00008000u);
    PIN(gc1); PIN(gs1); PIN(ngs0); PIN(ngs1); PIN(hc1); PIN(hs1); PIN(nhs0); PIN(nhs1);
    const bf16x8 Gc[2] = {as8(gc0), as8(gc1)}, Gs[2] = {as8(gs0), as8(gs1)}, nGs[2] = {as8(ngs0), as8(ngs1)};
    const bf16x8 Hc[2] = {as8(hc0), as8(hc1)}, Hs[2] = {as8(hs0), as8(hs1)}, nHs[2] = {as8(nhs0), as8(nhs1)};
    f32x4 Dr[2] = {}, Di[2] = {};
    #pragma unroll
    for (int u = 0; u < 2; ++u)
      #pragma unroll
      for (int mt = 0; mt < 2; ++mt) {
        Dr[mt] = mfma16(Zr[mt][u], Gc[u], Dr[mt]); Di[mt] = mfma16(Zi[mt][u], Gc[u], Di[mt]);
        Dr[mt] = mfma16(Zi[mt][u], Gs[u], Dr[mt]); Di[mt] = mfma16(Zr[mt][u], nGs[u], Di[mt]);
      }
    #pragma unroll
    for (int i = 0; i < 8; ++i) PIN(Wv[i]);
    SB(); WAIT_VM(0);
    if (lds_inc(cnt, l) == 7u && has_m1) flag_line(p.ws, FL_F2 + (rt * 16 + ct1) * FS, epoch, l);
    SB();
    u32x4 tr_, ti_;
    { float re[8], im[8];
      #pragma unroll
      for (int i = 0; i < 8; ++i) { const float x = Dr[i >> 2][i & 3], y = Di[i >> 2][i & 3]; re[i] = x * twc[i] + y * tws[i]; im[i] = y * twc[i] - x * tws[i]; }
      #pragma unroll
      for (int jj = 0; jj < 4; ++jj) { tr_[jj] = pk2(re[2 * jj], re[2 * jj + 1]); ti_[jj] = pk2(im[2 * jj], im[2 * jj + 1]); } }
    f32x4 Vr[2] = {}, Vi[2] = {};
    { const bf16x8 Tr = as8(tr_), Ti = as8(ti_);
      #pragma unroll
      for (int t = 0; t < 2; ++t) { Vr[t] = mfma16(Hc[t], Tr, Vr[t]); Vi[t] = mfma16(Hc[t], Ti, Vi[t]); }
      #pragma unroll
      for (int t = 0; t < 2; ++t) { Vr[t] = mfma16(Hs[t], Ti, Vr[t]); Vi[t] = mfma16(nHs[t], Tr, Vi[t]); } }
    if (kcol != 0) { float re[8], im[8];
      #pragma unroll
      for (int i = 0; i < 8; ++i) { const float x = Vr[i >> 2][i & 3], y = Vi[i >> 2][i & 3], wr = bflo(Wv[i]), wi = bfhi(Wv[i]);
        re[i] = x * wr - y * wi; im[i] = x * wi + y * wr; }
      #pragma unroll
      for (int jj = 0; jj < 4; ++jj) { tr_[jj] = pk2(re[2 * jj], re[2 * jj + 1]); ti_[jj] = pk2(im[2 * jj], im[2 * jj + 1]); }
    } else {
      const unsigned vx = ldsb + L_VX, w2c = ldsb + L_W256;
      #pragma unroll
      for (int i = 0; i < 8; ++i) { const int m = 16 * pw + c + 64 * (16 * (i >> 2) + 4 * q + (i & 3)); *(LAS f32x2*)(size_t)(vx + m * 8) = (f32x2){Vr[i >> 2][i & 3], Vi[i >> 2][i & 3]}; }
      WAIT_LGKM0();
      lds_inc(cnt + 8, l); lds_spin(cnt + 8, 4u);
      float re[8], im[8];
      #pragma unroll
      for (int i = 0; i < 8; ++i) { const int m = 16 * pw + c + 64 * (16 * (i >> 2) + 4 * q + (i & 3)), mm = (2048 - m) & 2047;
        const f32x2 zm = *(const LAS f32x2*)(size_t)(vx + mm * 8);
        const unsigned a0 = Wv[i], a1 = lds_read4(wc + mm * 4), b0 = lds_read4(w2c + m * 4), b1 = lds_read4(w2c + mm * 4);
        const float Ar = 0.5f * (bflo(a0) + bflo(a1)), Ai = 0.5f * (bfhi(a0) - bfhi(a1)), Br = 0.5f * (bflo(b0) + bflo(b1)), Bi = 0.5f * (bfhi(b0) - bfhi(b1));
        const float Pr = 0.5f * (Ar + Br), Pi = 0.5f * (Ai + Bi), Qr = 0.5f * (Ar - Br), Qi = 0.5f * (Ai - Bi);
        const float x = Vr[i >> 2][i & 3], y = Vi[i >> 2][i & 3], u = zm[0], v = -zm[1];
        re[i] = x * Pr - y * Pi + u * Qr - v * Qi; im[i] = x * Pi + y * Pr + u * Qi + v * Qr; }
      #pragma unroll
      for (int jj = 0; jj < 4; ++jj) { tr_[jj] = pk2(re[2 * jj], re[2 * jj + 1]); ti_[jj] = pk2(im[2 * jj], im[2 * jj + 1]); }
    }
    f32x4 Ur[2] = {}, Ui[2] = {};
    { const bf16x8 Tr = as8(tr_), Ti = as8(ti_);
      #pragma unroll
      for (int t = 0; t < 2; ++t) { Ur[t] = mfma16(Hc[t], Tr, Ur[t]); Ui[t] = mfma16(Hc[t], Ti, Ui[t]); }
      #pragma unroll
      for (int t = 0; t < 2; ++t) { Ur[t] = mfma16(nHs[t], Ti, Ur[t]); Ui[t] = mfma16(Hs[t], Tr, Ui[t]); } }
    #pragma unroll
    for (int t = 0; t < 2; ++t) { float re[4], im[4];
      #pragma unroll
      for (int r = 0; r < 4; ++r) { const float x = Ur[t][r], y = Ui[t][r]; re[r] = x * twc[4 * t + r] - y * tws[4 * t + r]; im[r] = y * twc[4 * t + r] + x * tws[4 * t + r]; }
      char LAS* dst = (char LAS*)(size_t)ub + (16 * pw + c) * 72 + (16 * t + 4 * q) * 2;
      *(LAS u32x2*)dst = (u32x2){pk2(re[0], re[1]), pk2(re[2], re[3])};
      *(LAS u32x2*)(dst + 64 * 72) = (u32x2){pk2(im[0], im[1]), pk2(im[2], im[3])}; }
    WAIT_LGKM0();
    { lds_inc(cnt + 4, l); lds_spin(cnt + 4, 4u); }
    STAMP(10);
    f32x4 yr[2] = {}, yi[2] = {};
    const int a = (l & 15) >> 2, pp = l & 3;
    #pragma unroll
    for (int u = 0; u < 2; ++u)
      #pragma unroll
      for (int t = 0; t < 2; ++t) {
        const unsigned ad = ub + (32 * u + 8 * q + a) * 72 + (8 * pp + 4 * t) * 2;
        const bf16x8 Ar = cat4(tr_read(ad), tr_read(ad + 4 * 72));
        const bf16x8 Ai = cat4(tr_read(ad + 64 * 72), tr_read(ad + 64 * 72 + 4 * 72));
        yr[t] = mfma16(Ar, Gc[u], yr[t]); yi[t] = mfma16(Ai, Gc[u], yi[t]);
        yr[t] = mfma16(Ai, nGs[u], yr[t]); yi[t] = mfma16(Ar, Gs[u], yi[t]);
      }
    const float ysc = kcol == 0 ? 0.5f : 1.0f;
    const u32x4 yre = {pk2(yr[0][0] * ysc, yr[0][1] * ysc), pk2(yr[0][2] * ysc, yr[0][3] * ysc), pk2(yr[1][0] * ysc, yr[1][1] * ysc), pk2(yr[1][2] * ysc, yr[1][3] * ysc)};
    const u32x4 yim = {pk2(yi[0][0] * ysc, yi[0][1] * ysc), pk2(yi[0][2] * ysc, yi[0][3] * ysc), pk2(yi[1][0] * ysc, yi[1][1] * ysc), pk2(yi[1][2] * ysc, yi[1][3] * ysc)};
    const unsigned so = (32 * (16 * pw + c) + 8 * q) * 2;
    st16_wt(yre, wsr, WS_Y + kcol * YROW + so);
    st16_wt(yim, wsr, WS_Y + (256 + kcol) * YROW + so);
    gate();
    STAMP(11);
  } else {
    WAIT_VM(0);
    if (lds_inc(cnt, l) == 7u && has_m1) flag_line(p.ws, FL_F2 + (rt * 16 + ct1) * FS, epoch, l);
  }
  if (w == 4 || w == 5) {
    #pragma unroll
    for (int u = 0; u < NWS; ++u)
      #pragma unroll
      for (int j = 0; j < 8; ++j) M2_ISSUE_W2(u, j);
  }
  if (!p2wave) gate();
  unsigned vst[4];
  #pragma unroll
  for (int i = 0; i < 4; ++i)
    vst[i] = w < 4 ? vrow + ((i >> 1) * 896 + (w & 3) * 16 + (i & 1) * 8) * 1024 + ct * 128
                   : l * 16 + ((2 * pr + ((w & 3) >> 1)) * 112 + (i >> 1) * 56 + 2 * (w & 1) + (i & 1)) * 1024;
  const char* stbase = w < 4 ? (const char*)p.w2 : p.ws + WS_H;
  const unsigned stmul = w < 4 ? 65536u : 4096u;
  const unsigned stdst = __builtin_amdgcn_readfirstlane(ldsb + (w < 4 ? 0u : L_HRING) + (w & 3) * 2048);
  #define M2_ISSUE(u, i) glds_sc1(vst[i] + M2_STAGE(u) * stmul, stbase, stdst + ((u) % 5) * 16384 + ((i) >> 1) * 8192 + ((i) & 1) * 1024)
  const unsigned vy8 = vrowY + ((w >> 2) * 256 + (w & 3) * 16) * YROW + pr * 128;
  const unsigned y8dst = __builtin_amdgcn_readfirstlane(ldsb + L_HRING + (w >> 2) * 8192 + (w & 3) * 2048);
  #define M2_ISSUE_Y(u, i) glds_sc1(vy8 + (((u) - 14) * 64 + (i) * 8) * YROW, p.ws + WS_Y, y8dst + ((u) % 5) * 16384 + (i) * 1024)
  if (w >= 4) {
    if (w == 6) { spin4(p.ws, FL_F2 + ((pr >> 1) * 16 + (l < 14 ? l : 0)) * FS, epoch, l); lds_inc(cnt + 16, l); }
    lds_spin(cnt + 16, 1u);
    lds_spin(cnt, 8u);
    if (b == 0) lds_spin(cnt + 4, 4u);
    STAMP(12);
    #pragma unroll
    for (int u = 0; u < 4; ++u) { M2_ISSUE(u, 0); M2_ISSUE(u, 1); M2_ISSUE(u, 2); M2_ISSUE(u, 3); }
    WAIT_VM(12);
  } else { WAIT_VM(0); }
  BAR();
  if (tid == 0) __hip_atomic_fetch_add((GAS unsigned*)(p.ws + FL_F3 + (b & 15) * FS), 1u, __ATOMIC_RELAXED, __HIP_MEMORY_SCOPE_AGENT);
  if (w >= 4) { M2_ISSUE(4, 0); M2_ISSUE(4, 1); M2_ISSUE(4, 2); M2_ISSUE(4, 3); }
  STAMP(13);
  f32x16 accb = {}, acca = {};
  {
    const int kh = w >> 2, nh = (w >> 1) & 1, sh = w & 1;
    const int h = l >> 5, blk = (l >> 4) & 1, a = (l & 15) >> 2, pp = l & 3;
    unsigned aoff[2];
    #pragma unroll
    for (int hh = 0; hh < 2; ++hh) { aoff[hh] = ldsb + hh * 65536 + kh * 8192 + (4 * h + a) * 128 + 16 * ((4 * nh + 2 * (pp & 1) + blk) ^ (4 * (a >> 1))) + 8 * (pp >> 1); PIN(aoff[hh]); }
    unsigned boffH[2], boffY[2];
    #pragma unroll
    for (int hh = 0; hh < 2; ++hh) { boffH[hh] = ldsb + L_HRING + hh * 49152 + kh * 8192 + sh * 4096 + l * 16; PIN(boffH[hh]);
      boffY[hh] = ldsb + L_HRING + hh * 49152 + kh * 8192 + (4 * h + a) * 128 + 16 * ((4 * sh + 2 * blk + (pp >> 1)) ^ (4 * (a >> 1))) + 8 * (pp & 1); PIN(boffY[hh]); }
    const int i31 = l & 31, dn = 64 * ct + 32 * nh + 16 * ((i31 >> 2) & 1) + 4 * (i31 >> 3) + (i31 & 3);
    const float dsc = (kh ? -1.0f : 1.0f) * (float)dn * (1.0f / 512.0f);
    const float cbase = (float)(4 * h) * dsc + (kh ? 0.0f : 0.25f);
    const float c256 = (dn & 1) ? -1.0f : 1.0f;
    u32x4 Cf[16];
    f32x2 cprev[4], ccur[4];
    const float two_c = 2.0f * cos_rev((float)(dn & 31) * (1.0f / 32.0f));
    auto cgen = [&](auto KS) { constexpr int ks = decltype(KS)::v;
      #pragma unroll
      for (int jj = 0; jj < 4; ++jj) { f32x2 v;
        if constexpr (ks < 2) {
          #pragma unroll
          for (int e = 0; e < 2; ++e) { const int j = 2 * jj + e; v[e] = sin_rev(__builtin_fmaf((float)(16 * ks + 8 * (j >> 2) + (j & 3)), dsc, cbase)); }
        } else v = ccur[jj] * two_c - cprev[jj];
        if constexpr (ks > 0) cprev[jj] = ccur[jj];
        ccur[jj] = v;
        if (ks == 0 && jj == 0) v[0] = (kh && h == 0) ? c256 : v[0];
        Cf[ks][jj] = pk2(v[0], v[1]); }
      PIN(Cf[ks]); };
    struct F2 { bf16x8 A[4], B[4]; } fa, fb;
    auto rdA = [&](auto ST, auto KS, F2& f) { constexpr int st = decltype(ST)::v, ks = decltype(KS)::v;
      if constexpr (st < 14) { constexpr unsigned o = (st % NWS) * 16384 + ks * 2048; f.A[ks] = cat4(tr_read(aoff[o >> 16] + (o & 65535)), tr_read(aoff[o >> 16] + (o & 65535) + 1024)); } };
    auto rdB = [&](auto ST, auto KS, F2& f) { constexpr int st = decltype(ST)::v, ks = decltype(KS)::v; constexpr int sl = st % NHS, hi = sl >= 3; constexpr unsigned o = (sl - 3 * hi) * 16384;
      if constexpr (st < 14) f.B[ks] = lds_read16(boffH[hi] + o + ks * 1024);
      else f.B[ks] = cat4(tr_read(boffY[hi] + o + ks * 2048), tr_read(boffY[hi] + o + ks * 2048 + 1024)); };
    rdA(IC<0>{}, IC<0>{}, fa); rdB(IC<0>{}, IC<0>{}, fa); rdA(IC<0>{}, IC<1>{}, fa); rdB(IC<0>{}, IC<1>{}, fa);
    rdA(IC<0>{}, IC<2>{}, fa); rdB(IC<0>{}, IC<2>{}, fa); rdA(IC<0>{}, IC<3>{}, fa); rdB(IC<0>{}, IC<3>{}, fa);
    unsigned f3v = 0;
    unsigned gv = 0;
    auto step = [&](auto ST, F2& cur, F2& nxt) {
      constexpr int st = decltype(ST)::v;
      if constexpr (st <= 8) { WAIT_VM(12); }
      else if constexpr (st == 9) { if (w < 4) { WAIT_VM(8); if (w == 0) { PIN(f3v); if (!__all(f3v == 16u)) spin4(p.ws, FL_F3 + (l & 15) * FS, 16u, l); } } else { WAIT_VM(12); } }
      else if constexpr (st == 10) { WAIT_VM(10); } else if constexpr (st == 11) { WAIT_VM(8); } else if constexpr (st <= 13) { WAIT_VM(6); }
      else if constexpr (st == 14) { WAIT_VM(4); } else if constexpr (st == 15) { WAIT_VM(2); } else if constexpr (st == 16) { WAIT_VM(0); }
      BAR();
      STAMP(30 + st);
      if constexpr (st == 7) { if (w == 0) gload4_sc1(f3v, (l & 15) * FS, p.ws + FL_F3); }
      if constexpr (st == 16) gload4_sc1(gv, (64 * pr + 32 * sh + (l & 31)) * 4, p.ws + WS_G);
      auto mm = [&](auto KS) { constexpr int ks = decltype(KS)::v;
        if constexpr (st < 14) accb = mfma32(cur.A[ks], cur.B[ks], accb); else acca = mfma32(as8(Cf[4 * (st - 14) + ks]), cur.B[ks], acca); };
      auto iss = [&](auto KS) { constexpr int ks = decltype(KS)::v;
        if constexpr (st + 5 < 14) { M2_ISSUE(st + 5, ks); }
        else if constexpr (st + 5 < 18 && ks >= 1 && ks <= 2) { M2_ISSUE_Y(st + 5, ks - 1); } };
      mm(IC<0>{}); SB();
      if constexpr (st < 17) { rdA(IC<st + 1>{}, IC<0>{}, nxt); rdB(IC<st + 1>{}, IC<0>{}, nxt); rdA(IC<st + 1>{}, IC<1>{}, nxt); rdB(IC<st + 1>{}, IC<1>{}, nxt); }
      iss(IC<0>{});
      SB();
      mm(IC<1>{}); SB();
      if constexpr (st < 17) { rdA(IC<st + 1>{}, IC<2>{}, nxt); rdB(IC<st + 1>{}, IC<2>{}, nxt); rdA(IC<st + 1>{}, IC<3>{}, nxt); rdB(IC<st + 1>{}, IC<3>{}, nxt); }
      iss(IC<1>{});
      SB();
      mm(IC<2>{}); SB();
      iss(IC<2>{});
      SB();
      mm(IC<3>{}); SB();
      iss(IC<3>{});
      if constexpr (st < 16) cgen(IC<(st < 16 ? st : 0)>{});
      SB();
    };
    step(IC<0>{}, fa, fb); step(IC<1>{}, fb, fa); step(IC<2>{}, fa, fb); step(IC<3>{}, fb, fa); step(IC<4>{}, fa, fb); step(IC<5>{}, fb, fa);
    step(IC<6>{}, fa, fb); step(IC<7>{}, fb, fa); step(IC<8>{}, fa, fb); step(IC<9>{}, fb, fa); step(IC<10>{}, fa, fb); step(IC<11>{}, fb, fa);
    step(IC<12>{}, fa, fb); step(IC<13>{}, fb, fa); step(IC<14>{}, fa, fb); step(IC<15>{}, fb, fa); step(IC<16>{}, fa, fb); step(IC<17>{}, fb, fa);
    STAMP(15);
    WAIT_VM(0); PIN(gv);
    if (b == 0 && w == 0) flag_line(p.ws, FL_E, epoch, l);
    LAS f32x4* ex = (LAS f32x4*)lds;
    float av[8], bv[8];
    auto fin = [&](auto KH) { constexpr int o = 8 * (1 - decltype(KH)::v), m = 8 * decltype(KH)::v;
      ex[(w * 4 + 0) * 64 + l] = (f32x4){accb[o], accb[o + 1], accb[o + 2], accb[o + 3]};
      ex[(w * 4 + 1) * 64 + l] = (f32x4){accb[o + 4], accb[o + 5], accb[o + 6], accb[o + 7]};
      ex[(w * 4 + 2) * 64 + l] = (f32x4){acca[o], acca[o + 1], acca[o + 2], acca[o + 3]};
      ex[(w * 4 + 3) * 64 + l] = (f32x4){acca[o + 4], acca[o + 5], acca[o + 6], acca[o + 7]};
      #pragma unroll
      for (int i = 0; i < 8; ++i) { av[i] = acca[m + i]; bv[i] = accb[m + i]; } };
    if (kh) fin(IC<1>{}); else fin(IC<0>{});
    BAR();
    const int pwv = w ^ 4;
    const f32x4 b0 = ex[(pwv * 4 + 0) * 64 + l], b1 = ex[(pwv * 4 + 1) * 64 + l], a0 = ex[(pwv * 4 + 2) * 64 + l], a1 = ex[(pwv * 4 + 3) * 64 + l];
    const int s = 64 * pr + 32 * sh + (l & 31), n = 64 * ct + 32 * nh + 16 * h + 8 * kh;
    const float g = __uint_as_float(gv), omg = rbf(1.0f - g);
    constexpr float ASC = 2.0f / 1048576.0f;
    unsigned o4[4];
    #pragma unroll
    for (int i = 0; i < 4; ++i) {
      const float a_0 = av[2 * i] + (i < 2 ? a0[2 * i] : a1[2 * i - 4]), a_1 = av[2 * i + 1] + (i < 2 ? a0[2 * i + 1] : a1[2 * i - 3]);
      const float b_0 = bv[2 * i] + (i < 2 ? b0[2 * i] : b1[2 * i - 4]), b_1 = bv[2 * i + 1] + (i < 2 ? b0[2 * i + 1] : b1[2 * i - 3]);
      const unsigned ab = pk2(a_0 * ASC, a_1 * ASC), bb = pk2(b_0, b_1);
      const unsigned t1_ = pk2(g * bflo(ab), g * bfhi(ab)), t2_ = pk2(omg * bflo(bb), omg * bfhi(bb));
      o4[i] = pk2(bflo(t1_) + bflo(t2_), bfhi(t1_) + bfhi(t2_)); }
    *(GAS u32x4*)((char*)p.out + (size_t)s * 1024 + n * 2) = (u32x4){o4[0], o4[1], o4[2], o4[3]};
  }
  STAMP(16);
  STAMP_FLUSH();
  asm volatile("fused_end_label:");
}

extern "C" void kernel_launch(void* const* d_in, const int* in_sizes, int n_in, void* d_out, int out_size, void* d_ws, size_t ws_size, hipStream_t stream) {
  Params p;
  p.ax = (const uint16_t*)d_in[0]; p.wr = (const uint16_t*)d_in[1]; p.wi = (const uint16_t*)d_in[2];
  p.w1 = (const uint16_t*)d_in[4]; p.w2 = (const uint16_t*)d_in[5]; p.gw = (const uint16_t*)d_in[6];
  p.out = (uint16_t*)d_out; p.ws = (char*)d_ws;
  (void)hipFuncSetAttribute((const void*)fused_kernel, hipFuncAttributeMaxDynamicSharedMemorySize, 163840);
  fused_kernel<<<256, 512, 163840, stream>>>(p);
}
```

```cpp
#include <hip/hip_runtime.h>
#include <hip/hip_bf16.h>
#include <stdint.h>

typedef short    bf16x8 __attribute__((ext_vector_type(8)));
typedef short    s16x4  __attribute__((ext_vector_type(4)));
typedef float    f32x2  __attribute__((ext_vector_type(2)));
typedef float    f32x4  __attribute__((ext_vector_type(4)));
typedef float    f32x16 __attribute__((ext_vector_type(16)));
typedef unsigned u32x2  __attribute__((ext_vector_type(2)));
typedef unsigned u32x4  __attribute__((ext_vector_type(4)));
typedef __bf16   bf16x2_t __attribute__((ext_vector_type(2)));
#define LAS __attribute__((address_space(3)))
#define GAS __attribute__((address_space(1)))
#define DI __device__ __forceinline__

constexpr int F_ = 1792, DF = 257;

constexpr unsigned XJ_STRIDE = 260 * 32;
constexpr unsigned FS = 128;
constexpr unsigned FL_F1 = 0, FL_F3 = 256 * FS, FL_F2 = 512 * FS, FL_E = 768 * FS, FL_ERR = 769 * FS, FL_FW = 772 * FS;
constexpr unsigned WS_X  = 804 * FS;
constexpr unsigned WS_WT = WS_X + 256 * XJ_STRIDE;
constexpr unsigned YROW = 4096 + 128;
constexpr unsigned WS_Y  = WS_WT + 257 * 8192;
constexpr unsigned WS_G  = WS_Y + 512 * YROW;
constexpr unsigned WS_H  = WS_G + 8192;
constexpr unsigned WS_END = WS_H + 64 * 112 * 1024;

struct Params {
  const uint16_t *ax, *wr, *wi, *w1, *w2, *gw;
  uint16_t* out;
  char* ws;
};

DI unsigned pk2(float lo, float hi) { return __builtin_bit_cast(unsigned, __builtin_convertvector((f32x2){lo, hi}, bf16x2_t)); }
DI float bflo(unsigned u) { return __uint_as_float(u << 16); }
DI float bfhi(unsigned u) { return __uint_as_float(u & 0xffff0000u); }
DI float rbf(float x) { return bflo(pk2(x, x)); }
DI float sin_rev(float x) { return __builtin_amdgcn_sinf(x); }
DI float cos_rev(float x) { return __builtin_amdgcn_cosf(x); }
DI f32x4 mfma16(bf16x8 a, bf16x8 b, f32x4 c) { return __builtin_amdgcn_mfma_f32_16x16x32_bf16(a, b, c, 0, 0, 0); }
DI f32x16 mfma32(bf16x8 a, bf16x8 b, f32x16 c) { return __builtin_amdgcn_mfma_f32_32x32x16_bf16(a, b, c, 0, 0, 0); }
DI bf16x8 mk8(unsigned a, unsigned b, unsigned c, unsigned d) { return __builtin_bit_cast(bf16x8, (u32x4){a, b, c, d}); }
DI bf16x8 as8(u32x4 v) { return __builtin_bit_cast(bf16x8, v); }
DI u32x4 xor4(u32x4 v, unsigned m) { v ^= m; return v; }
DI bf16x8 neg8(bf16x8 v) { return as8(xor4(__builtin_bit_cast(u32x4, v), 0x80008000u)); }
DI s16x4 tr_read(unsigned addr) { return __builtin_amdgcn_ds_read_tr16_b64_v4i16((LAS s16x4*)addr); }
DI bf16x8 lds_read16(unsigned addr) { return *(const LAS bf16x8*)addr; }
DI unsigned lds_read4(unsigned addr) { return *(const LAS unsigned*)addr; }
DI unsigned lds_read2(unsigned addr) { return *(const LAS unsigned short*)addr; }
DI bf16x8 cat4(s16x4 lo, s16x4 hi) { return __builtin_shufflevector(lo, hi, 0, 1, 2, 3, 4, 5, 6, 7); }
#define WAIT_VM(n) asm volatile("s_waitcnt vmcnt(%0)" :: "n"(n) : "memory")
#define WAIT_LGKM0() asm volatile("s_waitcnt lgkmcnt(0)" ::: "memory")
#define SB() __builtin_amdgcn_sched_barrier(0)
#define BARE_BAR() do { SB(); asm volatile("" ::: "memory"); __builtin_amdgcn_s_barrier(); asm volatile("" ::: "memory"); SB(); } while (0)
#define BAR() do { SB(); WAIT_LGKM0(); __builtin_amdgcn_s_barrier(); asm volatile("" ::: "memory"); SB(); } while (0)
#define PIN(x) asm volatile("" : "+v"(x))

DI void glds(unsigned voff, const void* sbase, unsigned dst) {
  asm volatile("s_mov_b32 m0, %2\n\ts_nop 0\n\tglobal_load_lds_dwordx4 %0, %1" :: "v"(voff), "s"(sbase), "s"(dst) : "memory");
}
DI void glds_sc1(unsigned voff, const void* sbase, unsigned dst) {
  asm volatile("s_mov_b32 m0, %2\n\ts_nop 0\n\tglobal_load_lds_dwordx4 %0, %1 sc1" :: "v"(voff), "s"(sbase), "s"(dst) : "memory");
}
DI void gload16(u32x4& d, unsigned voff, const void* sbase) { asm volatile("global_load_dwordx4 %0, %1, %2" : "=v"(d) : "v"(voff), "s"(sbase) : "memory"); }
DI void gload16_sc1(u32x4& d, unsigned voff, const void* sbase) { asm volatile("global_load_dwordx4 %0, %1, %2 sc1" : "=v"(d) : "v"(voff), "s"(sbase) : "memory"); }
DI void gload4_sc1(unsigned& d, unsigned voff, const void* sbase) { asm volatile("global_load_dword %0, %1, %2 sc1" : "=v"(d) : "v"(voff), "s"(sbase) : "memory"); }
typedef __amdgpu_buffer_rsrc_t rsrc_t;
DI void st16_wt(u32x4 v, rsrc_t r, unsigned off) { __builtin_amdgcn_raw_buffer_store_b128(v, r, off, 0, 16); }
DI void st4_wt(unsigned v, rsrc_t r, unsigned off) { __builtin_amdgcn_raw_buffer_store_b32(v, r, off, 0, 16); }
constexpr unsigned OOB = 0x80000000u;
DI void flag_store(char* ws, unsigned off, unsigned epoch) { __hip_atomic_store((GAS unsigned*)(ws + off), epoch, __ATOMIC_RELAXED, __HIP_MEMORY_SCOPE_AGENT); }
DI void flag_line(char* ws, unsigned off, unsigned epoch, int l) { if (l < 32) __hip_atomic_store((GAS unsigned*)(ws + off + l * 4), epoch, __ATOMIC_RELAXED, __HIP_MEMORY_SCOPE_AGENT); }

#define STAMP(i) do {} while (0)
#define STAMP_DECL
#define STAMP_FLUSH() do {} while (0)

struct Poll { unsigned a, b, c, d; };
DI void poll_issue(Poll& v, unsigned voff, const char* ws) { gload4_sc1(v.a, voff, ws); gload4_sc1(v.b, voff, ws + 64 * FS); gload4_sc1(v.c, voff, ws + 128 * FS); gload4_sc1(v.d, voff, ws + 192 * FS); }
DI void poll_pin(Poll& v) { asm volatile("" : "+v"(v.a), "+v"(v.b), "+v"(v.c), "+v"(v.d)); }
DI void spin_poll(Poll& v, const char* base, char* ws, unsigned epoch, int l) {
  if (__all(v.a == epoch && v.b == epoch && v.c == epoch && v.d == epoch)) return;
  const unsigned long long t0 = __builtin_amdgcn_s_memrealtime();
  for (;;) {
    __builtin_amdgcn_s_sleep(1);
    poll_issue(v, l * FS, base); WAIT_VM(0); poll_pin(v);
    if (__all(v.a == epoch && v.b == epoch && v.c == epoch && v.d == epoch)) break;
    if (__builtin_amdgcn_s_memrealtime() - t0 > 2000000ull) { if (l == 0) flag_store(ws, FL_ERR, 1u); break; }
  }
}
DI void spin4(char* ws, unsigned voff, unsigned epoch, int l) {
  const unsigned long long t0 = __builtin_amdgcn_s_memrealtime();
  for (;;) {
    unsigned v; gload4_sc1(v, voff, ws); WAIT_VM(0); PIN(v);
    if (__all(v == epoch)) break;
    __builtin_amdgcn_s_sleep(1);
    if (__builtin_amdgcn_s_memrealtime() - t0 > 2000000ull) { if (l == 0) flag_store(ws, FL_ERR, 1u); break; }
  }
}

constexpr unsigned L_AUX   = 131072;
constexpr unsigned L_XROWS = L_AUX;
constexpr unsigned L_WSCR  = 32768;
constexpr unsigned L_XCOL  = L_AUX;
constexpr unsigned L_WCOL  = L_AUX + 8192;
constexpr unsigned L_U     = L_AUX + 16384;
constexpr unsigned L_GW    = L_AUX + 25600;
constexpr unsigned L_HX    = 114688;
constexpr unsigned L_DUMMY = L_AUX + 26624;
constexpr unsigned L_CNT   = 163840 - 32;
constexpr unsigned L_VX    = 81920;
constexpr unsigned L_W256  = 81920 + 16384;
constexpr unsigned L_HRING = 81920;
constexpr int NWS = 5, NHS = 5;

template <int N> struct IC { static constexpr int v = N; };
DI unsigned lds_inc(unsigned addr, int l) {
  unsigned old = 0;
  if (l == 0) asm volatile("ds_add_rtn_u32 %0, %1, %2\n\ts_waitcnt lgkmcnt(0)" : "=&v"(old) : "v"(addr), "v"(1u) : "memory");
  return __builtin_amdgcn_readfirstlane(old);
}
DI void lds_spin(unsigned addr, unsigned n) { while (__builtin_amdgcn_readfirstlane(__hip_atomic_load((LAS unsigned*)addr, __ATOMIC_RELAXED, __HIP_MEMORY_SCOPE_WORKGROUP)) < n) __builtin_amdgcn_s_sleep(1); asm volatile("" ::: "memory"); }

__global__ __launch_bounds__(512, 2) void fused_kernel(Params p) {
  extern __shared__ __attribute__((aligned(16))) char smem[];
  const unsigned ldsb = (unsigned)(size_t)(char LAS*)smem;
  char LAS* lds = (char LAS*)smem;
  const int b = blockIdx.x, tid = threadIdx.x;
  const int w = __builtin_amdgcn_readfirstlane(tid >> 6), l = tid & 63;
  const int xcd = b & 7, jx = b >> 3;
  const bool has_m1 = jx < 28;
  const int rt = 4 * (xcd >> 1) + jx / 7, ct1 = 7 * (xcd & 1) + jx % 7;
  const int s0 = 128 * rt, f0 = 128 * ct1;
  const int c = l & 15, q = l >> 4;
  STAMP_DECL
  STAMP(0);

  const int n2 = jx, gq = xcd, pj = xcd * 32 + jx, pr4 = w & 3, t1 = w >> 2;
  const unsigned xrw = __builtin_amdgcn_readfirstlane(ldsb + L_XROWS + w * 2048);
  { const char* xr = (const char*)p.ax + (size_t)(n2 + 256 * gq + 64 * pr4) * 1024;
    glds(l * 16, xr, xrw); glds(l * 16, xr + 32 * 1024, xrw + 1024); }
  if (w == 7) glds(l * 16, p.gw, ldsb + L_GW);
  if (w == 6) asm volatile("s_mov_b32 m0, %1\n\ts_mov_b64 exec, 1\n\tglobal_load_lds_dword %0, %2 sc1\n\ts_mov_b64 exec, -1" :: "v"(FL_E), "s"(ldsb + L_CNT + 12), "s"(p.ws) : "memory");
  unsigned vx[2], vw[2];
  #pragma unroll
  for (int i = 0; i < 2; ++i) { const int row = 8 * (2 * w + i) + (l >> 3); vx[i] = row * 1024 + ((l & 7) ^ ((row >> 1) & 7)) * 16;
    const int r2 = 4 * (2 * w + i) + (l >> 4); vw[i] = r2 * (F_ * 2) + ((l & 15) ^ ((r2 & 3) << 2)) * 16; }
  const char* xbase = (const char*)p.ax + (size_t)s0 * 1024;
  const char* w1base = (const char*)p.w1 + f0 * 2;
  const unsigned rot1 = (jx % 7 + 2 * (jx / 7)) & 7;
  const unsigned m1dst = __builtin_amdgcn_readfirstlane(ldsb + w * 2048);
  #define M1_ISSUE(s, i) do { const unsigned ks_ = ((s) + rot1) & 7; \
      if ((i) < 2) glds(vx[(i) & 1] + ks_ * 128, xbase, m1dst + ((s) & 3) * 32768 + ((i) & 1) * 1024); \
      else glds(vw[(i) & 1] + ks_ * (64 * F_ * 2), w1base, m1dst + ((s) & 3) * 32768 + 16384 + ((i) & 1) * 1024); } while (0)
  #define M1_STAGE(s) do { M1_ISSUE(s, 0); M1_ISSUE(s, 2); M1_ISSUE(s, 1); M1_ISSUE(s, 3); } while (0)
  const int wsl = ((xcd + 8 * (jx - 28)) * 8 + w) & 255, m0 = 32 * (wsl >> 2), kq = wsl & 3;
  const unsigned wscr = __builtin_amdgcn_readfirstlane(ldsb + L_WSCR + w * 8192);
  unsigned er = 0, ei = 0;
  if (!has_m1 && kq == 3) { const unsigned vo = (m0 + (l & 31)) * (DF * 2) + 512;
    asm volatile("global_load_ushort %0, %2, %3\n\tglobal_load_ushort %1, %2, %4" : "=&v"(er), "=&v"(ei) : "v"(vo), "s"(p.wr), "s"(p.wi) : "memory"); }
  const rsrc_t wsr = __builtin_amdgcn_make_buffer_rsrc(p.ws, 0, WS_END, 0x00020000);
  if (tid < 8 && tid != 3) { *(LAS unsigned*)(lds + L_CNT + tid * 4) = 0u; }
  bf16x8 Fc, Fs, Fns, Are, Aim; float p1c[4], p1s[4];
  { const float kf = (float)(16 * t1 + c), k32 = kf * (1.0f / 32.0f), q4 = (float)(4 * q);
    u32x4 uc, us, ur, ui;
    auto wl = [&](int i) { const int row = 8 * (i & 3) + (l >> 3), ch = (l & 7) ^ (((row >> 1) & 3) << 1);
      glds((m0 + row) * (DF * 2) + 128 * kq + ch * 16, i < 4 ? (const char*)p.wr : (const char*)p.wi, wscr + i * 1024); };
    #pragma unroll
    for (int jj = 0; jj < 4; ++jj) {
      SB(); if (has_m1) M1_ISSUE(0, (jj == 1 ? 2 : jj == 2 ? 1 : jj)); else { wl(2 * jj); wl(2 * jj + 1); } SB();
      float cv[2], sv[2];
      #pragma unroll
      for (int e = 0; e < 2; ++e) { const int j = 2 * jj + e; const float ang = ((float)(j < 4 ? j : 12 + j) + q4) * k32; cv[e] = cos_rev(ang); sv[e] = sin_rev(ang); }
      uc[jj] = pk2(cv[0], cv[1]); us[jj] = pk2(sv[0], sv[1]); PIN(uc[jj]); PIN(us[jj]); }
    u32x4 un = xor4(us, 0x80008000u); PIN(un);
    Fc = as8(uc); Fs = as8(us); Fns = as8(un);
    #pragma unroll
    for (int r = 0; r < 4; ++r) {
      if (r < 2) { SB(); if (has_m1) M1_ISSUE(1, 2 + r); SB(); }
      const float nr = q4 + (float)r; const float ang = nr * ((float)c * (1.0f / 16.0f)); const float cv = cos_rev(ang), sv = sin_rev(ang);
      ur[r] = pk2(cv, sv); ui[r] = pk2(-sv, cv);
      const float a2 = nr * (kf * (1.0f / 512.0f)); p1c[r] = 0.5f * cos_rev(a2); p1s[r] = 0.5f * sin_rev(a2); PIN(ur[r]); PIN(ui[r]); PIN(p1c[r]); PIN(p1s[r]); }
    Are = as8(ur); Aim = as8(ui); }
  STAMP(1);
  if (has_m1) { WAIT_VM(6); } else { WAIT_VM(8); }
  STAMP(2);
  u32x4 xv;
  { const unsigned xa = xrw + l * 8, xb = xa + 1024;
    const bf16x8 A_a = cat4(tr_read(xa), tr_read(xa + 512)), A_b = cat4(tr_read(xb), tr_read(xb + 512));
    xv = __builtin_bit_cast(u32x4, lds_read16(xrw + t1 * 1024 + l * 16));
    f32x4 Tr = {0, 0, 0, 0}, Ti = {0, 0, 0, 0};
    Tr = mfma16(A_a, Fc, Tr); Ti = mfma16(A_a, Fns, Ti);
    Tr = mfma16(A_b, Fs, Tr); Ti = mfma16(A_b, Fc, Ti);
    PIN(xv); SB();
    unsigned tb[4];
    #pragma unroll
    for (int r = 0; r < 4; ++r) { const float re = Tr[r] * p1c[r] + Ti[r] * p1s[r], im = Ti[r] * p1c[r] - Tr[r] * p1s[r]; tb[r] = pk2(re, im); }
    const bf16x8 Bt = mk8(tb[0], tb[1], tb[2], tb[3]);
    f32x4 Zr = {0, 0, 0, 0}, Zi = {0, 0, 0, 0};
    Zr = mfma16(Are, Bt, Zr); Zi = mfma16(Aim, Bt, Zi);
    #pragma unroll
    for (int r = 0; r < 4; ++r) *(LAS f32x2*)(size_t)(xrw + ((4 * q + r) * 16 + c) * 8) = (f32x2){Zr[r], Zi[r]};
    BAR();
    STAMP(3);
    if (has_m1) { M1_ISSUE(1, 0); M1_ISSUE(1, 1); }
    {
      const bool sp = tid == 1;
      const int k = tid >> 1, c2 = tid & 1, k1 = sp ? 256 : k, k2 = sp ? 256 : (512 - k) & 511; f32x2 z1[4], z2[4];
      const unsigned a1 = ldsb + L_XROWS + ((k1 >> 4) & 1) * 8192 + ((k1 >> 5) * 16 + (k1 & 15)) * 8, a2 = ldsb + L_XROWS + ((k2 >> 4) & 1) * 8192 + ((k2 >> 5) * 16 + (k2 & 15)) * 8;
      #pragma unroll
      for (int pp = 0; pp < 4; ++pp) { z1[pp] = *(const LAS f32x2*)(size_t)(a1 + pp * 2048); z2[pp] = *(const LAS f32x2*)(size_t)(a2 + pp * 2048); }
      unsigned o[4];
      #pragma unroll
      for (int pp = 0; pp < 4; ++pp) o[pp] = (c2 == 0 || sp) ? pk2(z1[pp][0] + z2[pp][0], z1[pp][1] + z2[pp][1]) : pk2(z1[pp][1] - z2[pp][1], z2[pp][0] - z1[pp][0]);
      st16_wt((u32x4){o[0], o[1], o[2], o[3]}, wsr, WS_X + pj * XJ_STRIDE + k * 32 + c2 * 16);
    }
  }
  const unsigned epoch = __builtin_amdgcn_readfirstlane(lds_read4(ldsb + L_CNT + 12)) + 1u;
  STAMP(4);

  const bool p2wave = w < 4;
  const int pw = w & 3;
  const int kcol = xcd * 32 + jx;
  const int ct = jx & 7, pr = 4 * xcd + (jx >> 3);
  u32x4 gc0, gs0, hc0, hs0; float twc[8], tws[8];
  auto p2consts = [&]() {
    const float k1f = (float)(16 * pw + c), q4f = (float)(4 * q), k64 = k1f * (1.0f / 64.0f), c32 = (float)c * (1.0f / 32.0f), k2k = k1f * (1.0f / 2048.0f);
    #pragma unroll
    for (int i = 0; i < 4; ++i) { const float a0 = (2.0f * q4f + (float)(2 * i)) * k64, a1 = (2.0f * q4f + (float)(2 * i + 1)) * k64;
      gc0[i] = pk2(cos_rev(a0), cos_rev(a1)); gs0[i] = pk2(sin_rev(a0), sin_rev(a1));
      const float b0 = (q4f + (float)(2 * i < 4 ? 2 * i : 12 + 2 * i)) * c32, b1 = (q4f + (float)(2 * i + 1 < 4 ? 2 * i + 1 : 13 + 2 * i)) * c32;
      hc0[i] = pk2(cos_rev(b0), cos_rev(b1)); hs0[i] = pk2(sin_rev(b0), sin_rev(b1)); }
    #pragma unroll
    for (int i = 0; i < 8; ++i) { const float a = (q4f + (float)(16 * (i >> 2) + (i & 3))) * k2k; twc[i] = cos_rev(a); tws[i] = sin_rev(a); }
    PIN(gc0); PIN(gs0); PIN(hc0); PIN(hs0);
    #pragma unroll
    for (int i = 0; i < 8; ++i) { PIN(twc[i]); PIN(tws[i]); } };
  const unsigned cnt = ldsb + L_CNT;
  auto xgather = [&](int half) { glds_sc1((64 * pw + l) * XJ_STRIDE + half * 16, p.ws + WS_X + kcol * 32, __builtin_amdgcn_readfirstlane(ldsb + L_XCOL + pw * 1024 + half * 4096)); };
  unsigned Wv[8];
  auto wload = [&]() {
    const char* wb = p.ws + WS_WT + (size_t)(kcol >> 6) * (64 * 8192) + (kcol & 63) * 64;
    #pragma unroll
    for (int i = 0; i < 8; ++i) gload4_sc1(Wv[i], ((pw >> 1) + 2 * (16 * (i >> 2) + 4 * q + (i & 3))) * 8192 + (pw & 1) * 4096 + c * 4, wb);
    if (b == 0) { const unsigned wofs = __builtin_amdgcn_readfirstlane(ldsb + pw * 1024);
      #pragma unroll
      for (int i = 0; i < 2; ++i) { const int piece = 16 * (pw + 4 * i) + (l >> 2);
        glds_sc1((piece >> 1) * 8192 + (piece & 1) * 4096 + (l & 3) * 16, wb, wofs + L_WCOL + i * 4096);
        glds_sc1(i * 4096 + pw * 1024 + l * 16, p.ws + WS_WT + (size_t)256 * 8192, wofs + L_W256 + i * 4096); } } };
  auto h_done = [&]() { if (lds_inc(cnt, l) == 3u) flag_line(p.ws, FL_F2 + (rt * 16 + ct1) * FS, epoch, l); };
  auto gate = [&]() {
    const u32x4 gwv = __builtin_bit_cast(u32x4, lds_read16(ldsb + L_GW + l * 16));
    float sum = 0.f;
    #pragma unroll
    for (int i = 0; i < 4; ++i) { sum += rbf(bflo(xv[i]) * bflo(gwv[i])); sum += rbf(bfhi(xv[i]) * bfhi(gwv[i])); }
    sum += __builtin_bit_cast(float, __builtin_amdgcn_update_dpp(0, __builtin_bit_cast(int, sum), 0x111, 0xf, 0xf, true));
    sum += __builtin_bit_cast(float, __builtin_amdgcn_update_dpp(0, __builtin_bit_cast(int, sum), 0x112, 0xf, 0xf, true));
    sum += __builtin_bit_cast(float, __builtin_amdgcn_update_dpp(0, __builtin_bit_cast(int, sum), 0x114, 0xf, 0xf, true));
    sum += __builtin_bit_cast(float, __builtin_amdgcn_update_dpp(0, __builtin_bit_cast(int, sum), 0x118, 0xf, 0xf, true));
    sum += __builtin_bit_cast(float, __builtin_amdgcn_update_dpp(0, __builtin_bit_cast(int, sum), 0x142, 0xa, 0xf, true));
    sum += __builtin_bit_cast(float, __builtin_amdgcn_update_dpp(0, __builtin_bit_cast(int, sum), 0x143, 0xc, 0xf, true));
    const float gg = rbf(__builtin_amdgcn_rcpf(1.0f + __expf(-rbf(sum))));
    st4_wt(__float_as_uint(gg), wsr, l == 63 ? WS_G + (n2 + 256 * gq + 64 * pr4 + 32 * t1) * 4 : OOB); };

  const unsigned vrow = (l >> 3) * 1024 + ((l & 7) ^ (4 * ((l >> 4) & 1))) * 16;
  const unsigned vrowY = (l >> 3) * YROW + ((l & 7) ^ (4 * ((l >> 4) & 1))) * 16;
  #define M2_STAGE(u) (u)
  const unsigned w2dst = __builtin_amdgcn_readfirstlane(ldsb + (w & 1) * 8192), vw2 = vrow + (w & 1) * 896 * 1024 + ct * 128;
  #define M2_ISSUE_W2(u, j) glds(vw2 + (j) * 8192 + M2_STAGE(u) * 65536, p.w2, w2dst + ((u) % NWS) * 16384 + (j) * 1024)
  auto issue_w_prologue = [&]() {
    #pragma unroll
    for (int u = 0; u < NWS; ++u)
      #pragma unroll
      for (int j = 0; j < 8; ++j) M2_ISSUE_W2(u, j); };

  if (has_m1) {
    f32x16 acc1[2] = {};
    const int fw = w & 1, sw = w >> 1;
    unsigned xo[2][4], wo0[2], wo1[2];
    { const int h = l >> 5, blk = (l >> 4) & 1, a = (l & 15) >> 2, pp = l & 3, xrow = 32 * sw + (l & 31);
      #pragma unroll
      for (int hh = 0; hh < 2; ++hh) {
        #pragma unroll
        for (int ks = 0; ks < 4; ++ks) { xo[hh][ks] = ldsb + hh * 65536 + xrow * 128 + 16 * ((2 * ks + h) ^ ((xrow >> 1) & 7)); PIN(xo[hh][ks]); }
        wo0[hh] = ldsb + hh * 65536 + 16384 + (8 * h + a) * 256 + 16 * (((8 * fw + 0) ^ (a << 2)) + 2 * blk + (pp >> 1)) + 8 * (pp & 1); PIN(wo0[hh]);
        wo1[hh] = ldsb + hh * 65536 + 16384 + (8 * h + a) * 256 + 16 * (((8 * fw + 4) ^ (a << 2)) + 2 * blk + (pp >> 1)) + 8 * (pp & 1); PIN(wo1[hh]); } }
    struct Fr { bf16x8 B, A0, A1; } fa, fb;
    #define RB(f, g)  f.B  = lds_read16(xo[(((g) >> 2) & 3) >> 1][(g) & 3] + (((g) >> 2) & 1) * 32768)
    #define RA(base_, g) cat4(tr_read(base_[(((g) >> 2) & 3) >> 1] + (((g) >> 2) & 1) * 32768 + ((g) & 3) * 4096), tr_read(base_[(((g) >> 2) & 3) >> 1] + (((g) >> 2) & 1) * 32768 + ((g) & 3) * 4096 + 1024))
    SB();
    M1_STAGE(2);
    if (p2wave) p2consts();
    WAIT_VM(5);
    BARE_BAR();
    STAMP(5);
    RB(fa, 0); fa.A0 = RA(wo0, 0); fa.A1 = RA(wo1, 0);
    unsigned f1v = 0;
    auto kstep = [&](auto G, Fr& cur, Fr& nxt) {
      constexpr int g = decltype(G)::v, st = g >> 2, ks = g & 3;
      if constexpr (ks == 0 && st > 0) {
        if constexpr (st == 1) { WAIT_VM(4); }
        else if constexpr (st <= 3) { if (w == 0) { WAIT_VM(5); } else { WAIT_VM(4); } }
        else if constexpr (st <= 5) { WAIT_VM(4); }
        else if constexpr (st == 6) { if (w < 4) { WAIT_VM(0); PIN(f1v); if (!__all(f1v == epoch)) spin4(p.ws, FL_F1 + (64 * w + l) * FS, epoch, l); } else { WAIT_VM(2); } }
        BARE_BAR();
        STAMP(20 + st);
        if constexpr (st == 5) { if (w < 4) gload4_sc1(f1v, (64 * w + l) * FS, p.ws + FL_F1); }
      }
      acc1[0] = mfma32(cur.A0, cur.B, acc1[0]); SB();
      if constexpr (g + 1 < 32) { RB(nxt, g + 1); nxt.A0 = RA(wo0, g + 1); }
      SB();
      acc1[1] = mfma32(cur.A1, cur.B, acc1[1]); SB();
      if constexpr (g + 1 < 32) { nxt.A1 = RA(wo1, g + 1); }
      if constexpr (st <= 4) { M1_ISSUE(st + 3, ks); }
      if constexpr (g == 7) { if (w == 0) flag_line(p.ws, FL_F1 + b * FS, epoch, l); }
      if constexpr (g == 24 || g == 25) { if (w < 4) xgather(g & 1); }
      if constexpr (g == 20 || g == 21) { if (w >= 4) glds(l * 16, (const char*)p.w2 + (size_t)((xcd * 28 + jx) * 8 + (w & 3) * 2 + (g & 1)) * 1024, ldsb + L_DUMMY + (w & 3) * 1024); }
      SB();
    };
    #define K2(g) kstep(IC<g>{}, fa, fb); kstep(IC<g + 1>{}, fb, fa);
    K2(0) K2(2) K2(4) K2(6) K2(8) K2(10) K2(12) K2(14) K2(16) K2(18) K2(20) K2(22) K2(24) K2(26) K2(28) K2(30)
    STAMP(6);
    BARE_BAR();
    unsigned fwv = 0;
    if (w < 4) gload4_sc1(fwv, (l & 31) * FS, p.ws + FL_FW);
    STAMP(7);
    u32x4 hf[4];
    #pragma unroll
    for (int ft = 0; ft < 2; ++ft)
      #pragma unroll
      for (int u = 0; u < 2; ++u) {
        #pragma unroll
        for (int jj = 0; jj < 4; ++jj) {
          const f32x2 v = {acc1[ft][8 * u + 2 * jj], acc1[ft][8 * u + 2 * jj + 1]};
          const f32x2 t = v * -1.44269504088896340736f;
          const f32x2 e = (f32x2){__builtin_amdgcn_exp2f(t[0]), __builtin_amdgcn_exp2f(t[1])} + 1.0f;
          const f32x2 r = v * (f32x2){__builtin_amdgcn_rcpf(e[0]), __builtin_amdgcn_rcpf(e[1])};
          hf[2 * ft + u][jj] = pk2(r[0], r[1]);
        }
        if (w >= 4) st16_wt(hf[2 * ft + u], wsr, WS_H + ((((s0 >> 5) + sw) * 112 + ((f0 + 64 * fw + 32 * ft + 16 * u) >> 4)) * 64 + l) * 16);
        else *(LAS u32x4*)(size_t)(ldsb + L_HX + (w * 4 + 2 * ft + u) * 1024 + l * 16) = hf[2 * ft + u];
      }
    SB();
    STAMP(8);
    if (w < 4) { WAIT_LGKM0(); lds_inc(cnt + 28 + 0, l);
      WAIT_VM(0); PIN(fwv); if (!__all(fwv == epoch)) spin4(p.ws, FL_FW + (l & 31) * FS, epoch, l);
      wload(); }
    else {
      lds_spin(cnt + 28, 4u);
      const int pw4 = w - 4, psw = pw4 >> 1, pfw = pw4 & 1;
      #pragma unroll
      for (int i = 0; i < 4; ++i)
        st16_wt(*(const LAS u32x4*)(size_t)(ldsb + L_HX + (pw4 * 4 + i) * 1024 + l * 16), wsr, WS_H + ((((s0 >> 5) + psw) * 112 + ((f0 + 64 * pfw + 16 * i) >> 4)) * 64 + l) * 16);
      WAIT_VM(0);
      h_done();
      if (w < 6) issue_w_prologue();
    }
  } else {
    WAIT_VM(0); BAR();
    if (w == 0) flag_line(p.ws, FL_F1 + b * FS, epoch, l);
    STAMP(18);
    { PIN(er); PIN(ei);
      const int a = (l & 15) >> 2, pp = l & 3;
      #pragma unroll
      for (int rh = 0; rh < 2; ++rh)
        #pragma unroll
        for (int cb = 0; cb < 4; ++cb) { const int row = 16 * rh + 4 * q + a;
          const unsigned ad = wscr + row * 128 + ((cb ^ ((row >> 1) & 3)) * 32) + pp * 8;
          const u32x2 vr = __builtin_bit_cast(u32x2, tr_read(ad)), vi = __builtin_bit_cast(u32x2, tr_read(ad + 4096));
          const u32x4 o = {__builtin_amdgcn_perm(vi[0], vr[0], 0x05040100u), __builtin_amdgcn_perm(vi[0], vr[0], 0x07060302u), __builtin_amdgcn_perm(vi[1], vr[1], 0x05040100u), __builtin_amdgcn_perm(vi[1], vr[1], 0x07060302u)};
          st16_wt(o, wsr, WS_WT + (kq * 64 + (m0 >> 5)) * 8192 + rh * 4096 + cb * 1024 + c * 64 + q * 16); }
      st4_wt(er | (ei << 16), wsr, (kq == 3 && l < 32) ? WS_WT + 256 * 8192 + (m0 + l) * 4 : OOB); }
    if (w == 1 && jx == 28 && l < 32) { __hip_atomic_store((GAS unsigned*)(p.ws + FL_F3 + (xcd * 2 + 0) * FS + l * 4), 0u, __ATOMIC_RELAXED, __HIP_MEMORY_SCOPE_AGENT);
                                        __hip_atomic_store((GAS unsigned*)(p.ws + FL_F3 + (xcd * 2 + 1) * FS + l * 4), 0u, __ATOMIC_RELAXED, __HIP_MEMORY_SCOPE_AGENT); }
    STAMP(19);
    WAIT_VM(0); BAR();
    STAMP(14);
    if (w == 0) flag_line(p.ws, FL_FW + (xcd + 8 * (jx - 28)) * FS, epoch, l);
    if (w >= 4) {
      unsigned long long pc; unsigned rest;
      asm volatile("s_getpc_b64 %0\nfused_pc_label:\n\ts_mov_b32 %1, fused_end_label-fused_pc_label" : "=s"(pc), "=s"(rest));
      const char* code = (const char*)(pc & ~127ull);
      #pragma unroll
      for (int i = 0; i < 6; ++i) { const unsigned o = (unsigned)(i * 4 + (w & 3)) * 1024u; if (o + 1024u <= rest) glds(l * 16, code + o, ldsb + L_DUMMY + (w & 3) * 1024); }
    }
    if (w >= 4) {
      const int zi = (jx - 28) * 4 + (w & 3);
      const char* a = zi < 1 ? (const char*)p.out + (l & 31) * 65536 : (const char*)p.ws + WS_H + (size_t)(((zi - 1) * 64 + l) % 112) * 65536;
      if (zi == 15) a = (const char*)p.ws + WS_Y + (size_t)(l % 33) * 65536;
      asm volatile("s_mov_b32 m0, %1\n\ts_nop 0\n\tglobal_load_lds_dword %0, off" :: "v"(a), "s"(__builtin_amdgcn_readfirstlane(ldsb + L_DUMMY + (w & 3) * 1024)) : "memory");
    }
    if (w < 4) { p2consts(); spin4(p.ws, FL_F1 + (64 * w + l) * FS, epoch, l);
      lds_inc(cnt + 20, l); lds_spin(cnt + 20, 4u);
      xgather(0); xgather(1);
      spin4(p.ws, FL_FW + (l & 31) * FS, epoch, l);
      wload(); }
    else if (w < 6) issue_w_prologue();
    STAMP(8);
  }
  STAMP(9);
  if (p2wave) {
    { const unsigned gc = cnt + 20; lds_inc(gc, l); lds_spin(gc, has_m1 ? 4u : 8u); }
    const unsigned xc = ldsb + L_XCOL, wc = ldsb + L_WCOL, ub = ldsb + L_U;
    bf16x8 Zr[2][2], Zi[2][2];
    #pragma unroll
    for (int u = 0; u < 2; ++u)
      #pragma unroll
      for (int mt = 0; mt < 2; ++mt) { const unsigned ad = xc + ((4 * u + q) * 32 + 16 * mt + c) * 16; Zr[mt][u] = lds_read16(ad); Zi[mt][u] = lds_read16(ad + 4096); }
    const unsigned sgn = (c & 1) ? 0x80008000u : 0u;
    u32x4 gc1 = xor4(gc0, sgn), gs1 = xor4(gs0, sgn), ngs0 = xor4(gs0, 0x80008000u), ngs1 = xor4(gs0, sgn ^ 0x80008000u);
    u32x4 hc1 = xor4(hc0, 0x80000000u), hs1 = xor4(hs0, 0x80000000u), nhs0 = xor4(hs0, 0x80008000u), nhs1 = xor4(hs0, 0x00008000u);
    PIN(gc1); PIN(gs1); PIN(ngs0); PIN(ngs1); PIN(hc1); PIN(hs1); PIN(nhs0); PIN(nhs1);
    const bf16x8 Gc[2] = {as8(gc0), as8(gc1)}, Gs[2] = {as8(gs0), as8(gs1)}, nGs[2] = {as8(ngs0), as8(ngs1)};
    const bf16x8 Hc[2] = {as8(hc0), as8(hc1)}, Hs[2] = {as8(hs0), as8(hs1)}, nHs[2] = {as8(nhs0), as8(nhs1)};
    f32x4 Dr[2] = {}, Di[2] = {};
    #pragma unroll
    for (int u = 0; u < 2; ++u)
      #pragma unroll
      for (int mt = 0; mt < 2; ++mt) {
        Dr[mt] = mfma16(Zr[mt][u], Gc[u], Dr[mt]); Di[mt] = mfma16(Zi[mt][u], Gc[u], Di[mt]);
        Dr[mt] = mfma16(Zi[mt][u], Gs[u], Dr[mt]); Di[mt] = mfma16(Zr[mt][u], nGs[u], Di[mt]);
      }
    SB(); lds_inc(cnt + 24, l); SB();
    u32x4 tr_, ti_;
    { float re[8], im[8];
      #pragma unroll
      for (int i = 0; i < 8; ++i) { const float x = Dr[i >> 2][i & 3], y = Di[i >> 2][i & 3]; re[i] = x * twc[i] + y * tws[i]; im[i] = y * twc[i] - x * tws[i]; }
      #pragma unroll
      for (int jj = 0; jj < 4; ++jj) { tr_[jj] = pk2(re[2 * jj], re[2 * jj + 1]); ti_[jj] = pk2(im[2 * jj], im[2 * jj + 1]); } }
    f32x4 Vr[2] = {}, Vi[2] = {};
    { const bf16x8 Tr = as8(tr_), Ti = as8(ti_);
      #pragma unroll
      for (int t = 0; t < 2; ++t) { Vr[t] = mfma16(Hc[t], Tr, Vr[t]); Vi[t] = mfma16(Hc[t], Ti, Vi[t]); }
      #pragma unroll
      for (int t = 0; t < 2; ++t) { Vr[t] = mfma16(Hs[t], Ti, Vr[t]); Vi[t] = mfma16(nHs[t], Tr, Vi[t]); } }
    WAIT_VM(0);
    #pragma unroll
    for (int i = 0; i < 8; ++i) PIN(Wv[i]);
    if (kcol != 0) { float re[8], im[8];
      #pragma unroll
      for (int i = 0; i < 8; ++i) { const float x = Vr[i >> 2][i & 3], y = Vi[i >> 2][i & 3], wr = bflo(Wv[i]), wi = bfhi(Wv[i]);
        re[i] = x * wr - y * wi; im[i] = x * wi + y * wr; }
      #pragma unroll
      for (int jj = 0; jj < 4; ++jj) { tr_[jj] = pk2(re[2 * jj], re[2 * jj + 1]); ti_[jj] = pk2(im[2 * jj], im[2 * jj + 1]); }
    } else {
      const unsigned vx = ldsb + L_VX, w2c = ldsb + L_W256;
      #pragma unroll
      for (int i = 0; i < 8; ++i) { const int m = 16 * pw + c + 64 * (16 * (i >> 2) + 4 * q + (i & 3)); *(LAS f32x2*)(size_t)(vx + m * 8) = (f32x2){Vr[i >> 2][i & 3], Vi[i >> 2][i & 3]}; }
      WAIT_LGKM0();
      lds_inc(cnt + 8, l); lds_spin(cnt + 8, 4u);
      float re[8], im[8];
      #pragma unroll
      for (int i = 0; i < 8; ++i) { const int m = 16 * pw + c + 64 * (16 * (i >> 2) + 4 * q + (i & 3)), mm = (2048 - m) & 2047;
        const f32x2 zm = *(const LAS f32x2*)(size_t)(vx + mm * 8);
        const unsigned a0 = Wv[i], a1 = lds_read4(wc + mm * 4), b0 = lds_read4(w2c + m * 4), b1 = lds_read4(w2c + mm * 4);
        const float Ar = 0.5f * (bflo(a0) + bflo(a1)), Ai = 0.5f * (bfhi(a0) - bfhi(a1)), Br = 0.5f * (bflo(b0) + bflo(b1)), Bi = 0.5f * (bfhi(b0) - bfhi(b1));
        const float Pr = 0.5f * (Ar + Br), Pi = 0.5f * (Ai + Bi), Qr = 0.5f * (Ar - Br), Qi = 0.5f * (Ai - Bi);
        const float x = Vr[i >> 2][i & 3], y = Vi[i >> 2][i & 3], u = zm[0], v = -zm[1];
        re[i] = x * Pr - y * Pi + u * Qr - v * Qi; im[i] = x * Pi + y * Pr + u * Qi + v * Qr; }
      #pragma unroll
      for (int jj = 0; jj < 4; ++jj) { tr_[jj] = pk2(re[2 * jj], re[2 * jj + 1]); ti_[jj] = pk2(im[2 * jj], im[2 * jj + 1]); }
    }
    f32x4 Ur[2] = {}, Ui[2] = {};
    { const bf16x8 Tr = as8(tr_), Ti = as8(ti_);
      #pragma unroll
      for (int t = 0; t < 2; ++t) { Ur[t] = mfma16(Hc[t], Tr, Ur[t]); Ui[t] = mfma16(Hc[t], Ti, Ui[t]); }
      #pragma unroll
      for (int t = 0; t < 2; ++t) { Ur[t] = mfma16(nHs[t], Ti, Ur[t]); Ui[t] = mfma16(Hs[t], Tr, Ui[t]); } }
    #pragma unroll
    for (int t = 0; t < 2; ++t) { float re[4], im[4];
      #pragma unroll
      for (int r = 0; r < 4; ++r) { const float x = Ur[t][r], y = Ui[t][r]; re[r] = x * twc[4 * t + r] - y * tws[4 * t + r]; im[r] = y * twc[4 * t + r] + x * tws[4 * t + r]; }
      char LAS* dst = (char LAS*)(size_t)ub + (16 * pw + c) * 72 + (16 * t + 4 * q) * 2;
      *(LAS u32x2*)dst = (u32x2){pk2(re[0], re[1]), pk2(re[2], re[3])};
      *(LAS u32x2*)(dst + 64 * 72) = (u32x2){pk2(im[0], im[1]), pk2(im[2], im[3])}; }
    WAIT_LGKM0();
    { lds_inc(cnt + 4, l); lds_spin(cnt + 4, 4u); }
    STAMP(10);
    f32x4 yr[2] = {}, yi[2] = {};
    const int a = (l & 15) >> 2, pp = l & 3;
    #pragma unroll
    for (int u = 0; u < 2; ++u)
      #pragma unroll
      for (int t = 0; t < 2; ++t) {
        const unsigned ad = ub + (32 * u + 8 * q + a) * 72 + (8 * pp + 4 * t) * 2;
        const bf16x8 Ar = cat4(tr_read(ad), tr_read(ad + 4 * 72));
        const bf16x8 Ai = cat4(tr_read(ad + 64 * 72), tr_read(ad + 64 * 72 + 4 * 72));
        yr[t] = mfma16(Ar, Gc[u], yr[t]); yi[t] = mfma16(Ai, Gc[u], yi[t]);
        yr[t] = mfma16(Ai, nGs[u], yr[t]); yi[t] = mfma16(Ar, Gs[u], yi[t]);
      }
    const float ysc = kcol == 0 ? 0.5f : 1.0f;
    const u32x4 yre = {pk2(yr[0][0] * ysc, yr[0][1] * ysc), pk2(yr[0][2] * ysc, yr[0][3] * ysc), pk2(yr[1][0] * ysc, yr[1][1] * ysc), pk2(yr[1][2] * ysc, yr[1][3] * ysc)};
    const u32x4 yim = {pk2(yi[0][0] * ysc, yi[0][1] * ysc), pk2(yi[0][2] * ysc, yi[0][3] * ysc), pk2(yi[1][0] * ysc, yi[1][1] * ysc), pk2(yi[1][2] * ysc, yi[1][3] * ysc)};
    const unsigned so = (32 * (16 * pw + c) + 8 * q) * 2;
    st16_wt(yre, wsr, WS_Y + kcol * YROW + so);
    st16_wt(yim, wsr, WS_Y + (256 + kcol) * YROW + so);
    gate();
    STAMP(11);
  }
  if (!p2wave) gate();
  unsigned vst[4];
  #pragma unroll
  for (int i = 0; i < 4; ++i)
    vst[i] = w < 4 ? vrow + ((i >> 1) * 896 + (w & 3) * 16 + (i & 1) * 8) * 1024 + ct * 128
                   : l * 16 + ((2 * pr + ((w & 3) >> 1)) * 112 + (i >> 1) * 56 + 2 * (w & 1) + (i & 1)) * 1024;
  const char* stbase = w < 4 ? (const char*)p.w2 : p.ws + WS_H;
  const unsigned stmul = w < 4 ? 65536u : 4096u;
  const unsigned stdst = __builtin_amdgcn_readfirstlane(ldsb + (w < 4 ? 0u : L_HRING) + (w & 3) * 2048);
  #define M2_ISSUE(u, i) glds_sc1(vst[i] + M2_STAGE(u) * stmul, stbase, stdst + ((u) % 5) * 16384 + ((i) >> 1) * 8192 + ((i) & 1) * 1024)
  const unsigned vy8 = vrowY + ((w >> 2) * 256 + (w & 3) * 16) * YROW + pr * 128;
  const unsigned y8dst = __builtin_amdgcn_readfirstlane(ldsb + L_HRING + (w >> 2) * 8192 + (w & 3) * 2048);
  #define M2_ISSUE_Y(u, i) glds_sc1(vy8 + (((u) - 14) * 64 + (i) * 8) * YROW, p.ws + WS_Y, y8dst + ((u) % 5) * 16384 + (i) * 1024)
  if (w >= 4) {
    if (w == 6) { spin4(p.ws, FL_F2 + ((pr >> 1) * 16 + (l < 14 ? l : 0)) * FS, epoch, l); lds_inc(cnt + 16, l); }
    lds_spin(cnt + 16, 1u);
    if (has_m1) lds_spin(cnt, 4u);
    if (b == 0) lds_spin(cnt + 4, 4u);
    STAMP(12);
    #pragma unroll
    for (int u = 0; u < 3; ++u) { M2_ISSUE(u, 0); M2_ISSUE(u, 1); M2_ISSUE(u, 2); M2_ISSUE(u, 3); }
    lds_spin(cnt + 24, 4u);
    M2_ISSUE(3, 0); M2_ISSUE(3, 1); M2_ISSUE(3, 2); M2_ISSUE(3, 3);
    WAIT_VM(12);
  } else { WAIT_VM(0); }
  BAR();
  if (tid == 0) __hip_atomic_fetch_add((GAS unsigned*)(p.ws + FL_F3 + (b & 15) * FS), 1u, __ATOMIC_RELAXED, __HIP_MEMORY_SCOPE_AGENT);
  if (w >= 4) { M2_ISSUE(4, 0); M2_ISSUE(4, 1); M2_ISSUE(4, 2); M2_ISSUE(4, 3); }
  STAMP(13);
  f32x16 accb = {}, acca = {};
  {
    const int kh = w >> 2, nh = (w >> 1) & 1, sh = w & 1;
    const int h = l >> 5, blk = (l >> 4) & 1, a = (l & 15) >> 2, pp = l & 3;
    unsigned aoff[2];
    #pragma unroll
    for (int hh = 0; hh < 2; ++hh) { aoff[hh] = ldsb + hh * 65536 + kh * 8192 + (4 * h + a) * 128 + 16 * ((4 * nh + 2 * (pp & 1) + blk) ^ (4 * (a >> 1))) + 8 * (pp >> 1); PIN(aoff[hh]); }
    unsigned boffH[2], boffY[2];
    #pragma unroll
    for (int hh = 0; hh < 2; ++hh) { boffH[hh] = ldsb + L_HRING + hh * 49152 + kh * 8192 + sh * 4096 + l * 16; PIN(boffH[hh]);
      boffY[hh] = ldsb + L_HRING + hh * 49152 + kh * 8192 + (4 * h + a) * 128 + 16 * ((4 * sh + 2 * blk + (pp >> 1)) ^ (4 * (a >> 1))) + 8 * (pp & 1); PIN(boffY[hh]); }
    const int i31 = l & 31, dn = 64 * ct + 32 * nh + 16 * ((i31 >> 2) & 1) + 4 * (i31 >> 3) + (i31 & 3);
    const float dsc = (kh ? -1.0f : 1.0f) * (float)dn * (1.0f / 512.0f);
    const float cbase = (float)(4 * h) * dsc + (kh ? 0.0f : 0.25f);
    const float c256 = (dn & 1) ? -1.0f : 1.0f;
    u32x4 Cf[16];
    f32x2 cprev[4], ccur[4];
    const float two_c = 2.0f * cos_rev((float)(dn & 31) * (1.0f / 32.0f));
    auto cgen = [&](auto KS) { constexpr int ks = decltype(KS)::v;
      #pragma unroll
      for (int jj = 0; jj < 4; ++jj) { f32x2 v;
        if constexpr (ks < 2) {
          #pragma unroll
          for (int e = 0; e < 2; ++e) { const int j = 2 * jj + e; v[e] = sin_rev(__builtin_fmaf((float)(16 * ks + 8 * (j >> 2) + (j & 3)), dsc, cbase)); }
        } else v = ccur[jj] * two_c - cprev[jj];
        if constexpr (ks > 0) cprev[jj] = ccur[jj];
        ccur[jj] = v;
        if (ks == 0 && jj == 0) v[0] = (kh && h == 0) ? c256 : v[0];
        Cf[ks][jj] = pk2(v[0], v[1]); }
      PIN(Cf[ks]); };
    struct F2 { bf16x8 A[4], B[4]; } fa, fb;
    auto rdA = [&](auto ST, auto KS, F2& f) { constexpr int st = decltype(ST)::v, ks = decltype(KS)::v;
      if constexpr (st < 14) { constexpr unsigned o = (st % NWS) * 16384 + ks * 2048; f.A[ks] = cat4(tr_read(aoff[o >> 16] + (o & 65535)), tr_read(aoff[o >> 16] + (o & 65535) + 1024)); } };
    auto rdB = [&](auto ST, auto KS, F2& f) { constexpr int st = decltype(ST)::v, ks = decltype(KS)::v; constexpr int sl = st % NHS, hi = sl >= 3; constexpr unsigned o = (sl - 3 * hi) * 16384;
      if constexpr (st < 14) f.B[ks] = lds_read16(boffH[hi] + o + ks * 1024);
      else f.B[ks] = cat4(tr_read(boffY[hi] + o + ks * 2048), tr_read(boffY[hi] + o + ks * 2048 + 1024)); };
    rdA(IC<0>{}, IC<0>{}, fa); rdB(IC<0>{}, IC<0>{}, fa); rdA(IC<0>{}, IC<1>{}, fa); rdB(IC<0>{}, IC<1>{}, fa);
    rdA(IC<0>{}, IC<2>{}, fa); rdB(IC<0>{}, IC<2>{}, fa); rdA(IC<0>{}, IC<3>{}, fa); rdB(IC<0>{}, IC<3>{}, fa);
    unsigned f3v = 0;
    unsigned gv = 0;
    auto step = [&](auto ST, F2& cur, F2& nxt) {
      constexpr int st = decltype(ST)::v;
      if constexpr (st <= 8) { WAIT_VM(12); }
      else if constexpr (st == 9) { if (w < 4) { WAIT_VM(8); if (w == 0) { PIN(f3v); if (!__all(f3v == 16u)) spin4(p.ws, FL_F3 + (l & 15) * FS, 16u, l); } } else { WAIT_VM(12); } }
      else if constexpr (st == 10) { WAIT_VM(10); } else if constexpr (st == 11) { WAIT_VM(9); } else if constexpr (st <= 13) { WAIT_VM(7); }
      else if constexpr (st == 14) { WAIT_VM(4); } else if constexpr (st == 15) { WAIT_VM(0); }
      if constexpr (st <= 15) BAR();
      STAMP(30 + st);
      if constexpr (st == 7) { if (w == 0) gload4_sc1(f3v, (l & 15) * FS, p.ws + FL_F3); }
      if constexpr (st == 10) gload4_sc1(gv, (64 * pr + 32 * sh + (l & 31)) * 4, p.ws + WS_G);
      auto mm = [&](auto KS) { constexpr int ks = decltype(KS)::v;
        if constexpr (st < 14) accb = mfma32(cur.A[ks], cur.B[ks], accb); else acca = mfma32(as8(Cf[4 * (st - 14) + ks]), cur.B[ks], acca); };
      auto iss = [&](auto KS) { constexpr int ks = decltype(KS)::v;
        if constexpr (st + 5 < 14) { M2_ISSUE(st + 5, ks); }
        else if constexpr (st + 5 < 18 && ks >= 1 && ks <= 2) { M2_ISSUE_Y(st + 5, ks - 1); } };
      mm(IC<0>{}); SB();
      if constexpr (st < 17) { rdA(IC<st + 1>{}, IC<0>{}, nxt); rdB(IC<st + 1>{}, IC<0>{}, nxt); rdA(IC<st + 1>{}, IC<1>{}, nxt); rdB(IC<st + 1>{}, IC<1>{}, nxt); }
      iss(IC<0>{});
      SB();
      mm(IC<1>{}); SB();
      if constexpr (st < 17) { rdA(IC<st + 1>{}, IC<2>{}, nxt); rdB(IC<st + 1>{}, IC<2>{}, nxt); rdA(IC<st + 1>{}, IC<3>{}, nxt); rdB(IC<st + 1>{}, IC<3>{}, nxt); }
      iss(IC<1>{});
      SB();
      mm(IC<2>{}); SB();
      iss(IC<2>{});
      SB();
      mm(IC<3>{}); SB();
      iss(IC<3>{});
      if constexpr (st < 16) cgen(IC<(st < 16 ? st : 0)>{});
      SB();
    };
    step(IC<0>{}, fa, fb); step(IC<1>{}, fb, fa); step(IC<2>{}, fa, fb); step(IC<3>{}, fb, fa); step(IC<4>{}, fa, fb); step(IC<5>{}, fb, fa);
    step(IC<6>{}, fa, fb); step(IC<7>{}, fb, fa); step(IC<8>{}, fa, fb); step(IC<9>{}, fb, fa); step(IC<10>{}, fa, fb); step(IC<11>{}, fb, fa);
    step(IC<12>{}, fa, fb); step(IC<13>{}, fb, fa); step(IC<14>{}, fa, fb); step(IC<15>{}, fb, fa); step(IC<16>{}, fa, fb); step(IC<17>{}, fb, fa);
    STAMP(15);
    PIN(gv);
    if (b == 0 && w == 0) flag_line(p.ws, FL_E, epoch, l);
    LAS f32x4* ex = (LAS f32x4*)lds;
    const float g = __uint_as_float(gv), omg = rbf(1.0f - g);
    char* orow = (char*)p.out + (size_t)(64 * pr + 32 * sh + (l & 31)) * 1024 + (64 * ct + 32 * nh + 16 * h) * 2;
    auto tail = [&](auto KH) __attribute__((always_inline)) { constexpr int o = 8 * (1 - decltype(KH)::v), m = 8 * decltype(KH)::v;
      ex[(w * 4 + 0) * 64 + l] = (f32x4){accb[o], accb[o + 1], accb[o + 2], accb[o + 3]};
      ex[(w * 4 + 1) * 64 + l] = (f32x4){accb[o + 4], accb[o + 5], accb[o + 6], accb[o + 7]};
      ex[(w * 4 + 2) * 64 + l] = (f32x4){acca[o], acca[o + 1], acca[o + 2], acca[o + 3]};
      ex[(w * 4 + 3) * 64 + l] = (f32x4){acca[o + 4], acca[o + 5], acca[o + 6], acca[o + 7]};
      BAR();
      const int pwv = w ^ 4;
      const f32x4 b0 = ex[(pwv * 4 + 0) * 64 + l], b1 = ex[(pwv * 4 + 1) * 64 + l], a0 = ex[(pwv * 4 + 2) * 64 + l], a1 = ex[(pwv * 4 + 3) * 64 + l];
      constexpr float ASC = 2.0f / 1048576.0f;
      unsigned o4[4];
      #pragma unroll
      for (int i = 0; i < 4; ++i) {
        const float a_0 = acca[m + 2 * i] + (i < 2 ? a0[2 * i] : a1[2 * i - 4]), a_1 = acca[m + 2 * i + 1] + (i < 2 ? a0[2 * i + 1] : a1[2 * i - 3]);
        const float b_0 = accb[m + 2 * i] + (i < 2 ? b0[2 * i] : b1[2 * i - 4]), b_1 = accb[m + 2 * i + 1] + (i < 2 ? b0[2 * i + 1] : b1[2 * i - 3]);
        const unsigned ab = pk2(a_0 * ASC, a_1 * ASC), bb = pk2(b_0, b_1);
        const unsigned t1_ = pk2(g * bflo(ab), g * bfhi(ab)), t2_ = pk2(omg * bflo(bb), omg * bfhi(bb));
        o4[i] = pk2(bflo(t1_) + bflo(t2_), bfhi(t1_) + bfhi(t2_)); }
      *(GAS u32x4*)(orow + m * 2) = (u32x4){o4[0], o4[1], o4[2], o4[3]}; };
    if (kh) tail(IC<1>{}); else tail(IC<0>{});
  }
  STAMP(16);
  STAMP_FLUSH();
  asm volatile("fused_end_label:");
}

extern "C" void kernel_launch(void* const* d_in, const int* in_sizes, int n_in, void* d_out, int out_size, void* d_ws, size_t ws_size, hipStream_t stream) {
  Params p;
  p.ax = (const uint16_t*)d_in[0]; p.wr = (const uint16_t*)d_in[1]; p.wi = (const uint16_t*)d_in[2];
  p.w1 = (const uint16_t*)d_in[4]; p.w2 = (const uint16_t*)d_in[5]; p.gw = (const uint16_t*)d_in[6];
  p.out = (uint16_t*)d_out; p.ws = (char*)d_ws;
  (void)hipFuncSetAttribute((const void*)fused_kernel, hipFuncAttributeMaxDynamicSharedMemorySize, 163840);
  fused_kernel<<<256, 512, 163840, stream>>>(p);
}
```

```cpp
#include <hip/hip_runtime.h>
#include <hip/hip_bf16.h>
#include <stdint.h>

typedef short    bf16x8 __attribute__((ext_vector_type(8)));
typedef short    s16x4  __attribute__((ext_vector_type(4)));
typedef float    f32x2  __attribute__((ext_vector_type(2)));
typedef float    f32x4  __attribute__((ext_vector_type(4)));
typedef float    f32x16 __attribute__((ext_vector_type(16)));
typedef unsigned u32x2  __attribute__((ext_vector_type(2)));
typedef unsigned u32x4  __attribute__((ext_vector_type(4)));
typedef __bf16   bf16x2_t __attribute__((ext_vector_type(2)));
#define LAS __attribute__((address_space(3)))
#define GAS __attribute__((address_space(1)))
#define DI __device__ __forceinline__

constexpr int F_ = 1792, DF = 257;

constexpr unsigned XJ_STRIDE = 260 * 32;
constexpr unsigned FS = 128;
constexpr unsigned FL_F1 = 0, FL_F3 = 256 * FS, FL_F2 = 512 * FS, FL_E = 768 * FS, FL_ERR = 769 * FS, FL_FW = 772 * FS;
constexpr unsigned WS_X  = 804 * FS;
constexpr unsigned WS_WT = WS_X + 256 * XJ_STRIDE;
constexpr unsigned YROW = 4096 + 128;
constexpr unsigned WS_Y  = WS_WT + 257 * 8192;
constexpr unsigned WS_G  = WS_Y + 512 * YROW;
constexpr unsigned WS_H  = WS_G + 8192;
constexpr unsigned WS_END = WS_H + 64 * 112 * 1024;

struct Params {
  const uint16_t *ax, *wr, *wi, *w1, *w2, *gw;
  uint16_t* out;
  char* ws;
};

DI unsigned pk2(float lo, float hi) { return __builtin_bit_cast(unsigned, __builtin_convertvector((f32x2){lo, hi}, bf16x2_t)); }
DI float bflo(unsigned u) { return __uint_as_float(u << 16); }
DI float bfhi(unsigned u) { return __uint_as_float(u & 0xffff0000u); }
DI float rbf(float x) { return bflo(pk2(x, x)); }
DI float sin_rev(float x) { return __builtin_amdgcn_sinf(x); }
DI float cos_rev(float x) { return __builtin_amdgcn_cosf(x); }
DI f32x4 mfma16(bf16x8 a, bf16x8 b, f32x4 c) { return __builtin_amdgcn_mfma_f32_16x16x32_bf16(a, b, c, 0, 0, 0); }
DI f32x16 mfma32(bf16x8 a, bf16x8 b, f32x16 c) { return __builtin_amdgcn_mfma_f32_32x32x16_bf16(a, b, c, 0, 0, 0); }
DI bf16x8 mk8(unsigned a, unsigned b, unsigned c, unsigned d) { return __builtin_bit_cast(bf16x8, (u32x4){a, b, c, d}); }
DI bf16x8 as8(u32x4 v) { return __builtin_bit_cast(bf16x8, v); }
DI u32x4 xor4(u32x4 v, unsigned m) { v ^= m; return v; }
DI bf16x8 neg8(bf16x8 v) { return as8(xor4(__builtin_bit_cast(u32x4, v), 0x80008000u)); }
DI s16x4 tr_read(unsigned addr) { return __builtin_amdgcn_ds_read_tr16_b64_v4i16((LAS s16x4*)addr); }
DI bf16x8 lds_read16(unsigned addr) { return *(const LAS bf16x8*)addr; }
DI unsigned lds_read4(unsigned addr) { return *(const LAS unsigned*)addr; }
DI unsigned lds_read2(unsigned addr) { return *(const LAS unsigned short*)addr; }
DI bf16x8 cat4(s16x4 lo, s16x4 hi) { return __builtin_shufflevector(lo, hi, 0, 1, 2, 3, 4, 5, 6, 7); }
#define WAIT_VM(n) asm volatile("s_waitcnt vmcnt(%0)" :: "n"(n) : "memory")
#define WAIT_LGKM0() asm volatile("s_waitcnt lgkmcnt(0)" ::: "memory")
#define SB() __builtin_amdgcn_sched_barrier(0)
#define BARE_BAR() do { SB(); asm volatile("" ::: "memory"); __builtin_amdgcn_s_barrier(); asm volatile("" ::: "memory"); SB(); } while (0)
#define BAR() do { SB(); WAIT_LGKM0(); __builtin_amdgcn_s_barrier(); asm volatile("" ::: "memory"); SB(); } while (0)
#define PIN(x) asm volatile("" : "+v"(x))

DI void glds(unsigned voff, const void* sbase, unsigned dst) {
  asm volatile("s_mov_b32 m0, %2\n\ts_nop 0\n\tglobal_load_lds_dwordx4 %0, %1" :: "v"(voff), "s"(sbase), "s"(dst) : "memory");
}
DI void glds_sc1(unsigned voff, const void* sbase, unsigned dst) {
  asm volatile("s_mov_b32 m0, %2\n\ts_nop 0\n\tglobal_load_lds_dwordx4 %0, %1 sc1" :: "v"(voff), "s"(sbase), "s"(dst) : "memory");
}
DI void gload16(u32x4& d, unsigned voff, const void* sbase) { asm volatile("global_load_dwordx4 %0, %1, %2" : "=v"(d) : "v"(voff), "s"(sbase) : "memory"); }
DI void gload16_sc1(u32x4& d, unsigned voff, const void* sbase) { asm volatile("global_load_dwordx4 %0, %1, %2 sc1" : "=v"(d) : "v"(voff), "s"(sbase) : "memory"); }
DI void gload4_sc1(unsigned& d, unsigned voff, const void* sbase) { asm volatile("global_load_dword %0, %1, %2 sc1" : "=v"(d) : "v"(voff), "s"(sbase) : "memory"); }
typedef __amdgpu_buffer_rsrc_t rsrc_t;
DI void st16_wt(u32x4 v, rsrc_t r, unsigned off) { __builtin_amdgcn_raw_buffer_store_b128(v, r, off, 0, 16); }
DI void st4_wt(unsigned v, rsrc_t r, unsigned off) { __builtin_amdgcn_raw_buffer_store_b32(v, r, off, 0, 16); }
constexpr unsigned OOB = 0x80000000u;
DI void flag_store(char* ws, unsigned off, unsigned epoch) { __hip_atomic_store((GAS unsigned*)(ws + off), epoch, __ATOMIC_RELAXED, __HIP_MEMORY_SCOPE_AGENT); }
DI void flag_line(char* ws, unsigned off, unsigned epoch, int l) { if (l < 32) __hip_atomic_store((GAS unsigned*)(ws + off + l * 4), epoch, __ATOMIC_RELAXED, __HIP_MEMORY_SCOPE_AGENT); }

#define STAMP(i) do {} while (0)
#define STAMP_DECL
#define STAMP_FLUSH() do {} while (0)

struct Poll { unsigned a, b, c, d; };
DI void poll_issue(Poll& v, unsigned voff, const char* ws) { gload4_sc1(v.a, voff, ws); gload4_sc1(v.b, voff, ws + 64 * FS); gload4_sc1(v.c, voff, ws + 128 * FS); gload4_sc1(v.d, voff, ws + 192 * FS); }
DI void poll_pin(Poll& v) { asm volatile("" : "+v"(v.a), "+v"(v.b), "+v"(v.c), "+v"(v.d)); }
DI void spin_poll(Poll& v, const char* base, char* ws, unsigned epoch, int l) {
  if (__all(v.a == epoch && v.b == epoch && v.c == epoch && v.d == epoch)) return;
  const unsigned long long t0 = __builtin_amdgcn_s_memrealtime();
  for (;;) {
    __builtin_amdgcn_s_sleep(1);
    poll_issue(v, l * FS, base); WAIT_VM(0); poll_pin(v);
    if (__all(v.a == epoch && v.b == epoch && v.c == epoch && v.d == epoch)) break;
    if (__builtin_amdgcn_s_memrealtime() - t0 > 2000000ull) { if (l == 0) flag_store(ws, FL_ERR, 1u); break; }
  }
}
DI void spin4(char* ws, unsigned voff, unsigned epoch, int l) {
  const unsigned long long t0 = __builtin_amdgcn_s_memrealtime();
  for (;;) {
    unsigned v; gload4_sc1(v, voff, ws); WAIT_VM(0); PIN(v);
    if (__all(v == epoch)) break;
    __builtin_amdgcn_s_sleep(1);
    if (__builtin_amdgcn_s_memrealtime() - t0 > 2000000ull) { if (l == 0) flag_store(ws, FL_ERR, 1u); break; }
  }
}

constexpr unsigned L_AUX   = 131072;
constexpr unsigned L_XROWS = L_AUX;
constexpr unsigned L_WSCR  = 32768;
constexpr unsigned L_XCOL  = L_AUX;
constexpr unsigned L_WCOL  = L_AUX + 8192;
constexpr unsigned L_U     = L_AUX + 16384;
constexpr unsigned L_GW    = L_AUX + 25600;
constexpr unsigned L_HX    = 114688;
constexpr unsigned L_DUMMY = L_AUX + 26624;
constexpr unsigned L_CNT   = 163840 - 32;
constexpr unsigned L_VX    = 81920;
constexpr unsigned L_W256  = 81920 + 16384;
constexpr unsigned L_HRING = 81920;
constexpr int NWS = 5, NHS = 5;

template <int N> struct IC { static constexpr int v = N; };
DI unsigned lds_inc(unsigned addr, int l) {
  unsigned old = 0;
  if (l == 0) asm volatile("ds_add_rtn_u32 %0, %1, %2\n\ts_waitcnt lgkmcnt(0)" : "=&v"(old) : "v"(addr), "v"(1u) : "memory");
  return __builtin_amdgcn_readfirstlane(old);
}
DI void lds_spin(unsigned addr, unsigned n) { while (__builtin_amdgcn_readfirstlane(__hip_atomic_load((LAS unsigned*)addr, __ATOMIC_RELAXED, __HIP_MEMORY_SCOPE_WORKGROUP)) < n) __builtin_amdgcn_s_sleep(1); asm volatile("" ::: "memory"); }

__global__ __launch_bounds__(512, 2) void fused_kernel(Params p) {
  extern __shared__ __attribute__((aligned(16))) char smem[];
  const unsigned ldsb = (unsigned)(size_t)(char LAS*)smem;
  char LAS* lds = (char LAS*)smem;
  const int b = blockIdx.x, tid = threadIdx.x;
  const int w = __builtin_amdgcn_readfirstlane(tid >> 6), l = tid & 63;
  const int xcd = b & 7, jx = b >> 3;
  const bool has_m1 = jx < 28;
  const int rt = 4 * (xcd >> 1) + jx / 7, ct1 = 7 * (xcd & 1) + jx % 7;
  const int s0 = 128 * rt, f0 = 128 * ct1;
  const int c = l & 15, q = l >> 4;
  STAMP_DECL
  STAMP(0);

  const int n2 = jx, gq = xcd, pj = xcd * 32 + jx, pr4 = w & 3, t1 = w >> 2;
  const unsigned xrw = __builtin_amdgcn_readfirstlane(ldsb + L_XROWS + w * 2048);
  { const char* xr = (const char*)p.ax + (size_t)(n2 + 256 * gq + 64 * pr4) * 1024;
    glds(l * 16, xr, xrw); glds(l * 16, xr + 32 * 1024, xrw + 1024); }
  if (w == 7) glds(l * 16, p.gw, ldsb + L_GW);
  if (w == 6) asm volatile("s_mov_b32 m0, %1\n\ts_mov_b64 exec, 1\n\tglobal_load_lds_dword %0, %2 sc1\n\ts_mov_b64 exec, -1" :: "v"(FL_E), "s"(ldsb + L_CNT + 12), "s"(p.ws) : "memory");
  unsigned vx[2], vw[2];
  #pragma unroll
  for (int i = 0; i < 2; ++i) { const int row = 8 * (2 * w + i) + (l >> 3); vx[i] = row * 1024 + ((l & 7) ^ ((row >> 1) & 7)) * 16;
    const int r2 = 4 * (2 * w + i) + (l >> 4); vw[i] = r2 * (F_ * 2) + ((l & 15) ^ ((r2 & 3) << 2)) * 16; }
  const char* xbase = (const char*)p.ax + (size_t)s0 * 1024;
  const char* w1base = (const char*)p.w1 + f0 * 2;
  const unsigned rot1 = (jx % 7 + 2 * (jx / 7)) & 7;
  const unsigned m1dst = __builtin_amdgcn_readfirstlane(ldsb + w * 2048);
  #define M1_ISSUE(s, i) do { const unsigned ks_ = ((s) + rot1) & 7; \
      if ((i) < 2) glds(vx[(i) & 1] + ks_ * 128, xbase, m1dst + ((s) & 3) * 32768 + ((i) & 1) * 1024); \
      else glds(vw[(i) & 1] + ks_ * (64 * F_ * 2), w1base, m1dst + ((s) & 3) * 32768 + 16384 + ((i) & 1) * 1024); } while (0)
  #define M1_STAGE(s) do { M1_ISSUE(s, 0); M1_ISSUE(s, 2); M1_ISSUE(s, 1); M1_ISSUE(s, 3); } while (0)
  const int wsl = ((xcd + 8 * (jx - 28)) * 8 + w) & 255, m0 = 32 * (wsl >> 2), kq = wsl & 3;
  const unsigned wscr = __builtin_amdgcn_readfirstlane(ldsb + L_WSCR + w * 8192);
  unsigned er = 0, ei = 0;
  if (!has_m1 && kq == 3) { const unsigned vo = (m0 + (l & 31)) * (DF * 2) + 512;
    asm volatile("global_load_ushort %0, %2, %3\n\tglobal_load_ushort %1, %2, %4" : "=&v"(er), "=&v"(ei) : "v"(vo), "s"(p.wr), "s"(p.wi) : "memory"); }
  const rsrc_t wsr = __builtin_amdgcn_make_buffer_rsrc(p.ws, 0, WS_END, 0x00020000);
  if (tid < 8 && tid != 3) { *(LAS unsigned*)(lds + L_CNT + tid * 4) = 0u; }
  bf16x8 Fc, Fs, Fns, Are, Aim; float p1c[4], p1s[4];
  { const float kf = (float)(16 * t1 + c), k32 = kf * (1.0f / 32.0f), q4 = (float)(4 * q);
    u32x4 uc, us, ur, ui;
    auto wl = [&](int i) { const int row = 8 * (i & 3) + (l >> 3), ch = (l & 7) ^ (((row >> 1) & 3) << 1);
      glds((m0 + row) * (DF * 2) + 128 * kq + ch * 16, i < 4 ? (const char*)p.wr : (const char*)p.wi, wscr + i * 1024); };
    #pragma unroll
    for (int jj = 0; jj < 4; ++jj) {
      SB(); if (has_m1) M1_ISSUE(0, (jj == 1 ? 2 : jj == 2 ? 1 : jj)); else { wl(2 * jj); wl(2 * jj + 1); } SB();
      float cv[2], sv[2];
      #pragma unroll
      for (int e = 0; e < 2; ++e) { const int j = 2 * jj + e; const float ang = ((float)(j < 4 ? j : 12 + j) + q4) * k32; cv[e] = cos_rev(ang); sv[e] = sin_rev(ang); }
      uc[jj] = pk2(cv[0], cv[1]); us[jj] = pk2(sv[0], sv[1]); PIN(uc[jj]); PIN(us[jj]); }
    u32x4 un = xor4(us, 0x80008000u); PIN(un);
    Fc = as8(uc); Fs = as8(us); Fns = as8(un);
    #pragma unroll
    for (int r = 0; r < 4; ++r) {
      if (r < 2) { SB(); if (has_m1) M1_ISSUE(1, 2 + r); SB(); }
      const float nr = q4 + (float)r; const float ang = nr * ((float)c * (1.0f / 16.0f)); const float cv = cos_rev(ang), sv = sin_rev(ang);
      ur[r] = pk2(cv, sv); ui[r] = pk2(-sv, cv);
      const float a2 = nr * (kf * (1.0f / 512.0f)); p1c[r] = 0.5f * cos_rev(a2); p1s[r] = 0.5f * sin_rev(a2); PIN(ur[r]); PIN(ui[r]); PIN(p1c[r]); PIN(p1s[r]); }
    Are = as8(ur); Aim = as8(ui); }
  STAMP(1);
  if (has_m1) { WAIT_VM(6); } else { WAIT_VM(8); }
  STAMP(2);
  u32x4 xv;
  { const unsigned xa = xrw + l * 8, xb = xa + 1024;
    const bf16x8 A_a = cat4(tr_read(xa), tr_read(xa + 512)), A_b = cat4(tr_read(xb), tr_read(xb + 512));
    xv = __builtin_bit_cast(u32x4, lds_read16(xrw + t1 * 1024 + l * 16));
    f32x4 Tr = {0, 0, 0, 0}, Ti = {0, 0, 0, 0};
    Tr = mfma16(A_a, Fc, Tr); Ti = mfma16(A_a, Fns, Ti);
    Tr = mfma16(A_b, Fs, Tr); Ti = mfma16(A_b, Fc, Ti);
    PIN(xv); SB();
    unsigned tb[4];
    #pragma unroll
    for (int r = 0; r < 4; ++r) { const float re = Tr[r] * p1c[r] + Ti[r] * p1s[r], im = Ti[r] * p1c[r] - Tr[r] * p1s[r]; tb[r] = pk2(re, im); }
    const bf16x8 Bt = mk8(tb[0], tb[1], tb[2], tb[3]);
    f32x4 Zr = {0, 0, 0, 0}, Zi = {0, 0, 0, 0};
    Zr = mfma16(Are, Bt, Zr); Zi = mfma16(Aim, Bt, Zi);
    #pragma unroll
    for (int r = 0; r < 4; ++r) *(LAS f32x2*)(size_t)(xrw + ((4 * q + r) * 16 + c) * 8) = (f32x2){Zr[r], Zi[r]};
    BAR();
    STAMP(3);
    if (has_m1) { M1_ISSUE(1, 0); M1_ISSUE(1, 1); }
    {
      const bool sp = tid == 1;
      const int k = tid >> 1, c2 = tid & 1, k1 = sp ? 256 : k, k2 = sp ? 256 : (512 - k) & 511; f32x2 z1[4], z2[4];
      const unsigned a1 = ldsb + L_XROWS + ((k1 >> 4) & 1) * 8192 + ((k1 >> 5) * 16 + (k1 & 15)) * 8, a2 = ldsb + L_XROWS + ((k2 >> 4) & 1) * 8192 + ((k2 >> 5) * 16 + (k2 & 15)) * 8;
      #pragma unroll
      for (int pp = 0; pp < 4; ++pp) { z1[pp] = *(const LAS f32x2*)(size_t)(a1 + pp * 2048); z2[pp] = *(const LAS f32x2*)(size_t)(a2 + pp * 2048); }
      unsigned o[4];
      #pragma unroll
      for (int pp = 0; pp < 4; ++pp) o[pp] = (c2 == 0 || sp) ? pk2(z1[pp][0] + z2[pp][0], z1[pp][1] + z2[pp][1]) : pk2(z1[pp][1] - z2[pp][1], z2[pp][0] - z1[pp][0]);
      st16_wt((u32x4){o[0], o[1], o[2], o[3]}, wsr, WS_X + pj * XJ_STRIDE + k * 32 + c2 * 16);
    }
  }
  const unsigned epoch = __builtin_amdgcn_readfirstlane(lds_read4(ldsb + L_CNT + 12)) + 1u;
  STAMP(4);

  const bool p2wave = w < 4;
  const int pw = w & 3;
  const int kcol = xcd * 32 + jx;
  const int ct = jx & 7, pr = 4 * xcd + (jx >> 3);
  u32x4 gc0, gs0, hc0, hs0; float twc[8], tws[8];
  auto p2consts = [&]() {
    const float k1f = (float)(16 * pw + c), q4f = (float)(4 * q), k64 = k1f * (1.0f / 64.0f), c32 = (float)c * (1.0f / 32.0f), k2k = k1f * (1.0f / 2048.0f);
    #pragma unroll
    for (int i = 0; i < 4; ++i) { const float a0 = (2.0f * q4f + (float)(2 * i)) * k64, a1 = (2.0f * q4f + (float)(2 * i + 1)) * k64;
      gc0[i] = pk2(cos_rev(a0), cos_rev(a1)); gs0[i] = pk2(sin_rev(a0), sin_rev(a1));
      const float b0 = (q4f + (float)(2 * i < 4 ? 2 * i : 12 + 2 * i)) * c32, b1 = (q4f + (float)(2 * i + 1 < 4 ? 2 * i + 1 : 13 + 2 * i)) * c32;
      hc0[i] = pk2(cos_rev(b0), cos_rev(b1)); hs0[i] = pk2(sin_rev(b0), sin_rev(b1)); }
    #pragma unroll
    for (int i = 0; i < 8; ++i) { const float a = (q4f + (float)(16 * (i >> 2) + (i & 3))) * k2k; twc[i] = cos_rev(a); tws[i] = sin_rev(a); }
    PIN(gc0); PIN(gs0); PIN(hc0); PIN(hs0);
    #pragma unroll
    for (int i = 0; i < 8; ++i) { PIN(twc[i]); PIN(tws[i]); } };
  const unsigned cnt = ldsb + L_CNT;
  auto xgather = [&](int half) { glds_sc1((64 * pw + l) * XJ_STRIDE + half * 16, p.ws + WS_X + kcol * 32, __builtin_amdgcn_readfirstlane(ldsb + L_XCOL + pw * 1024 + half * 4096)); };
  unsigned Wv[8];
  auto wload = [&]() {
    const char* wb = p.ws + WS_WT + (size_t)(kcol >> 6) * (64 * 8192) + (kcol & 63) * 64;
    #pragma unroll
    for (int i = 0; i < 8; ++i) gload4_sc1(Wv[i], ((pw >> 1) + 2 * (16 * (i >> 2) + 4 * q + (i & 3))) * 8192 + (pw & 1) * 4096 + c * 4, wb);
    if (b == 0) { const unsigned wofs = __builtin_amdgcn_readfirstlane(ldsb + pw * 1024);
      #pragma unroll
      for (int i = 0; i < 2; ++i) { const int piece = 16 * (pw + 4 * i) + (l >> 2);
        glds_sc1((piece >> 1) * 8192 + (piece & 1) * 4096 + (l & 3) * 16, wb, wofs + L_WCOL + i * 4096);
        glds_sc1(i * 4096 + pw * 1024 + l * 16, p.ws + WS_WT + (size_t)256 * 8192, wofs + L_W256 + i * 4096); } } };
  auto h_done = [&]() { if (lds_inc(cnt, l) == 3u) flag_line(p.ws, FL_F2 + (rt * 16 + ct1) * FS, epoch, l); };
  auto gate = [&]() {
    const u32x4 gwv = __builtin_bit_cast(u32x4, lds_read16(ldsb + L_GW + l * 16));
    float sum = 0.f;
    #pragma unroll
    for (int i = 0; i < 4; ++i) { sum += rbf(bflo(xv[i]) * bflo(gwv[i])); sum += rbf(bfhi(xv[i]) * bfhi(gwv[i])); }
    sum += __builtin_bit_cast(float, __builtin_amdgcn_update_dpp(0, __builtin_bit_cast(int, sum), 0x111, 0xf, 0xf, true));
    sum += __builtin_bit_cast(float, __builtin_amdgcn_update_dpp(0, __builtin_bit_cast(int, sum), 0x112, 0xf, 0xf, true));
    sum += __builtin_bit_cast(float, __builtin_amdgcn_update_dpp(0, __builtin_bit_cast(int, sum), 0x114, 0xf, 0xf, true));
    sum += __builtin_bit_cast(float, __builtin_amdgcn_update_dpp(0, __builtin_bit_cast(int, sum), 0x118, 0xf, 0xf, true));
    sum += __builtin_bit_cast(float, __builtin_amdgcn_update_dpp(0, __builtin_bit_cast(int, sum), 0x142, 0xa, 0xf, true));
    sum += __builtin_bit_cast(float, __builtin_amdgcn_update_dpp(0, __builtin_bit_cast(int, sum), 0x143, 0xc, 0xf, true));
    const float gg = rbf(__builtin_amdgcn_rcpf(1.0f + __expf(-rbf(sum))));
    st4_wt(__float_as_uint(gg), wsr, l == 63 ? WS_G + (n2 + 256 * gq + 64 * pr4 + 32 * t1) * 4 : OOB); };

  const unsigned vrow = (l >> 3) * 1024 + ((l & 7) ^ (4 * ((l >> 4) & 1))) * 16;
  const unsigned vrowY = (l >> 3) * YROW + ((l & 7) ^ (4 * ((l >> 4) & 1))) * 16;
  #define M2_STAGE(u) (u)
  const unsigned w2dst = __builtin_amdgcn_readfirstlane(ldsb + (w & 1) * 8192), vw2 = vrow + (w & 1) * 896 * 1024 + ct * 128;
  #define M2_ISSUE_W2(u, j) glds(vw2 + (j) * 8192 + M2_STAGE(u) * 65536, p.w2, w2dst + ((u) % NWS) * 16384 + (j) * 1024)
  auto issue_w_prologue = [&]() {
    #pragma unroll
    for (int u = 0; u < NWS; ++u)
      #pragma unroll
      for (int j = 0; j < 8; ++j) M2_ISSUE_W2(u, j); };

  if (has_m1) {
    f32x16 acc1[2] = {};
    const int fw = w & 1, sw = w >> 1;
    unsigned xo[2][4], wo0[2], wo1[2];
    { const int h = l >> 5, blk = (l >> 4) & 1, a = (l & 15) >> 2, pp = l & 3, xrow = 32 * sw + (l & 31);
      #pragma unroll
      for (int hh = 0; hh < 2; ++hh) {
        #pragma unroll
        for (int ks = 0; ks < 4; ++ks) { xo[hh][ks] = ldsb + hh * 65536 + xrow * 128 + 16 * ((2 * ks + h) ^ ((xrow >> 1) & 7)); PIN(xo[hh][ks]); }
        wo0[hh] = ldsb + hh * 65536 + 16384 + (8 * h + a) * 256 + 16 * (((8 * fw + 0) ^ (a << 2)) + 2 * blk + (pp >> 1)) + 8 * (pp & 1); PIN(wo0[hh]);
        wo1[hh] = ldsb + hh * 65536 + 16384 + (8 * h + a) * 256 + 16 * (((8 * fw + 4) ^ (a << 2)) + 2 * blk + (pp >> 1)) + 8 * (pp & 1); PIN(wo1[hh]); } }
    struct Fr { bf16x8 B, A0, A1; } fa, fb;
    #define RB(f, g)  f.B  = lds_read16(xo[(((g) >> 2) & 3) >> 1][(g) & 3] + (((g) >> 2) & 1) * 32768)
    #define RA(base_, g) cat4(tr_read(base_[(((g) >> 2) & 3) >> 1] + (((g) >> 2) & 1) * 32768 + ((g) & 3) * 4096), tr_read(base_[(((g) >> 2) & 3) >> 1] + (((g) >> 2) & 1) * 32768 + ((g) & 3) * 4096 + 1024))
    SB();
    M1_STAGE(2);
    if (p2wave) p2consts();
    WAIT_VM(5);
    BARE_BAR();
    STAMP(5);
    RB(fa, 0); fa.A0 = RA(wo0, 0); fa.A1 = RA(wo1, 0);
    unsigned f1v = 0;
    auto kstep = [&](auto G, Fr& cur, Fr& nxt) {
      constexpr int g = decltype(G)::v, st = g >> 2, ks = g & 3;
      if constexpr (ks == 0 && st > 0) {
        if constexpr (st == 1) { WAIT_VM(4); }
        else if constexpr (st <= 3) { if (w == 0) { WAIT_VM(5); } else { WAIT_VM(4); } }
        else if constexpr (st <= 5) { WAIT_VM(4); }
        else if constexpr (st == 6) { if (w < 4) { WAIT_VM(0); PIN(f1v); if (!__all(f1v == epoch)) spin4(p.ws, FL_F1 + (64 * w + l) * FS, epoch, l); } else { WAIT_VM(2); } }
        BARE_BAR();
        STAMP(20 + st);
        if constexpr (st == 5) { if (w < 4) gload4_sc1(f1v, (64 * w + l) * FS, p.ws + FL_F1); }
      }
      acc1[0] = mfma32(cur.A0, cur.B, acc1[0]); SB();
      if constexpr (g + 1 < 32) { RB(nxt, g + 1); nxt.A0 = RA(wo0, g + 1); }
      SB();
      acc1[1] = mfma32(cur.A1, cur.B, acc1[1]); SB();
      if constexpr (g + 1 < 32) { nxt.A1 = RA(wo1, g + 1); }
      if constexpr (st <= 4) { M1_ISSUE(st + 3, ks); }
      if constexpr (g == 7) { if (w == 0) flag_line(p.ws, FL_F1 + b * FS, epoch, l); }
      if constexpr (g == 24 || g == 25) { if (w < 4) xgather(g & 1); }
      if constexpr (g == 20 || g == 21) { if (w >= 4) glds(l * 16, (const char*)p.w2 + (size_t)((xcd * 28 + jx) * 8 + (w & 3) * 2 + (g & 1)) * 1024, ldsb + L_DUMMY + (w & 3) * 1024); }
      SB();
    };
    #define K2(g) kstep(IC<g>{}, fa, fb); kstep(IC<g + 1>{}, fb, fa);
    K2(0) K2(2) K2(4) K2(6) K2(8) K2(10) K2(12) K2(14) K2(16) K2(18) K2(20) K2(22) K2(24) K2(26) K2(28) K2(30)
    STAMP(6);
    BARE_BAR();
    unsigned fwv = 0;
    if (w < 4) gload4_sc1(fwv, (l & 31) * FS, p.ws + FL_FW);
    STAMP(7);
    u32x4 hf[4];
    #pragma unroll
    for (int ft = 0; ft < 2; ++ft)
      #pragma unroll
      for (int u = 0; u < 2; ++u) {
        #pragma unroll
        for (int jj = 0; jj < 4; ++jj) {
          const f32x2 v = {acc1[ft][8 * u + 2 * jj], acc1[ft][8 * u + 2 * jj + 1]};
          const f32x2 t = v * -1.44269504088896340736f;
          const f32x2 e = (f32x2){__builtin_amdgcn_exp2f(t[0]), __builtin_amdgcn_exp2f(t[1])} + 1.0f;
          const f32x2 r = v * (f32x2){__builtin_amdgcn_rcpf(e[0]), __builtin_amdgcn_rcpf(e[1])};
          hf[2 * ft + u][jj] = pk2(r[0], r[1]);
        }
        if (w >= 4) st16_wt(hf[2 * ft + u], wsr, WS_H + ((((s0 >> 5) + sw) * 112 + ((f0 + 64 * fw + 32 * ft + 16 * u) >> 4)) * 64 + l) * 16);
        else *(LAS u32x4*)(size_t)(ldsb + L_HX + (w * 4 + 2 * ft + u) * 1024 + l * 16) = hf[2 * ft + u];
      }
    SB();
    STAMP(8);
    if (w < 4) { WAIT_LGKM0(); lds_inc(cnt + 28 + 0, l);
      WAIT_VM(0); PIN(fwv); if (!__all(fwv == epoch)) spin4(p.ws, FL_FW + (l & 31) * FS, epoch, l);
      wload(); }
    else {
      lds_spin(cnt + 28, 4u);
      const int pw4 = w - 4, psw = pw4 >> 1, pfw = pw4 & 1;
      #pragma unroll
      for (int i = 0; i < 4; ++i)
        st16_wt(*(const LAS u32x4*)(size_t)(ldsb + L_HX + (pw4 * 4 + i) * 1024 + l * 16), wsr, WS_H + ((((s0 >> 5) + psw) * 112 + ((f0 + 64 * pfw + 16 * i) >> 4)) * 64 + l) * 16);
      WAIT_VM(0);
      h_done();
      if (w < 6) issue_w_prologue();
    }
  } else {
    WAIT_VM(0); BAR();
    if (w == 0) flag_line(p.ws, FL_F1 + b * FS, epoch, l);
    STAMP(18);
    { PIN(er); PIN(ei);
      const int a = (l & 15) >> 2, pp = l & 3;
      #pragma unroll
      for (int rh = 0; rh < 2; ++rh)
        #pragma unroll
        for (int cb = 0; cb < 4; ++cb) { const int row = 16 * rh + 4 * q + a;
          const unsigned ad = wscr + row * 128 + ((cb ^ ((row >> 1) & 3)) * 32) + pp * 8;
          const u32x2 vr = __builtin_bit_cast(u32x2, tr_read(ad)), vi = __builtin_bit_cast(u32x2, tr_read(ad + 4096));
          const u32x4 o = {__builtin_amdgcn_perm(vi[0], vr[0], 0x05040100u), __builtin_amdgcn_perm(vi[0], vr[0], 0x07060302u), __builtin_amdgcn_perm(vi[1], vr[1], 0x05040100u), __builtin_amdgcn_perm(vi[1], vr[1], 0x07060302u)};
          st16_wt(o, wsr, WS_WT + (kq * 64 + (m0 >> 5)) * 8192 + rh * 4096 + cb * 1024 + c * 64 + q * 16); }
      st4_wt(er | (ei << 16), wsr, (kq == 3 && l < 32) ? WS_WT + 256 * 8192 + (m0 + l) * 4 : OOB); }
    if (w == 1 && jx == 28 && l < 32) { __hip_atomic_store((GAS unsigned*)(p.ws + FL_F3 + (xcd * 2 + 0) * FS + l * 4), 0u, __ATOMIC_RELAXED, __HIP_MEMORY_SCOPE_AGENT);
                                        __hip_atomic_store((GAS unsigned*)(p.ws + FL_F3 + (xcd * 2 + 1) * FS + l * 4), 0u, __ATOMIC_RELAXED, __HIP_MEMORY_SCOPE_AGENT); }
    STAMP(19);
    WAIT_VM(0); BAR();
    STAMP(14);
    if (w == 0) flag_line(p.ws, FL_FW + (xcd + 8 * (jx - 28)) * FS, epoch, l);
    if (w >= 4) {
      unsigned long long pc; unsigned rest;
      asm volatile("s_getpc_b64 %0\nfused_pc_label:\n\ts_mov_b32 %1, fused_end_label-fused_pc_label" : "=s"(pc), "=s"(rest));
      const char* code = (const char*)(pc & ~127ull);
      #pragma unroll
      for (int i = 0; i < 6; ++i) { const unsigned o = (unsigned)(i * 4 + (w & 3)) * 1024u; if (o + 1024u <= rest) glds(l * 16, code + o, ldsb + L_DUMMY + (w & 3) * 1024); }
    }
    if (w < 4) { p2consts(); spin4(p.ws, FL_F1 + (64 * w + l) * FS, epoch, l);
      lds_inc(cnt + 20, l); lds_spin(cnt + 20, 4u);
      xgather(0); xgather(1);
      spin4(p.ws, FL_FW + (l & 31) * FS, epoch, l);
      wload(); }
    else if (w < 6) issue_w_prologue();
    STAMP(8);
  }
  STAMP(9);
  if (p2wave) {
    { const unsigned gc = cnt + 20; lds_inc(gc, l); lds_spin(gc, has_m1 ? 4u : 8u); }
    const unsigned xc = ldsb + L_XCOL, wc = ldsb + L_WCOL, ub = ldsb + L_U;
    bf16x8 Zr[2][2], Zi[2][2];
    #pragma unroll
    for (int u = 0; u < 2; ++u)
      #pragma unroll
      for (int mt = 0; mt < 2; ++mt) { const unsigned ad = xc + ((4 * u + q) * 32 + 16 * mt + c) * 16; Zr[mt][u] = lds_read16(ad); Zi[mt][u] = lds_read16(ad + 4096); }
    const unsigned sgn = (c & 1) ? 0x80008000u : 0u;
    u32x4 gc1 = xor4(gc0, sgn), gs1 = xor4(gs0, sgn), ngs0 = xor4(gs0, 0x80008000u), ngs1 = xor4(gs0, sgn ^ 0x80008000u);
    u32x4 hc1 = xor4(hc0, 0x80000000u), hs1 = xor4(hs0, 0x80000000u), nhs0 = xor4(hs0, 0x80008000u), nhs1 = xor4(hs0, 0x00008000u);
    PIN(gc1); PIN(gs1); PIN(ngs0); PIN(ngs1); PIN(hc1); PIN(hs1); PIN(nhs0); PIN(nhs1);
    const bf16x8 Gc[2] = {as8(gc0), as8(gc1)}, Gs[2] = {as8(gs0), as8(gs1)}, nGs[2] = {as8(ngs0), as8(ngs1)};
    const bf16x8 Hc[2] = {as8(hc0), as8(hc1)}, Hs[2] = {as8(hs0), as8(hs1)}, nHs[2] = {as8(nhs0), as8(nhs1)};
    f32x4 Dr[2] = {}, Di[2] = {};
    #pragma unroll
    for (int u = 0; u < 2; ++u)
      #pragma unroll
      for (int mt = 0; mt < 2; ++mt) {
        Dr[mt] = mfma16(Zr[mt][u], Gc[u], Dr[mt]); Di[mt] = mfma16(Zi[mt][u], Gc[u], Di[mt]);
        Dr[mt] = mfma16(Zi[mt][u], Gs[u], Dr[mt]); Di[mt] = mfma16(Zr[mt][u], nGs[u], Di[mt]);
      }
    SB(); lds_inc(cnt + 24, l); SB();
    u32x4 tr_, ti_;
    { float re[8], im[8];
      #pragma unroll
      for (int i = 0; i < 8; ++i) { const float x = Dr[i >> 2][i & 3], y = Di[i >> 2][i & 3]; re[i] = x * twc[i] + y * tws[i]; im[i] = y * twc[i] - x * tws[i]; }
      #pragma unroll
      for (int jj = 0; jj < 4; ++jj) { tr_[jj] = pk2(re[2 * jj], re[2 * jj + 1]); ti_[jj] = pk2(im[2 * jj], im[2 * jj + 1]); } }
    f32x4 Vr[2] = {}, Vi[2] = {};
    { const bf16x8 Tr = as8(tr_), Ti = as8(ti_);
      #pragma unroll
      for (int t = 0; t < 2; ++t) { Vr[t] = mfma16(Hc[t], Tr, Vr[t]); Vi[t] = mfma16(Hc[t], Ti, Vi[t]); }
      #pragma unroll
      for (int t = 0; t < 2; ++t) { Vr[t] = mfma16(Hs[t], Ti, Vr[t]); Vi[t] = mfma16(nHs[t], Tr, Vi[t]); } }
    WAIT_VM(0);
    #pragma unroll
    for (int i = 0; i < 8; ++i) PIN(Wv[i]);
    if (kcol != 0) { float re[8], im[8];
      #pragma unroll
      for (int i = 0; i < 8; ++i) { const float x = Vr[i >> 2][i & 3], y = Vi[i >> 2][i & 3], wr = bflo(Wv[i]), wi = bfhi(Wv[i]);
        re[i] = x * wr - y * wi; im[i] = x * wi + y * wr; }
      #pragma unroll
      for (int jj = 0; jj < 4; ++jj) { tr_[jj] = pk2(re[2 * jj], re[2 * jj + 1]); ti_[jj] = pk2(im[2 * jj], im[2 * jj + 1]); }
    } else {
      const unsigned vx = ldsb + L_VX, w2c = ldsb + L_W256;
      #pragma unroll
      for (int i = 0; i < 8; ++i) { const int m = 16 * pw + c + 64 * (16 * (i >> 2) + 4 * q + (i & 3)); *(LAS f32x2*)(size_t)(vx + m * 8) = (f32x2){Vr[i >> 2][i & 3], Vi[i >> 2][i & 3]}; }
      WAIT_LGKM0();
      lds_inc(cnt + 8, l); lds_spin(cnt + 8, 4u);
      float re[8], im[8];
      #pragma unroll
      for (int i = 0; i < 8; ++i) { const int m = 16 * pw + c + 64 * (16 * (i >> 2) + 4 * q + (i & 3)), mm = (2048 - m) & 2047;
        const f32x2 zm = *(const LAS f32x2*)(size_t)(vx + mm * 8);
        const unsigned a0 = Wv[i], a1 = lds_read4(wc + mm * 4), b0 = lds_read4(w2c + m * 4), b1 = lds_read4(w2c + mm * 4);
        const float Ar = 0.5f * (bflo(a0) + bflo(a1)), Ai = 0.5f * (bfhi(a0) - bfhi(a1)), Br = 0.5f * (bflo(b0) + bflo(b1)), Bi = 0.5f * (bfhi(b0) - bfhi(b1));
        const float Pr = 0.5f * (Ar + Br), Pi = 0.5f * (Ai + Bi), Qr = 0.5f * (Ar - Br), Qi = 0.5f * (Ai - Bi);
        const float x = Vr[i >> 2][i & 3], y = Vi[i >> 2][i & 3], u = zm[0], v = -zm[1];
        re[i] = x * Pr - y * Pi + u * Qr - v * Qi; im[i] = x * Pi + y * Pr + u * Qi + v * Qr; }
      #pragma unroll
      for (int jj = 0; jj < 4; ++jj) { tr_[jj] = pk2(re[2 * jj], re[2 * jj + 1]); ti_[jj] = pk2(im[2 * jj], im[2 * jj + 1]); }
    }
    f32x4 Ur[2] = {}, Ui[2] = {};
    { const bf16x8 Tr = as8(tr_), Ti = as8(ti_);
      #pragma unroll
      for (int t = 0; t < 2; ++t) { Ur[t] = mfma16(Hc[t], Tr, Ur[t]); Ui[t] = mfma16(Hc[t], Ti, Ui[t]); }
      #pragma unroll
      for (int t = 0; t < 2; ++t) { Ur[t] = mfma16(nHs[t], Ti, Ur[t]); Ui[t] = mfma16(Hs[t], Tr, Ui[t]); } }
    #pragma unroll
    for (int t = 0; t < 2; ++t) { float re[4], im[4];
      #pragma unroll
      for (int r = 0; r < 4; ++r) { const float x = Ur[t][r], y = Ui[t][r]; re[r] = x * twc[4 * t + r] - y * tws[4 * t + r]; im[r] = y * twc[4 * t + r] + x * tws[4 * t + r]; }
      char LAS* dst = (char LAS*)(size_t)ub + (16 * pw + c) * 72 + (16 * t + 4 * q) * 2;
      *(LAS u32x2*)dst = (u32x2){pk2(re[0], re[1]), pk2(re[2], re[3])};
      *(LAS u32x2*)(dst + 64 * 72) = (u32x2){pk2(im[0], im[1]), pk2(im[2], im[3])}; }
    WAIT_LGKM0();
    { lds_inc(cnt + 4, l); lds_spin(cnt + 4, 4u); }
    STAMP(10);
    f32x4 yr[2] = {}, yi[2] = {};
    const int a = (l & 15) >> 2, pp = l & 3;
    #pragma unroll
    for (int u = 0; u < 2; ++u)
      #pragma unroll
      for (int t = 0; t < 2; ++t) {
        const unsigned ad = ub + (32 * u + 8 * q + a) * 72 + (8 * pp + 4 * t) * 2;
        const bf16x8 Ar = cat4(tr_read(ad), tr_read(ad + 4 * 72));
        const bf16x8 Ai = cat4(tr_read(ad + 64 * 72), tr_read(ad + 64 * 72 + 4 * 72));
        yr[t] = mfma16(Ar, Gc[u], yr[t]); yi[t] = mfma16(Ai, Gc[u], yi[t]);
        yr[t] = mfma16(Ai, nGs[u], yr[t]); yi[t] = mfma16(Ar, Gs[u], yi[t]);
      }
    const float ysc = kcol == 0 ? 0.5f : 1.0f;
    const u32x4 yre = {pk2(yr[0][0] * ysc, yr[0][1] * ysc), pk2(yr[0][2] * ysc, yr[0][3] * ysc), pk2(yr[1][0] * ysc, yr[1][1] * ysc), pk2(yr[1][2] * ysc, yr[1][3] * ysc)};
    const u32x4 yim = {pk2(yi[0][0] * ysc, yi[0][1] * ysc), pk2(yi[0][2] * ysc, yi[0][3] * ysc), pk2(yi[1][0] * ysc, yi[1][1] * ysc), pk2(yi[1][2] * ysc, yi[1][3] * ysc)};
    const unsigned so = (32 * (16 * pw + c) + 8 * q) * 2;
    st16_wt(yre, wsr, WS_Y + kcol * YROW + so);
    st16_wt(yim, wsr, WS_Y + (256 + kcol) * YROW + so);
    gate();
    STAMP(11);
  }
  if (!p2wave) gate();
  unsigned vst[4];
  #pragma unroll
  for (int i = 0; i < 4; ++i)
    vst[i] = w < 4 ? vrow + ((i >> 1) * 896 + (w & 3) * 16 + (i & 1) * 8) * 1024 + ct * 128
                   : l * 16 + ((2 * pr + ((w & 3) >> 1)) * 112 + (i >> 1) * 56 + 2 * (w & 1) + (i & 1)) * 1024;
  const char* stbase = w < 4 ? (const char*)p.w2 : p.ws + WS_H;
  const unsigned stmul = w < 4 ? 65536u : 4096u;
  const unsigned stdst = __builtin_amdgcn_readfirstlane(ldsb + (w < 4 ? 0u : L_HRING) + (w & 3) * 2048);
  #define M2_ISSUE(u, i) glds_sc1(vst[i] + M2_STAGE(u) * stmul, stbase, stdst + ((u) % 5) * 16384 + ((i) >> 1) * 8192 + ((i) & 1) * 1024)
  const unsigned vy8 = vrowY + ((w >> 2) * 256 + (w & 3) * 16) * YROW + pr * 128;
  const unsigned y8dst = __builtin_amdgcn_readfirstlane(ldsb + L_HRING + (w >> 2) * 8192 + (w & 3) * 2048);
  #define M2_ISSUE_Y(u, i) glds_sc1(vy8 + (((u) - 14) * 64 + (i) * 8) * YROW, p.ws + WS_Y, y8dst + ((u) % 5) * 16384 + (i) * 1024)
  if (w >= 4) {
    if (w == 6) { spin4(p.ws, FL_F2 + ((pr >> 1) * 16 + (l < 14 ? l : 0)) * FS, epoch, l); lds_inc(cnt + 16, l); }
    lds_spin(cnt + 16, 1u);
    if (has_m1) lds_spin(cnt, 4u);
    if (b == 0) lds_spin(cnt + 4, 4u);
    STAMP(12);
    #pragma unroll
    for (int u = 0; u < 3; ++u) { M2_ISSUE(u, 0); M2_ISSUE(u, 1); M2_ISSUE(u, 2); M2_ISSUE(u, 3); }
    lds_spin(cnt + 24, 4u);
    M2_ISSUE(3, 0); M2_ISSUE(3, 1); M2_ISSUE(3, 2); M2_ISSUE(3, 3);
    WAIT_VM(12);
  } else { WAIT_VM(0); }
  BAR();
  if (tid == 0) __hip_atomic_fetch_add((GAS unsigned*)(p.ws + FL_F3 + (b & 15) * FS), 1u, __ATOMIC_RELAXED, __HIP_MEMORY_SCOPE_AGENT);
  if (w >= 4) { M2_ISSUE(4, 0); M2_ISSUE(4, 1); M2_ISSUE(4, 2); M2_ISSUE(4, 3); }
  STAMP(13);
  f32x16 accb = {}, acca = {};
  {
    const int kh = w >> 2, nh = (w >> 1) & 1, sh = w & 1;
    const int h = l >> 5, blk = (l >> 4) & 1, a = (l & 15) >> 2, pp = l & 3;
    unsigned aoff[2];
    #pragma unroll
    for (int hh = 0; hh < 2; ++hh) { aoff[hh] = ldsb + hh * 65536 + kh * 8192 + (4 * h + a) * 128 + 16 * ((4 * nh + 2 * (pp & 1) + blk) ^ (4 * (a >> 1))) + 8 * (pp >> 1); PIN(aoff[hh]); }
    unsigned boffH[2], boffY[2];
    #pragma unroll
    for (int hh = 0; hh < 2; ++hh) { boffH[hh] = ldsb + L_HRING + hh * 49152 + kh * 8192 + sh * 4096 + l * 16; PIN(boffH[hh]);
      boffY[hh] = ldsb + L_HRING + hh * 49152 + kh * 8192 + (4 * h + a) * 128 + 16 * ((4 * sh + 2 * blk + (pp >> 1)) ^ (4 * (a >> 1))) + 8 * (pp & 1); PIN(boffY[hh]); }
    const int i31 = l & 31, dn = 64 * ct + 32 * nh + 16 * ((i31 >> 2) & 1) + 4 * (i31 >> 3) + (i31 & 3);
    const float dsc = (kh ? -1.0f : 1.0f) * (float)dn * (1.0f / 512.0f);
    const float cbase = (float)(4 * h) * dsc + (kh ? 0.0f : 0.25f);
    const float c256 = (dn & 1) ? -1.0f : 1.0f;
    u32x4 Cf[16];
    f32x2 cprev[4], ccur[4];
    const float two_c = 2.0f * cos_rev((float)(dn & 31) * (1.0f / 32.0f));
    auto cgen = [&](auto KS) { constexpr int ks = decltype(KS)::v;
      #pragma unroll
      for (int jj = 0; jj < 4; ++jj) { f32x2 v;
        if constexpr (ks < 2) {
          #pragma unroll
          for (int e = 0; e < 2; ++e) { const int j = 2 * jj + e; v[e] = sin_rev(__builtin_fmaf((float)(16 * ks + 8 * (j >> 2) + (j & 3)), dsc, cbase)); }
        } else v = ccur[jj] * two_c - cprev[jj];
        if constexpr (ks > 0) cprev[jj] = ccur[jj];
        ccur[jj] = v;
        if (ks == 0 && jj == 0) v[0] = (kh && h == 0) ? c256 : v[0];
        Cf[ks][jj] = pk2(v[0], v[1]); }
      PIN(Cf[ks]); };
    struct F2 { bf16x8 A[4], B[4]; } fa, fb;
    auto rdA = [&](auto ST, auto KS, F2& f) { constexpr int st = decltype(ST)::v, ks = decltype(KS)::v;
      if constexpr (st < 14) { constexpr unsigned o = (st % NWS) * 16384 + ks * 2048; f.A[ks] = cat4(tr_read(aoff[o >> 16] + (o & 65535)), tr_read(aoff[o >> 16] + (o & 65535) + 1024)); } };
    auto rdB = [&](auto ST, auto KS, F2& f) { constexpr int st = decltype(ST)::v, ks = decltype(KS)::v; constexpr int sl = st % NHS, hi = sl >= 3; constexpr unsigned o = (sl - 3 * hi) * 16384;
      if constexpr (st < 14) f.B[ks] = lds_read16(boffH[hi] + o + ks * 1024);
      else f.B[ks] = cat4(tr_read(boffY[hi] + o + ks * 2048), tr_read(boffY[hi] + o + ks * 2048 + 1024)); };
    rdA(IC<0>{}, IC<0>{}, fa); rdB(IC<0>{}, IC<0>{}, fa); rdA(IC<0>{}, IC<1>{}, fa); rdB(IC<0>{}, IC<1>{}, fa);
    rdA(IC<0>{}, IC<2>{}, fa); rdB(IC<0>{}, IC<2>{}, fa); rdA(IC<0>{}, IC<3>{}, fa); rdB(IC<0>{}, IC<3>{}, fa);
    unsigned f3v = 0;
    unsigned gv = 0;
    auto step = [&](auto ST, F2& cur, F2& nxt) {
      constexpr int st = decltype(ST)::v;
      if constexpr (st <= 8) { WAIT_VM(12); }
      else if constexpr (st == 9) { if (w < 4) { WAIT_VM(8); if (w == 0) { PIN(f3v); if (!__all(f3v == 16u)) spin4(p.ws, FL_F3 + (l & 15) * FS, 16u, l); } } else { WAIT_VM(12); } }
      else if constexpr (st == 10) { WAIT_VM(10); } else if constexpr (st == 11) { WAIT_VM(9); } else if constexpr (st <= 13) { WAIT_VM(7); }
      else if constexpr (st == 14) { WAIT_VM(4); } else if constexpr (st == 15) { WAIT_VM(0); }
      if constexpr (st <= 15) BAR();
      STAMP(30 + st);
      if constexpr (st == 7) { if (w == 0) gload4_sc1(f3v, (l & 15) * FS, p.ws + FL_F3); }
      if constexpr (st == 10) gload4_sc1(gv, (64 * pr + 32 * sh + (l & 31)) * 4, p.ws + WS_G);
      auto mm = [&](auto KS) { constexpr int ks = decltype(KS)::v;
        if constexpr (st < 14) accb = mfma32(cur.A[ks], cur.B[ks], accb); else acca = mfma32(as8(Cf[4 * (st - 14) + ks]), cur.B[ks], acca); };
      auto iss = [&](auto KS) { constexpr int ks = decltype(KS)::v;
        if constexpr (st + 5 < 14) { M2_ISSUE(st + 5, ks); }
        else if constexpr (st + 5 < 18 && ks >= 1 && ks <= 2) { M2_ISSUE_Y(st + 5, ks - 1); } };
      mm(IC<0>{}); SB();
      if constexpr (st < 17) { rdA(IC<st + 1>{}, IC<0>{}, nxt); rdB(IC<st + 1>{}, IC<0>{}, nxt); rdA(IC<st + 1>{}, IC<1>{}, nxt); rdB(IC<st + 1>{}, IC<1>{}, nxt); }
      iss(IC<0>{});
      SB();
      mm(IC<1>{}); SB();
      if constexpr (st < 17) { rdA(IC<st + 1>{}, IC<2>{}, nxt); rdB(IC<st + 1>{}, IC<2>{}, nxt); rdA(IC<st + 1>{}, IC<3>{}, nxt); rdB(IC<st + 1>{}, IC<3>{}, nxt); }
      iss(IC<1>{});
      SB();
      mm(IC<2>{}); SB();
      iss(IC<2>{});
      SB();
      mm(IC<3>{}); SB();
      iss(IC<3>{});
      if constexpr (st < 16) cgen(IC<(st < 16 ? st : 0)>{});
      SB();
    };
    step(IC<0>{}, fa, fb); step(IC<1>{}, fb, fa); step(IC<2>{}, fa, fb); step(IC<3>{}, fb, fa); step(IC<4>{}, fa, fb); step(IC<5>{}, fb, fa);
    step(IC<6>{}, fa, fb); step(IC<7>{}, fb, fa); step(IC<8>{}, fa, fb); step(IC<9>{}, fb, fa); step(IC<10>{}, fa, fb); step(IC<11>{}, fb, fa);
    step(IC<12>{}, fa, fb); step(IC<13>{}, fb, fa); step(IC<14>{}, fa, fb); step(IC<15>{}, fb, fa); step(IC<16>{}, fa, fb); step(IC<17>{}, fb, fa);
    STAMP(15);
    PIN(gv);
    if (b == 0 && w == 0) flag_line(p.ws, FL_E, epoch, l);
    LAS f32x4* ex = (LAS f32x4*)lds;
    const float g = __uint_as_float(gv), omg = rbf(1.0f - g);
    char* orow = (char*)p.out + (size_t)(64 * pr + 32 * sh + (l & 31)) * 1024 + (64 * ct + 32 * nh + 16 * h) * 2;
    auto tail = [&](auto KH) __attribute__((always_inline)) { constexpr int o = 8 * (1 - decltype(KH)::v), m = 8 * decltype(KH)::v;
      ex[(w * 4 + 0) * 64 + l] = (f32x4){accb[o], accb[o + 1], accb[o + 2], accb[o + 3]};
      ex[(w * 4 + 1) * 64 + l] = (f32x4){accb[o + 4], accb[o + 5], accb[o + 6], accb[o + 7]};
      ex[(w * 4 + 2) * 64 + l] = (f32x4){acca[o], acca[o + 1], acca[o + 2], acca[o + 3]};
      ex[(w * 4 + 3) * 64 + l] = (f32x4){acca[o + 4], acca[o + 5], acca[o + 6], acca[o + 7]};
      BAR();
      const int pwv = w ^ 4;
      const f32x4 b0 = ex[(pwv * 4 + 0) * 64 + l], b1 = ex[(pwv * 4 + 1) * 64 + l], a0 = ex[(pwv * 4 + 2) * 64 + l], a1 = ex[(pwv * 4 + 3) * 64 + l];
      constexpr float ASC = 2.0f / 1048576.0f;
      unsigned o4[4];
      #pragma unroll
      for (int i = 0; i < 4; ++i) {
        const float a_0 = acca[m + 2 * i] + (i < 2 ? a0[2 * i] : a1[2 * i - 4]), a_1 = acca[m + 2 * i + 1] + (i < 2 ? a0[2 * i + 1] : a1[2 * i - 3]);
        const float b_0 = accb[m + 2 * i] + (i < 2 ? b0[2 * i] : b1[2 * i - 4]), b_1 = accb[m + 2 * i + 1] + (i < 2 ? b0[2 * i + 1] : b1[2 * i - 3]);
        const unsigned ab = pk2(a_0 * ASC, a_1 * ASC), bb = pk2(b_0, b_1);
        const unsigned t1_ = pk2(g * bflo(ab), g * bfhi(ab)), t2_ = pk2(omg * bflo(bb), omg * bfhi(bb));
        o4[i] = pk2(bflo(t1_) + bflo(t2_), bfhi(t1_) + bfhi(t2_)); }
      *(GAS u32x4*)(orow + m * 2) = (u32x4){o4[0], o4[1], o4[2], o4[3]}; };
    if (kh) tail(IC<1>{}); else tail(IC<0>{});
  }
  STAMP(16);
  STAMP_FLUSH();
  asm volatile("fused_end_label:");
}

extern "C" void kernel_launch(void* const* d_in, const int* in_sizes, int n_in, void* d_out, int out_size, void* d_ws, size_t ws_size, hipStream_t stream) {
  Params p;
  p.ax = (const uint16_t*)d_in[0]; p.wr = (const uint16_t*)d_in[1]; p.wi = (const uint16_t*)d_in[2];
  p.w1 = (const uint16_t*)d_in[4]; p.w2 = (const uint16_t*)d_in[5]; p.gw = (const uint16_t*)d_in[6];
  p.out = (uint16_t*)d_out; p.ws = (char*)d_ws;
  (void)hipFuncSetAttribute((const void*)fused_kernel, hipFuncAttributeMaxDynamicSharedMemorySize, 163840);
  fused_kernel<<<256, 512, 163840, stream>>>(p);
}
```

```cpp
#include <hip/hip_runtime.h>
#include <hip/hip_bf16.h>
#include <stdint.h>

typedef short    bf16x8 __attribute__((ext_vector_type(8)));
typedef short    s16x4  __attribute__((ext_vector_type(4)));
typedef float    f32x2  __attribute__((ext_vector_type(2)));
typedef float    f32x4  __attribute__((ext_vector_type(4)));
typedef float    f32x16 __attribute__((ext_vector_type(16)));
typedef unsigned u32x2  __attribute__((ext_vector_type(2)));
typedef unsigned u32x4  __attribute__((ext_vector_type(4)));
typedef __bf16   bf16x2_t __attribute__((ext_vector_type(2)));
#define LAS __attribute__((address_space(3)))
#define GAS __attribute__((address_space(1)))
#define DI __device__ __forceinline__

constexpr int F_ = 1792, DF = 257;

constexpr unsigned XJ_STRIDE = 260 * 32;
constexpr unsigned FS = 128;
constexpr unsigned FL_F1 = 0, FL_F3 = 256 * FS, FL_F2 = 512 * FS, FL_E = 768 * FS, FL_ERR = 769 * FS, FL_FW = 772 * FS;
constexpr unsigned WS_X  = 804 * FS;
constexpr unsigned WS_WT = WS_X + 256 * XJ_STRIDE;
constexpr unsigned YROW = 4096 + 128;
constexpr unsigned WS_Y  = WS_WT + 257 * 8192;
constexpr unsigned WS_G  = WS_Y + 512 * YROW;
constexpr unsigned WS_H  = WS_G + 8192;
constexpr unsigned WS_END = WS_H + 64 * 112 * 1024;

struct Params {
  const uint16_t *ax, *wr, *wi, *w1, *w2, *gw;
  uint16_t* out;
  char* ws;
};

DI unsigned pk2(float lo, float hi) { return __builtin_bit_cast(unsigned, __builtin_convertvector((f32x2){lo, hi}, bf16x2_t)); }
DI float bflo(unsigned u) { return __uint_as_float(u << 16); }
DI float bfhi(unsigned u) { return __uint_as_float(u & 0xffff0000u); }
DI float rbf(float x) { return bflo(pk2(x, x)); }
DI float sin_rev(float x) { return __builtin_amdgcn_sinf(x); }
DI float cos_rev(float x) { return __builtin_amdgcn_cosf(x); }
DI f32x4 mfma16(bf16x8 a, bf16x8 b, f32x4 c) { return __builtin_amdgcn_mfma_f32_16x16x32_bf16(a, b, c, 0, 0, 0); }
DI f32x16 mfma32(bf16x8 a, bf16x8 b, f32x16 c) { return __builtin_amdgcn_mfma_f32_32x32x16_bf16(a, b, c, 0, 0, 0); }
DI bf16x8 mk8(unsigned a, unsigned b, unsigned c, unsigned d) { return __builtin_bit_cast(bf16x8, (u32x4){a, b, c, d}); }
DI bf16x8 as8(u32x4 v) { return __builtin_bit_cast(bf16x8, v); }
DI u32x4 xor4(u32x4 v, unsigned m) { v ^= m; return v; }
DI bf16x8 neg8(bf16x8 v) { return as8(xor4(__builtin_bit_cast(u32x4, v), 0x80008000u)); }
DI s16x4 tr_read(unsigned addr) { return __builtin_amdgcn_ds_read_tr16_b64_v4i16((LAS s16x4*)addr); }
DI bf16x8 lds_read16(unsigned addr) { return *(const LAS bf16x8*)addr; }
DI unsigned lds_read4(unsigned addr) { return *(const LAS unsigned*)addr; }
DI unsigned lds_read2(unsigned addr) { return *(const LAS unsigned short*)addr; }
DI bf16x8 cat4(s16x4 lo, s16x4 hi) { return __builtin_shufflevector(lo, hi, 0, 1, 2, 3, 4, 5, 6, 7); }
#define WAIT_VM(n) asm volatile("s_waitcnt vmcnt(%0)" :: "n"(n) : "memory")
#define WAIT_LGKM0() asm volatile("s_waitcnt lgkmcnt(0)" ::: "memory")
#define SB() __builtin_amdgcn_sched_barrier(0)
#define BARE_BAR() do { SB(); asm volatile("" ::: "memory"); __builtin_amdgcn_s_barrier(); asm volatile("" ::: "memory"); SB(); } while (0)
#define BAR() do { SB(); WAIT_LGKM0(); __builtin_amdgcn_s_barrier(); asm volatile("" ::: "memory"); SB(); } while (0)
#define PIN(x) asm volatile("" : "+v"(x))

DI void glds(unsigned voff, const void* sbase, unsigned dst) {
  asm volatile("s_mov_b32 m0, %2\n\ts_nop 0\n\tglobal_load_lds_dwordx4 %0, %1" :: "v"(voff), "s"(sbase), "s"(dst) : "memory");
}
DI void glds_sc1(unsigned voff, const void* sbase, unsigned dst) {
  asm volatile("s_mov_b32 m0, %2\n\ts_nop 0\n\tglobal_load_lds_dwordx4 %0, %1 sc1" :: "v"(voff), "s"(sbase), "s"(dst) : "memory");
}
DI void gload16(u32x4& d, unsigned voff, const void* sbase) { asm volatile("global_load_dwordx4 %0, %1, %2" : "=v"(d) : "v"(voff), "s"(sbase) : "memory"); }
DI void gload16_sc1(u32x4& d, unsigned voff, const void* sbase) { asm volatile("global_load_dwordx4 %0, %1, %2 sc1" : "=v"(d) : "v"(voff), "s"(sbase) : "memory"); }
DI void gload4_sc1(unsigned& d, unsigned voff, const void* sbase) { asm volatile("global_load_dword %0, %1, %2 sc1" : "=v"(d) : "v"(voff), "s"(sbase) : "memory"); }
typedef __amdgpu_buffer_rsrc_t rsrc_t;
DI void st16_wt(u32x4 v, rsrc_t r, unsigned off) { __builtin_amdgcn_raw_buffer_store_b128(v, r, off, 0, 16); }
DI void st4_wt(unsigned v, rsrc_t r, unsigned off) { __builtin_amdgcn_raw_buffer_store_b32(v, r, off, 0, 16); }
constexpr unsigned OOB = 0x80000000u;
DI void flag_store(char* ws, unsigned off, unsigned epoch) { __hip_atomic_store((GAS unsigned*)(ws + off), epoch, __ATOMIC_RELAXED, __HIP_MEMORY_SCOPE_AGENT); }
DI void flag_line(char* ws, unsigned off, unsigned epoch, int l) { if (l < 32) __hip_atomic_store((GAS unsigned*)(ws + off + l * 4), epoch, __ATOMIC_RELAXED, __HIP_MEMORY_SCOPE_AGENT); }

#define STAMP(i) do {} while (0)
#define STAMP_DECL
#define STAMP_FLUSH() do {} while (0)

struct Poll { unsigned a, b, c, d; };
DI void poll_issue(Poll& v, unsigned voff, const char* ws) { gload4_sc1(v.a, voff, ws); gload4_sc1(v.b, voff, ws + 64 * FS); gload4_sc1(v.c, voff, ws + 128 * FS); gload4_sc1(v.d, voff, ws + 192 * FS); }
DI void poll_pin(Poll& v) { asm volatile("" : "+v"(v.a), "+v"(v.b), "+v"(v.c), "+v"(v.d)); }
DI void spin_poll(Poll& v, const char* base, char* ws, unsigned epoch, int l) {
  if (__all(v.a == epoch && v.b == epoch && v.c == epoch && v.d == epoch)) return;
  const unsigned long long t0 = __builtin_amdgcn_s_memrealtime();
  for (;;) {
    __builtin_amdgcn_s_sleep(1);
    poll_issue(v, l * FS, base); WAIT_VM(0); poll_pin(v);
    if (__all(v.a == epoch && v.b == epoch && v.c == epoch && v.d == epoch)) break;
    if (__builtin_amdgcn_s_memrealtime() - t0 > 2000000ull) { if (l == 0) flag_store(ws, FL_ERR, 1u); break; }
  }
}
DI void spin4(char* ws, unsigned voff, unsigned epoch, int l) {
  const unsigned long long t0 = __builtin_amdgcn_s_memrealtime();
  for (;;) {
    unsigned v; gload4_sc1(v, voff, ws); WAIT_VM(0); PIN(v);
    if (__all(v == epoch)) break;
    __builtin_amdgcn_s_sleep(1);
    if (__builtin_amdgcn_s_memrealtime() - t0 > 2000000ull) { if (l == 0) flag_store(ws, FL_ERR, 1u); break; }
  }
}

constexpr unsigned L_AUX   = 131072;
constexpr unsigned L_XROWS = L_AUX;
constexpr unsigned L_WSCR  = 32768;
constexpr unsigned L_XCOL  = L_AUX;
constexpr unsigned L_WCOL  = L_AUX + 8192;
constexpr unsigned L_U     = L_AUX + 16384;
constexpr unsigned L_GW    = L_AUX + 25600;
constexpr unsigned L_HX    = 114688;
constexpr unsigned L_DUMMY = L_AUX + 26624;
constexpr unsigned L_CNT   = 163840 - 32;
constexpr unsigned L_VX    = 81920;
constexpr unsigned L_W256  = 81920 + 16384;
constexpr unsigned L_HRING = 81920;
constexpr int NWS = 5, NHS = 5;

template <int N> struct IC { static constexpr int v = N; };
DI unsigned lds_inc(unsigned addr, int l) {
  unsigned old = 0;
  if (l == 0) asm volatile("ds_add_rtn_u32 %0, %1, %2\n\ts_waitcnt lgkmcnt(0)" : "=&v"(old) : "v"(addr), "v"(1u) : "memory");
  return __builtin_amdgcn_readfirstlane(old);
}
DI void lds_spin(unsigned addr, unsigned n) { while (__builtin_amdgcn_readfirstlane(__hip_atomic_load((LAS unsigned*)addr, __ATOMIC_RELAXED, __HIP_MEMORY_SCOPE_WORKGROUP)) < n) __builtin_amdgcn_s_sleep(1); asm volatile("" ::: "memory"); }

__global__ __launch_bounds__(512, 2) void fused_kernel(Params p) {
  extern __shared__ __attribute__((aligned(16))) char smem[];
  const unsigned ldsb = (unsigned)(size_t)(char LAS*)smem;
  char LAS* lds = (char LAS*)smem;
  const int b = blockIdx.x, tid = threadIdx.x;
  const int w = __builtin_amdgcn_readfirstlane(tid >> 6), l = tid & 63;
  const int xcd = b & 7, jx = b >> 3;
  const bool has_m1 = jx < 28;
  const int rt = 4 * (xcd >> 1) + jx / 7, ct1 = 7 * (xcd & 1) + jx % 7;
  const int s0 = 128 * rt, f0 = 128 * ct1;
  const int c = l & 15, q = l >> 4;
  STAMP_DECL
  STAMP(0);

  const int n2 = jx, gq = xcd, pj = xcd * 32 + jx, pr4 = w & 3, t1 = w >> 2;
  const unsigned xrw = __builtin_amdgcn_readfirstlane(ldsb + L_XROWS + w * 2048);
  { const char* xr = (const char*)p.ax + (size_t)(n2 + 256 * gq + 64 * pr4) * 1024;
    glds(l * 16, xr, xrw); glds(l * 16, xr + 32 * 1024, xrw + 1024); }
  if (w == 7) glds(l * 16, p.gw, ldsb + L_GW);
  if (w == 6) asm volatile("s_mov_b32 m0, %1\n\ts_mov_b64 exec, 1\n\tglobal_load_lds_dword %0, %2 sc1\n\ts_mov_b64 exec, -1" :: "v"(FL_E), "s"(ldsb + L_CNT + 12), "s"(p.ws) : "memory");
  unsigned vx[2], vw[2];
  #pragma unroll
  for (int i = 0; i < 2; ++i) { const int row = 8 * (2 * w + i) + (l >> 3); vx[i] = row * 1024 + ((l & 7) ^ ((row >> 1) & 7)) * 16;
    const int r2 = 4 * (2 * w + i) + (l >> 4); vw[i] = r2 * (F_ * 2) + ((l & 15) ^ ((r2 & 3) << 2)) * 16; }
  const char* xbase = (const char*)p.ax + (size_t)s0 * 1024;
  const char* w1base = (const char*)p.w1 + f0 * 2;
  const unsigned rot1 = (jx % 7 + 2 * (jx / 7)) & 7;
  const unsigned m1dst = __builtin_amdgcn_readfirstlane(ldsb + w * 2048);
  #define M1_ISSUE(s, i) do { const unsigned ks_ = ((s) + rot1) & 7; \
      if ((i) < 2) glds(vx[(i) & 1] + ks_ * 128, xbase, m1dst + ((s) & 3) * 32768 + ((i) & 1) * 1024); \
      else glds(vw[(i) & 1] + ks_ * (64 * F_ * 2), w1base, m1dst + ((s) & 3) * 32768 + 16384 + ((i) & 1) * 1024); } while (0)
  #define M1_STAGE(s) do { M1_ISSUE(s, 0); M1_ISSUE(s, 2); M1_ISSUE(s, 1); M1_ISSUE(s, 3); } while (0)
  const int wsl = ((xcd + 8 * (jx - 28)) * 8 + w) & 255, m0 = 32 * (wsl >> 2), kq = wsl & 3;
  const unsigned wscr = __builtin_amdgcn_readfirstlane(ldsb + L_WSCR + w * 8192);
  unsigned er = 0, ei = 0;
  if (!has_m1 && kq == 3) { const unsigned vo = (m0 + (l & 31)) * (DF * 2) + 512;
    asm volatile("global_load_ushort %0, %2, %3\n\tglobal_load_ushort %1, %2, %4" : "=&v"(er), "=&v"(ei) : "v"(vo), "s"(p.wr), "s"(p.wi) : "memory"); }
  const rsrc_t wsr = __builtin_amdgcn_make_buffer_rsrc(p.ws, 0, WS_END, 0x00020000);
  if (tid < 8 && tid != 3) { *(LAS unsigned*)(lds + L_CNT + tid * 4) = 0u; }
  bf16x8 Fc, Fs, Fns, Are, Aim; float p1c[4], p1s[4];
  { const float kf = (float)(16 * t1 + c), k32 = kf * (1.0f / 32.0f), q4 = (float)(4 * q);
    u32x4 uc, us, ur, ui;
    auto wl = [&](int i) { const int row = 8 * (i & 3) + (l >> 3), ch = (l & 7) ^ (((row >> 1) & 3) << 1);
      glds((m0 + row) * (DF * 2) + 128 * kq + ch * 16, i < 4 ? (const char*)p.wr : (const char*)p.wi, wscr + i * 1024); };
    #pragma unroll
    for (int jj = 0; jj < 4; ++jj) {
      SB(); if (has_m1) M1_ISSUE(0, (jj == 1 ? 2 : jj == 2 ? 1 : jj)); else { wl(2 * jj); wl(2 * jj + 1); } SB();
      float cv[2], sv[2];
      #pragma unroll
      for (int e = 0; e < 2; ++e) { const int j = 2 * jj + e; const float ang = ((float)(j < 4 ? j : 12 + j) + q4) * k32; cv[e] = cos_rev(ang); sv[e] = sin_rev(ang); }
      uc[jj] = pk2(cv[0], cv[1]); us[jj] = pk2(sv[0], sv[1]); PIN(uc[jj]); PIN(us[jj]); }
    u32x4 un = xor4(us, 0x80008000u); PIN(un);
    Fc = as8(uc); Fs = as8(us); Fns = as8(un);
    #pragma unroll
    for (int r = 0; r < 4; ++r) {
      if (r < 2) { SB(); if (has_m1) M1_ISSUE(1, 2 + r); SB(); }
      const float nr = q4 + (float)r; const float ang = nr * ((float)c * (1.0f / 16.0f)); const float cv = cos_rev(ang), sv = sin_rev(ang);
      ur[r] = pk2(cv, sv); ui[r] = pk2(-sv, cv);
      const float a2 = nr * (kf * (1.0f / 512.0f)); p1c[r] = 0.5f * cos_rev(a2); p1s[r] = 0.5f * sin_rev(a2); PIN(ur[r]); PIN(ui[r]); PIN(p1c[r]); PIN(p1s[r]); }
    Are = as8(ur); Aim = as8(ui); }
  STAMP(1);
  if (has_m1) { WAIT_VM(6); } else { WAIT_VM(8); }
  STAMP(2);
  u32x4 xv;
  { const unsigned xa = xrw + l * 8, xb = xa + 1024;
    const bf16x8 A_a = cat4(tr_read(xa), tr_read(xa + 512)), A_b = cat4(tr_read(xb), tr_read(xb + 512));
    xv = __builtin_bit_cast(u32x4, lds_read16(xrw + t1 * 1024 + l * 16));
    f32x4 Tr = {0, 0, 0, 0}, Ti = {0, 0, 0, 0};
    Tr = mfma16(A_a, Fc, Tr); Ti = mfma16(A_a, Fns, Ti);
    Tr = mfma16(A_b, Fs, Tr); Ti = mfma16(A_b, Fc, Ti);
    PIN(xv); SB();
    unsigned tb[4];
    #pragma unroll
    for (int r = 0; r < 4; ++r) { const float re = Tr[r] * p1c[r] + Ti[r] * p1s[r], im = Ti[r] * p1c[r] - Tr[r] * p1s[r]; tb[r] = pk2(re, im); }
    const bf16x8 Bt = mk8(tb[0], tb[1], tb[2], tb[3]);
    f32x4 Zr = {0, 0, 0, 0}, Zi = {0, 0, 0, 0};
    Zr = mfma16(Are, Bt, Zr); Zi = mfma16(Aim, Bt, Zi);
    #pragma unroll
    for (int r = 0; r < 4; ++r) *(LAS f32x2*)(size_t)(xrw + ((4 * q + r) * 16 + c) * 8) = (f32x2){Zr[r], Zi[r]};
    BAR();
    STAMP(3);
    if (has_m1) { M1_ISSUE(1, 0); M1_ISSUE(1, 1); }
    {
      const bool sp = tid == 1;
      const int k = tid >> 1, c2 = tid & 1, k1 = sp ? 256 : k, k2 = sp ? 256 : (512 - k) & 511; f32x2 z1[4], z2[4];
      const unsigned a1 = ldsb + L_XROWS + ((k1 >> 4) & 1) * 8192 + ((k1 >> 5) * 16 + (k1 & 15)) * 8, a2 = ldsb + L_XROWS + ((k2 >> 4) & 1) * 8192 + ((k2 >> 5) * 16 + (k2 & 15)) * 8;
      #pragma unroll
      for (int pp = 0; pp < 4; ++pp) { z1[pp] = *(const LAS f32x2*)(size_t)(a1 + pp * 2048); z2[pp] = *(const LAS f32x2*)(size_t)(a2 + pp * 2048); }
      unsigned o[4];
      #pragma unroll
      for (int pp = 0; pp < 4; ++pp) o[pp] = (c2 == 0 || sp) ? pk2(z1[pp][0] + z2[pp][0], z1[pp][1] + z2[pp][1]) : pk2(z1[pp][1] - z2[pp][1], z2[pp][0] - z1[pp][0]);
      st16_wt((u32x4){o[0], o[1], o[2], o[3]}, wsr, WS_X + pj * XJ_STRIDE + k * 32 + c2 * 16);
    }
  }
  const unsigned epoch = __builtin_amdgcn_readfirstlane(lds_read4(ldsb + L_CNT + 12)) + 1u;
  STAMP(4);

  const bool p2wave = w < 4;
  const int pw = w & 3;
  const int kcol = xcd * 32 + jx;
  const int ct = jx & 7, pr = 4 * xcd + (jx >> 3);
  u32x4 gc0, gs0, hc0, hs0; float twc[8], tws[8];
  auto p2consts = [&]() {
    const float k1f = (float)(16 * pw + c), q4f = (float)(4 * q), k64 = k1f * (1.0f / 64.0f), c32 = (float)c * (1.0f / 32.0f), k2k = k1f * (1.0f / 2048.0f);
    #pragma unroll
    for (int i = 0; i < 4; ++i) { const float a0 = (2.0f * q4f + (float)(2 * i)) * k64, a1 = (2.0f * q4f + (float)(2 * i + 1)) * k64;
      gc0[i] = pk2(cos_rev(a0), cos_rev(a1)); gs0[i] = pk2(sin_rev(a0), sin_rev(a1));
      const float b0 = (q4f + (float)(2 * i < 4 ? 2 * i : 12 + 2 * i)) * c32, b1 = (q4f + (float)(2 * i + 1 < 4 ? 2 * i + 1 : 13 + 2 * i)) * c32;
      hc0[i] = pk2(cos_rev(b0), cos_rev(b1)); hs0[i] = pk2(sin_rev(b0), sin_rev(b1)); }
    #pragma unroll
    for (int i = 0; i < 8; ++i) { const float a = (q4f + (float)(16 * (i >> 2) + (i & 3))) * k2k; twc[i] = cos_rev(a); tws[i] = sin_rev(a); }
    PIN(gc0); PIN(gs0); PIN(hc0); PIN(hs0);
    #pragma unroll
    for (int i = 0; i < 8; ++i) { PIN(twc[i]); PIN(tws[i]); } };
  const unsigned cnt = ldsb + L_CNT;
  auto xgather = [&](int half) { glds_sc1((64 * pw + l) * XJ_STRIDE + half * 16, p.ws + WS_X + kcol * 32, __builtin_amdgcn_readfirstlane(ldsb + L_XCOL + pw * 1024 + half * 4096)); };
  unsigned Wv[8];
  auto wload = [&]() {
    const char* wb = p.ws + WS_WT + (size_t)(kcol >> 6) * (64 * 8192) + (kcol & 63) * 64;
    #pragma unroll
    for (int i = 0; i < 8; ++i) gload4_sc1(Wv[i], ((pw >> 1) + 2 * (16 * (i >> 2) + 4 * q + (i & 3))) * 8192 + (pw & 1) * 4096 + c * 4, wb);
    if (b == 0) { const unsigned wofs = __builtin_amdgcn_readfirstlane(ldsb + pw * 1024);
      #pragma unroll
      for (int i = 0; i < 2; ++i) { const int piece = 16 * (pw + 4 * i) + (l >> 2);
        glds_sc1((piece >> 1) * 8192 + (piece & 1) * 4096 + (l & 3) * 16, wb, wofs + L_WCOL + i * 4096);
        glds_sc1(i * 4096 + pw * 1024 + l * 16, p.ws + WS_WT + (size_t)256 * 8192, wofs + L_W256 + i * 4096); } } };
  auto h_done = [&]() { if (lds_inc(cnt, l) == 3u) flag_line(p.ws, FL_F2 + (rt * 16 + ct1) * FS, epoch, l); };
  auto gate = [&]() {
    const u32x4 gwv = __builtin_bit_cast(u32x4, lds_read16(ldsb + L_GW + l * 16));
    float sum = 0.f;
    #pragma unroll
    for (int i = 0; i < 4; ++i) { sum += rbf(bflo(xv[i]) * bflo(gwv[i])); sum += rbf(bfhi(xv[i]) * bfhi(gwv[i])); }
    sum += __builtin_bit_cast(float, __builtin_amdgcn_update_dpp(0, __builtin_bit_cast(int, sum), 0x111, 0xf, 0xf, true));
    sum += __builtin_bit_cast(float, __builtin_amdgcn_update_dpp(0, __builtin_bit_cast(int, sum), 0x112, 0xf, 0xf, true));
    sum += __builtin_bit_cast(float, __builtin_amdgcn_update_dpp(0, __builtin_bit_cast(int, sum), 0x114, 0xf, 0xf, true));
    sum += __builtin_bit_cast(float, __builtin_amdgcn_update_dpp(0, __builtin_bit_cast(int, sum), 0x118, 0xf, 0xf, true));
    sum += __builtin_bit_cast(float, __builtin_amdgcn_update_dpp(0, __builtin_bit_cast(int, sum), 0x142, 0xa, 0xf, true));
    sum += __builtin_bit_cast(float, __builtin_amdgcn_update_dpp(0, __builtin_bit_cast(int, sum), 0x143, 0xc, 0xf, true));
    const float gg = rbf(__builtin_amdgcn_rcpf(1.0f + __expf(-rbf(sum))));
    st4_wt(__float_as_uint(gg), wsr, l == 63 ? WS_G + (n2 + 256 * gq + 64 * pr4 + 32 * t1) * 4 : OOB); };

  const unsigned vrow = (l >> 3) * 1024 + ((l & 7) ^ (4 * ((l >> 4) & 1))) * 16;
  const unsigned vrowY = (l >> 3) * YROW + ((l & 7) ^ (4 * ((l >> 4) & 1))) * 16;
  #define M2_STAGE(u) (u)
  const unsigned w2dst = __builtin_amdgcn_readfirstlane(ldsb + (w & 1) * 8192), vw2 = vrow + (w & 1) * 896 * 1024 + ct * 128;
  #define M2_ISSUE_W2(u, j) glds(vw2 + (j) * 8192 + M2_STAGE(u) * 65536, p.w2, w2dst + ((u) % NWS) * 16384 + (j) * 1024)
  auto issue_w_prologue = [&]() {
    #pragma unroll
    for (int u = 0; u < NWS; ++u)
      #pragma unroll
      for (int j = 0; j < 8; ++j) M2_ISSUE_W2(u, j); };

  if (has_m1) {
    f32x16 acc1[2] = {};
    const int fw = w & 1, sw = w >> 1;
    unsigned xo[2][4], wo0[2], wo1[2];
    { const int h = l >> 5, blk = (l >> 4) & 1, a = (l & 15) >> 2, pp = l & 3, xrow = 32 * sw + (l & 31);
      #pragma unroll
      for (int hh = 0; hh < 2; ++hh) {
        #pragma unroll
        for (int ks = 0; ks < 4; ++ks) { xo[hh][ks] = ldsb + hh * 65536 + xrow * 128 + 16 * ((2 * ks + h) ^ ((xrow >> 1) & 7)); PIN(xo[hh][ks]); }
        wo0[hh] = ldsb + hh * 65536 + 16384 + (8 * h + a) * 256 + 16 * (((8 * fw + 0) ^ (a << 2)) + 2 * blk + (pp >> 1)) + 8 * (pp & 1); PIN(wo0[hh]);
        wo1[hh] = ldsb + hh * 65536 + 16384 + (8 * h + a) * 256 + 16 * (((8 * fw + 4) ^ (a << 2)) + 2 * blk + (pp >> 1)) + 8 * (pp & 1); PIN(wo1[hh]); } }
    struct Fr { bf16x8 B, A0, A1; } fa, fb;
    #define RB(f, g)  f.B  = lds_read16(xo[(((g) >> 2) & 3) >> 1][(g) & 3] + (((g) >> 2) & 1) * 32768)
    #define RA(base_, g) cat4(tr_read(base_[(((g) >> 2) & 3) >> 1] + (((g) >> 2) & 1) * 32768 + ((g) & 3) * 4096), tr_read(base_[(((g) >> 2) & 3) >> 1] + (((g) >> 2) & 1) * 32768 + ((g) & 3) * 4096 + 1024))
    SB();
    M1_STAGE(2);
    if (p2wave) p2consts();
    WAIT_VM(5);
    BARE_BAR();
    STAMP(5);
    RB(fa, 0); fa.A0 = RA(wo0, 0); fa.A1 = RA(wo1, 0);
    unsigned f1v = 0;
    auto kstep = [&](auto G, Fr& cur, Fr& nxt) {
      constexpr int g = decltype(G)::v, st = g >> 2, ks = g & 3;
      if constexpr (ks == 0 && st > 0) {
        if constexpr (st == 1) { WAIT_VM(4); }
        else if constexpr (st <= 3) { if (w == 0) { WAIT_VM(5); } else { WAIT_VM(4); } }
        else if constexpr (st <= 5) { WAIT_VM(4); }
        else if constexpr (st == 6) { if (w < 4) { WAIT_VM(0); PIN(f1v); if (!__all(f1v == epoch)) spin4(p.ws, FL_F1 + (64 * w + l) * FS, epoch, l); } else { WAIT_VM(2); } }
        BARE_BAR();
        STAMP(20 + st);
        if constexpr (st == 5) { if (w < 4) gload4_sc1(f1v, (64 * w + l) * FS, p.ws + FL_F1); }
      }
      acc1[0] = mfma32(cur.A0, cur.B, acc1[0]); SB();
      if constexpr (g + 1 < 32) { RB(nxt, g + 1); nxt.A0 = RA(wo0, g + 1); }
      SB();
      acc1[1] = mfma32(cur.A1, cur.B, acc1[1]); SB();
      if constexpr (g + 1 < 32) { nxt.A1 = RA(wo1, g + 1); }
      if constexpr (st <= 4) { M1_ISSUE(st + 3, ks); }
      if constexpr (g == 7) { if (w == 0) flag_line(p.ws, FL_F1 + b * FS, epoch, l); }
      if constexpr (g == 24 || g == 25) { if (w < 4) xgather(g & 1); }
      if constexpr (g == 20 || g == 21) { if (w >= 4) glds(l * 16, (const char*)p.w2 + (size_t)((xcd * 28 + jx) * 8 + (w & 3) * 2 + (g & 1)) * 1024, ldsb + L_DUMMY + (w & 3) * 1024); }
      SB();
    };
    #define K2(g) kstep(IC<g>{}, fa, fb); kstep(IC<g + 1>{}, fb, fa);
    K2(0) K2(2) K2(4) K2(6) K2(8) K2(10) K2(12) K2(14) K2(16) K2(18) K2(20) K2(22) K2(24) K2(26) K2(28) K2(30)
    STAMP(6);
    BARE_BAR();
    unsigned fwv = 0;
    if (w < 4) gload4_sc1(fwv, (l & 31) * FS, p.ws + FL_FW);
    STAMP(7);
    u32x4 hf[4];
    #pragma unroll
    for (int ft = 0; ft < 2; ++ft)
      #pragma unroll
      for (int u = 0; u < 2; ++u) {
        #pragma unroll
        for (int jj = 0; jj < 4; ++jj) {
          const f32x2 v = {acc1[ft][8 * u + 2 * jj], acc1[ft][8 * u + 2 * jj + 1]};
          const f32x2 t = v * -1.44269504088896340736f;
          const f32x2 e = (f32x2){__builtin_amdgcn_exp2f(t[0]), __builtin_amdgcn_exp2f(t[1])} + 1.0f;
          const f32x2 r = v * (f32x2){__builtin_amdgcn_rcpf(e[0]), __builtin_amdgcn_rcpf(e[1])};
          hf[2 * ft + u][jj] = pk2(r[0], r[1]);
        }
        if (w >= 4) st16_wt(hf[2 * ft + u], wsr, WS_H + ((((s0 >> 5) + sw) * 112 + ((f0 + 64 * fw + 32 * ft + 16 * u) >> 4)) * 64 + l) * 16);
        else *(LAS u32x4*)(size_t)(ldsb + L_HX + (w * 4 + 2 * ft + u) * 1024 + l * 16) = hf[2 * ft + u];
      }
    SB();
    STAMP(8);
    if (w < 4) { WAIT_LGKM0(); lds_inc(cnt + 28 + 0, l);
      WAIT_VM(0); PIN(fwv); if (!__all(fwv == epoch)) spin4(p.ws, FL_FW + (l & 31) * FS, epoch, l);
      wload(); }
    else {
      lds_spin(cnt + 28, 4u);
      const int pw4 = w - 4, psw = pw4 >> 1, pfw = pw4 & 1;
      #pragma unroll
      for (int i = 0; i < 4; ++i)
        st16_wt(*(const LAS u32x4*)(size_t)(ldsb + L_HX + (pw4 * 4 + i) * 1024 + l * 16), wsr, WS_H + ((((s0 >> 5) + psw) * 112 + ((f0 + 64 * pfw + 16 * i) >> 4)) * 64 + l) * 16);
      WAIT_VM(0);
      h_done();
      if (w < 6) issue_w_prologue();
    }
  } else {
    WAIT_VM(0); BAR();
    if (w == 0) flag_line(p.ws, FL_F1 + b * FS, epoch, l);
    STAMP(18);
    { PIN(er); PIN(ei);
      const int a = (l & 15) >> 2, pp = l & 3;
      #pragma unroll
      for (int rh = 0; rh < 2; ++rh)
        #pragma unroll
        for (int cb = 0; cb < 4; ++cb) { const int row = 16 * rh + 4 * q + a;
          const unsigned ad = wscr + row * 128 + ((cb ^ ((row >> 1) & 3)) * 32) + pp * 8;
          const u32x2 vr = __builtin_bit_cast(u32x2, tr_read(ad)), vi = __builtin_bit_cast(u32x2, tr_read(ad + 4096));
          const u32x4 o = {__builtin_amdgcn_perm(vi[0], vr[0], 0x05040100u), __builtin_amdgcn_perm(vi[0], vr[0], 0x07060302u), __builtin_amdgcn_perm(vi[1], vr[1], 0x05040100u), __builtin_amdgcn_perm(vi[1], vr[1], 0x07060302u)};
          st16_wt(o, wsr, WS_WT + (kq * 64 + (m0 >> 5)) * 8192 + rh * 4096 + cb * 1024 + c * 64 + q * 16); }
      st4_wt(er | (ei << 16), wsr, (kq == 3 && l < 32) ? WS_WT + 256 * 8192 + (m0 + l) * 4 : OOB); }
    if (w == 1 && jx == 28 && l < 32) { __hip_atomic_store((GAS unsigned*)(p.ws + FL_F3 + (xcd * 2 + 0) * FS + l * 4), 0u, __ATOMIC_RELAXED, __HIP_MEMORY_SCOPE_AGENT);
                                        __hip_atomic_store((GAS unsigned*)(p.ws + FL_F3 + (xcd * 2 + 1) * FS + l * 4), 0u, __ATOMIC_RELAXED, __HIP_MEMORY_SCOPE_AGENT); }
    STAMP(19);
    WAIT_VM(0); BAR();
    STAMP(14);
    if (w == 0) flag_line(p.ws, FL_FW + (xcd + 8 * (jx - 28)) * FS, epoch, l);
    if (w >= 4) {
      unsigned long long pc; unsigned rest;
      asm volatile("s_getpc_b64 %0\nfused_pc_label:\n\ts_mov_b32 %1, fused_end_label-fused_pc_label" : "=s"(pc), "=s"(rest));
      const char* code = (const char*)(pc & ~127ull);
      #pragma unroll
      for (int i = 0; i < 6; ++i) { const unsigned o = (unsigned)(i * 4 + (w & 3)) * 1024u; if (o + 1024u <= rest) glds(l * 16, code + o, ldsb + L_DUMMY + (w & 3) * 1024); }
    }
    if (w < 4) { p2consts(); spin4(p.ws, FL_F1 + (64 * w + l) * FS, epoch, l);
      lds_inc(cnt + 20, l); lds_spin(cnt + 20, 4u);
      xgather(0); xgather(1);
      spin4(p.ws, FL_FW + (l & 31) * FS, epoch, l);
      wload(); }
    else if (w < 6) issue_w_prologue();
    STAMP(8);
  }
  STAMP(9);
  if (p2wave) {
    { const unsigned gc = cnt + 20; lds_inc(gc, l); lds_spin(gc, has_m1 ? 4u : 8u); }
    const unsigned xc = ldsb + L_XCOL, wc = ldsb + L_WCOL, ub = ldsb + L_U;
    bf16x8 Zr[2][2], Zi[2][2];
    #pragma unroll
    for (int u = 0; u < 2; ++u)
      #pragma unroll
      for (int mt = 0; mt < 2; ++mt) { const unsigned ad = xc + ((4 * u + q) * 32 + 16 * mt + c) * 16; Zr[mt][u] = lds_read16(ad); Zi[mt][u] = lds_read16(ad + 4096); }
    const unsigned sgn = (c & 1) ? 0x80008000u : 0u;
    u32x4 gc1 = xor4(gc0, sgn), gs1 = xor4(gs0, sgn), ngs0 = xor4(gs0, 0x80008000u), ngs1 = xor4(gs0, sgn ^ 0x80008000u);
    u32x4 hc1 = xor4(hc0, 0x80000000u), hs1 = xor4(hs0, 0x80000000u), nhs0 = xor4(hs0, 0x80008000u), nhs1 = xor4(hs0, 0x00008000u);
    PIN(gc1); PIN(gs1); PIN(ngs0); PIN(ngs1); PIN(hc1); PIN(hs1); PIN(nhs0); PIN(nhs1);
    const bf16x8 Gc[2] = {as8(gc0), as8(gc1)}, Gs[2] = {as8(gs0), as8(gs1)}, nGs[2] = {as8(ngs0), as8(ngs1)};
    const bf16x8 Hc[2] = {as8(hc0), as8(hc1)}, Hs[2] = {as8(hs0), as8(hs1)}, nHs[2] = {as8(nhs0), as8(nhs1)};
    f32x4 Dr[2] = {}, Di[2] = {};
    #pragma unroll
    for (int u = 0; u < 2; ++u)
      #pragma unroll
      for (int mt = 0; mt < 2; ++mt) {
        Dr[mt] = mfma16(Zr[mt][u], Gc[u], Dr[mt]); Di[mt] = mfma16(Zi[mt][u], Gc[u], Di[mt]);
        Dr[mt] = mfma16(Zi[mt][u], Gs[u], Dr[mt]); Di[mt] = mfma16(Zr[mt][u], nGs[u], Di[mt]);
      }
    SB(); lds_inc(cnt + 24, l); SB();
    u32x4 tr_, ti_;
    { float re[8], im[8];
      #pragma unroll
      for (int i = 0; i < 8; ++i) { const float x = Dr[i >> 2][i & 3], y = Di[i >> 2][i & 3]; re[i] = x * twc[i] + y * tws[i]; im[i] = y * twc[i] - x * tws[i]; }
      #pragma unroll
      for (int jj = 0; jj < 4; ++jj) { tr_[jj] = pk2(re[2 * jj], re[2 * jj + 1]); ti_[jj] = pk2(im[2 * jj], im[2 * jj + 1]); } }
    f32x4 Vr[2] = {}, Vi[2] = {};
    { const bf16x8 Tr = as8(tr_), Ti = as8(ti_);
      #pragma unroll
      for (int t = 0; t < 2; ++t) { Vr[t] = mfma16(Hc[t], Tr, Vr[t]); Vi[t] = mfma16(Hc[t], Ti, Vi[t]); }
      #pragma unroll
      for (int t = 0; t < 2; ++t) { Vr[t] = mfma16(Hs[t], Ti, Vr[t]); Vi[t] = mfma16(nHs[t], Tr, Vi[t]); } }
    WAIT_VM(0);
    #pragma unroll
    for (int i = 0; i < 8; ++i) PIN(Wv[i]);
    if (kcol != 0) { float re[8], im[8];
      #pragma unroll
      for (int i = 0; i < 8; ++i) { const float x = Vr[i >> 2][i & 3], y = Vi[i >> 2][i & 3], wr = bflo(Wv[i]), wi = bfhi(Wv[i]);
        re[i] = x * wr - y * wi; im[i] = x * wi + y * wr; }
      #pragma unroll
      for (int jj = 0; jj < 4; ++jj) { tr_[jj] = pk2(re[2 * jj], re[2 * jj + 1]); ti_[jj] = pk2(im[2 * jj], im[2 * jj + 1]); }
    } else {
      const unsigned vx = ldsb + L_VX, w2c = ldsb + L_W256;
      #pragma unroll
      for (int i = 0; i < 8; ++i) { const int m = 16 * pw + c + 64 * (16 * (i >> 2) + 4 * q + (i & 3)); *(LAS f32x2*)(size_t)(vx + m * 8) = (f32x2){Vr[i >> 2][i & 3], Vi[i >> 2][i & 3]}; }
      WAIT_LGKM0();
      lds_inc(cnt + 8, l); lds_spin(cnt + 8, 4u);
      float re[8], im[8];
      #pragma unroll
      for (int i = 0; i < 8; ++i) { const int m = 16 * pw + c + 64 * (16 * (i >> 2) + 4 * q + (i & 3)), mm = (2048 - m) & 2047;
        const f32x2 zm = *(const LAS f32x2*)(size_t)(vx + mm * 8);
        const unsigned a0 = Wv[i], a1 = lds_read4(wc + mm * 4), b0 = lds_read4(w2c + m * 4), b1 = lds_read4(w2c + mm * 4);
        const float Ar = 0.5f * (bflo(a0) + bflo(a1)), Ai = 0.5f * (bfhi(a0) - bfhi(a1)), Br = 0.5f * (bflo(b0) + bflo(b1)), Bi = 0.5f * (bfhi(b0) - bfhi(b1));
        const float Pr = 0.5f * (Ar + Br), Pi = 0.5f * (Ai + Bi), Qr = 0.5f * (Ar - Br), Qi = 0.5f * (Ai - Bi);
        const float x = Vr[i >> 2][i & 3], y = Vi[i >> 2][i & 3], u = zm[0], v = -zm[1];
        re[i] = x * Pr - y * Pi + u * Qr - v * Qi; im[i] = x * Pi + y * Pr + u * Qi + v * Qr; }
      #pragma unroll
      for (int jj = 0; jj < 4; ++jj) { tr_[jj] = pk2(re[2 * jj], re[2 * jj + 1]); ti_[jj] = pk2(im[2 * jj], im[2 * jj + 1]); }
    }
    f32x4 Ur[2] = {}, Ui[2] = {};
    { const bf16x8 Tr = as8(tr_), Ti = as8(ti_);
      #pragma unroll
      for (int t = 0; t < 2; ++t) { Ur[t] = mfma16(Hc[t], Tr, Ur[t]); Ui[t] = mfma16(Hc[t], Ti, Ui[t]); }
      #pragma unroll
      for (int t = 0; t < 2; ++t) { Ur[t] = mfma16(nHs[t], Ti, Ur[t]); Ui[t] = mfma16(Hs[t], Tr, Ui[t]); } }
    #pragma unroll
    for (int t = 0; t < 2; ++t) { float re[4], im[4];
      #pragma unroll
      for (int r = 0; r < 4; ++r) { const float x = Ur[t][r], y = Ui[t][r]; re[r] = x * twc[4 * t + r] - y * tws[4 * t + r]; im[r] = y * twc[4 * t + r] + x * tws[4 * t + r]; }
      char LAS* dst = (char LAS*)(size_t)ub + (16 * pw + c) * 72 + (16 * t + 4 * q) * 2;
      *(LAS u32x2*)dst = (u32x2){pk2(re[0], re[1]), pk2(re[2], re[3])};
      *(LAS u32x2*)(dst + 64 * 72) = (u32x2){pk2(im[0], im[1]), pk2(im[2], im[3])}; }
    WAIT_LGKM0();
    { lds_inc(cnt + 4, l); lds_spin(cnt + 4, 4u); }
    STAMP(10);
    f32x4 yr[2] = {}, yi[2] = {};
    const int a = (l & 15) >> 2, pp = l & 3;
    #pragma unroll
    for (int u = 0; u < 2; ++u)
      #pragma unroll
      for (int t = 0; t < 2; ++t) {
        const unsigned ad = ub + (32 * u + 8 * q + a) * 72 + (8 * pp + 4 * t) * 2;
        const bf16x8 Ar = cat4(tr_read(ad), tr_read(ad + 4 * 72));
        const bf16x8 Ai = cat4(tr_read(ad + 64 * 72), tr_read(ad + 64 * 72 + 4 * 72));
        yr[t] = mfma16(Ar, Gc[u], yr[t]); yi[t] = mfma16(Ai, Gc[u], yi[t]);
        yr[t] = mfma16(Ai, nGs[u], yr[t]); yi[t] = mfma16(Ar, Gs[u], yi[t]);
      }
    const float ysc = kcol == 0 ? 0.5f : 1.0f;
    const u32x4 yre = {pk2(yr[0][0] * ysc, yr[0][1] * ysc), pk2(yr[0][2] * ysc, yr[0][3] * ysc), pk2(yr[1][0] * ysc, yr[1][1] * ysc), pk2(yr[1][2] * ysc, yr[1][3] * ysc)};
    const u32x4 yim = {pk2(yi[0][0] * ysc, yi[0][1] * ysc), pk2(yi[0][2] * ysc, yi[0][3] * ysc), pk2(yi[1][0] * ysc, yi[1][1] * ysc), pk2(yi[1][2] * ysc, yi[1][3] * ysc)};
    const unsigned so = (32 * (16 * pw + c) + 8 * q) * 2;
    st16_wt(yre, wsr, WS_Y + kcol * YROW + so);
    st16_wt(yim, wsr, WS_Y + (256 + kcol) * YROW + so);
    gate();
    STAMP(11);
  }
  if (!p2wave) gate();
  unsigned vst[4];
  #pragma unroll
  for (int i = 0; i < 4; ++i)
    vst[i] = w < 4 ? vrow + ((i >> 1) * 896 + (w & 3) * 16 + (i & 1) * 8) * 1024 + ct * 128
                   : l * 16 + ((2 * pr + ((w & 3) >> 1)) * 112 + (i >> 1) * 56 + 2 * (w & 1) + (i & 1)) * 1024;
  const char* stbase = w < 4 ? (const char*)p.w2 : p.ws + WS_H;
  const unsigned stmul = w < 4 ? 65536u : 4096u;
  const unsigned stdst = __builtin_amdgcn_readfirstlane(ldsb + (w < 4 ? 0u : L_HRING) + (w & 3) * 2048);
  #define M2_ISSUE(u, i) glds_sc1(vst[i] + M2_STAGE(u) * stmul, stbase, stdst + ((u) % 5) * 16384 + ((i) >> 1) * 8192 + ((i) & 1) * 1024)
  const unsigned vy8 = vrowY + ((w >> 2) * 256 + (w & 3) * 16) * YROW + pr * 128;
  const unsigned y8dst = __builtin_amdgcn_readfirstlane(ldsb + L_HRING + (w >> 2) * 8192 + (w & 3) * 2048);
  #define M2_ISSUE_Y(u, i) glds_sc1(vy8 + (((u) - 14) * 64 + (i) * 8) * YROW, p.ws + WS_Y, y8dst + ((u) % 5) * 16384 + (i) * 1024)
  if (w >= 4) {
    if (w == 6) { spin4(p.ws, FL_F2 + ((pr >> 1) * 16 + (l < 14 ? l : 0)) * FS, epoch, l); lds_inc(cnt + 16, l); }
    lds_spin(cnt + 16, 1u);
    if (has_m1) lds_spin(cnt, 4u);
    if (b == 0) lds_spin(cnt + 4, 4u);
    STAMP(12);
    #pragma unroll
    for (int u = 0; u < 3; ++u) { M2_ISSUE(u, 0); M2_ISSUE(u, 1); M2_ISSUE(u, 2); M2_ISSUE(u, 3); }
    lds_spin(cnt + 24, 4u);
    M2_ISSUE(3, 0); M2_ISSUE(3, 1); M2_ISSUE(3, 2); M2_ISSUE(3, 3);
    WAIT_VM(12);
  } else { WAIT_VM(0); }
  BAR();
  if (tid == 0) __hip_atomic_fetch_add((GAS unsigned*)(p.ws + FL_F3 + (b & 15) * FS), 1u, __ATOMIC_RELAXED, __HIP_MEMORY_SCOPE_AGENT);
  if (w >= 4) { M2_ISSUE(4, 0); M2_ISSUE(4, 1); M2_ISSUE(4, 2); M2_ISSUE(4, 3); }
  STAMP(13);
  f32x16 accb = {}, acca = {};
  {
    const int kh = w >> 2, nh = (w >> 1) & 1, sh = w & 1;
    const int h = l >> 5, blk = (l >> 4) & 1, a = (l & 15) >> 2, pp = l & 3;
    unsigned aoff[2];
    #pragma unroll
    for (int hh = 0; hh < 2; ++hh) { aoff[hh] = ldsb + hh * 65536 + kh * 8192 + (4 * h + a) * 128 + 16 * ((4 * nh + 2 * (pp & 1) + blk) ^ (4 * (a >> 1))) + 8 * (pp >> 1); PIN(aoff[hh]); }
    unsigned boffH[2], boffY[2];
    #pragma unroll
    for (int hh = 0; hh < 2; ++hh) { boffH[hh] = ldsb + L_HRING + hh * 49152 + kh * 8192 + sh * 4096 + l * 16; PIN(boffH[hh]);
      boffY[hh] = ldsb + L_HRING + hh * 49152 + kh * 8192 + (4 * h + a) * 128 + 16 * ((4 * sh + 2 * blk + (pp >> 1)) ^ (4 * (a >> 1))) + 8 * (pp & 1); PIN(boffY[hh]); }
    const int i31 = l & 31, dn = 64 * ct + 32 * nh + 16 * ((i31 >> 2) & 1) + 4 * (i31 >> 3) + (i31 & 3);
    const float dsc = (kh ? -1.0f : 1.0f) * (float)dn * (1.0f / 512.0f);
    const float cbase = (float)(4 * h) * dsc + (kh ? 0.0f : 0.25f);
    const float c256 = (dn & 1) ? -1.0f : 1.0f;
    u32x4 Cf[16];
    f32x2 cprev[4], ccur[4];
    const float two_c = 2.0f * cos_rev((float)(dn & 31) * (1.0f / 32.0f));
    auto cgen = [&](auto KS) { constexpr int ks = decltype(KS)::v;
      #pragma unroll
      for (int jj = 0; jj < 4; ++jj) { f32x2 v;
        if constexpr (ks < 2) {
          #pragma unroll
          for (int e = 0; e < 2; ++e) { const int j = 2 * jj + e; v[e] = sin_rev(__builtin_fmaf((float)(16 * ks + 8 * (j >> 2) + (j & 3)), dsc, cbase)); }
        } else v = ccur[jj] * two_c - cprev[jj];
        if constexpr (ks > 0) cprev[jj] = ccur[jj];
        ccur[jj] = v;
        if (ks == 0 && jj == 0) v[0] = (kh && h == 0) ? c256 : v[0];
        Cf[ks][jj] = pk2(v[0], v[1]); }
      PIN(Cf[ks]); };
    struct F2 { bf16x8 A[4], B[4]; } fa, fb;
    auto rdA = [&](auto ST, auto KS, F2& f) { constexpr int st = decltype(ST)::v, ks = decltype(KS)::v;
      if constexpr (st < 14) { constexpr unsigned o = (st % NWS) * 16384 + ks * 2048; f.A[ks] = cat4(tr_read(aoff[o >> 16] + (o & 65535)), tr_read(aoff[o >> 16] + (o & 65535) + 1024)); } };
    auto rdB = [&](auto ST, auto KS, F2& f) { constexpr int st = decltype(ST)::v, ks = decltype(KS)::v; constexpr int sl = st % NHS, hi = sl >= 3; constexpr unsigned o = (sl - 3 * hi) * 16384;
      if constexpr (st < 14) f.B[ks] = lds_read16(boffH[hi] + o + ks * 1024);
      else f.B[ks] = cat4(tr_read(boffY[hi] + o + ks * 2048), tr_read(boffY[hi] + o + ks * 2048 + 1024)); };
    rdA(IC<0>{}, IC<0>{}, fa); rdB(IC<0>{}, IC<0>{}, fa); rdA(IC<0>{}, IC<1>{}, fa); rdB(IC<0>{}, IC<1>{}, fa);
    rdA(IC<0>{}, IC<2>{}, fa); rdB(IC<0>{}, IC<2>{}, fa); rdA(IC<0>{}, IC<3>{}, fa); rdB(IC<0>{}, IC<3>{}, fa);
    unsigned f3v = 0;
    unsigned gv = 0;
    auto step = [&](auto ST, F2& cur, F2& nxt) {
      constexpr int st = decltype(ST)::v;
      if constexpr (st <= 8) { WAIT_VM(12); }
      else if constexpr (st == 9) { if (w < 4) { WAIT_VM(8); if (w == 0) { PIN(f3v); if (!__all(f3v == 16u)) spin4(p.ws, FL_F3 + (l & 15) * FS, 16u, l); } } else { WAIT_VM(12); } }
      else if constexpr (st == 10) { WAIT_VM(10); } else if constexpr (st == 11) { WAIT_VM(9); } else if constexpr (st <= 13) { WAIT_VM(7); }
      else if constexpr (st == 14) { WAIT_VM(4); } else if constexpr (st == 15) { WAIT_VM(2); } else if constexpr (st == 16) { WAIT_VM(0); }
      BAR();
      STAMP(30 + st);
      if constexpr (st == 7) { if (w == 0) gload4_sc1(f3v, (l & 15) * FS, p.ws + FL_F3); }
      if constexpr (st == 10) gload4_sc1(gv, (64 * pr + 32 * sh + (l & 31)) * 4, p.ws + WS_G);
      auto mm = [&](auto KS) { constexpr int ks = decltype(KS)::v;
        if constexpr (st < 14) accb = mfma32(cur.A[ks], cur.B[ks], accb); else acca = mfma32(as8(Cf[4 * (st - 14) + ks]), cur.B[ks], acca); };
      auto iss = [&](auto KS) { constexpr int ks = decltype(KS)::v;
        if constexpr (st + 5 < 14) { M2_ISSUE(st + 5, ks); }
        else if constexpr (st + 5 < 18 && ks >= 1 && ks <= 2) { M2_ISSUE_Y(st + 5, ks - 1); } };
      mm(IC<0>{}); SB();
      if constexpr (st < 17) { rdA(IC<st + 1>{}, IC<0>{}, nxt); rdB(IC<st + 1>{}, IC<0>{}, nxt); rdA(IC<st + 1>{}, IC<1>{}, nxt); rdB(IC<st + 1>{}, IC<1>{}, nxt); }
      iss(IC<0>{});
      SB();
      mm(IC<1>{}); SB();
      if constexpr (st < 17) { rdA(IC<st + 1>{}, IC<2>{}, nxt); rdB(IC<st + 1>{}, IC<2>{}, nxt); rdA(IC<st + 1>{}, IC<3>{}, nxt); rdB(IC<st + 1>{}, IC<3>{}, nxt); }
      iss(IC<1>{});
      SB();
      mm(IC<2>{}); SB();
      iss(IC<2>{});
      SB();
      mm(IC<3>{}); SB();
      iss(IC<3>{});
      if constexpr (st < 16) cgen(IC<(st < 16 ? st : 0)>{});
      SB();
    };
    step(IC<0>{}, fa, fb); step(IC<1>{}, fb, fa); step(IC<2>{}, fa, fb); step(IC<3>{}, fb, fa); step(IC<4>{}, fa, fb); step(IC<5>{}, fb, fa);
    step(IC<6>{}, fa, fb); step(IC<7>{}, fb, fa); step(IC<8>{}, fa, fb); step(IC<9>{}, fb, fa); step(IC<10>{}, fa, fb); step(IC<11>{}, fb, fa);
    step(IC<12>{}, fa, fb); step(IC<13>{}, fb, fa); step(IC<14>{}, fa, fb); step(IC<15>{}, fb, fa); step(IC<16>{}, fa, fb); step(IC<17>{}, fb, fa);
    STAMP(15);
    PIN(gv);
    if (b == 0 && w == 0) flag_line(p.ws, FL_E, epoch, l);
    LAS f32x4* ex = (LAS f32x4*)lds;
    float av[8], bv[8];
    auto fin = [&](auto KH) { constexpr int o = 8 * (1 - decltype(KH)::v), m = 8 * decltype(KH)::v;
      ex[(w * 4 + 0) * 64 + l] = (f32x4){accb[o], accb[o + 1], accb[o + 2], accb[o + 3]};
      ex[(w * 4 + 1) * 64 + l] = (f32x4){accb[o + 4], accb[o + 5], accb[o + 6], accb[o + 7]};
      ex[(w * 4 + 2) * 64 + l] = (f32x4){acca[o], acca[o + 1], acca[o + 2], acca[o + 3]};
      ex[(w * 4 + 3) * 64 + l] = (f32x4){acca[o + 4], acca[o + 5], acca[o + 6], acca[o + 7]};
      #pragma unroll
      for (int i = 0; i < 8; ++i) { av[i] = acca[m + i]; bv[i] = accb[m + i]; } };
    if (kh) fin(IC<1>{}); else fin(IC<0>{});
    BAR();
    const int pwv = w ^ 4;
    const f32x4 b0 = ex[(pwv * 4 + 0) * 64 + l], b1 = ex[(pwv * 4 + 1) * 64 + l], a0 = ex[(pwv * 4 + 2) * 64 + l], a1 = ex[(pwv * 4 + 3) * 64 + l];
    const int s = 64 * pr + 32 * sh + (l & 31), n = 64 * ct + 32 * nh + 16 * h + 8 * kh;
    const float g = __uint_as_float(gv), omg = rbf(1.0f - g);
    constexpr float ASC = 2.0f / 1048576.0f;
    unsigned o4[4];
    #pragma unroll
    for (int i = 0; i < 4; ++i) {
      const float a_0 = av[2 * i] + (i < 2 ? a0[2 * i] : a1[2 * i - 4]), a_1 = av[2 * i + 1] + (i < 2 ? a0[2 * i + 1] : a1[2 * i - 3]);
      const float b_0 = bv[2 * i] + (i < 2 ? b0[2 * i] : b1[2 * i - 4]), b_1 = bv[2 * i + 1] + (i < 2 ? b0[2 * i + 1] : b1[2 * i - 3]);
      const unsigned ab = pk2(a_0 * ASC, a_1 * ASC), bb = pk2(b_0, b_1);
      const unsigned t1_ = pk2(g * bflo(ab), g * bfhi(ab)), t2_ = pk2(omg * bflo(bb), omg * bfhi(bb));
      o4[i] = pk2(bflo(t1_) + bflo(t2_), bfhi(t1_) + bfhi(t2_)); }
    *(GAS u32x4*)((char*)p.out + (size_t)s * 1024 + n * 2) = (u32x4){o4[0], o4[1], o4[2], o4[3]};
  }
  STAMP(16);
  STAMP_FLUSH();
  asm volatile("fused_end_label:");
}

extern "C" void kernel_launch(void* const* d_in, const int* in_sizes, int n_in, void* d_out, int out_size, void* d_ws, size_t ws_size, hipStream_t stream) {
  Params p;
  p.ax = (const uint16_t*)d_in[0]; p.wr = (const uint16_t*)d_in[1]; p.wi = (const uint16_t*)d_in[2];
  p.w1 = (const uint16_t*)d_in[4]; p.w2 = (const uint16_t*)d_in[5]; p.gw = (const uint16_t*)d_in[6];
  p.out = (uint16_t*)d_out; p.ws = (char*)d_ws;
  (void)hipFuncSetAttribute((const void*)fused_kernel, hipFuncAttributeMaxDynamicSharedMemorySize, 163840);
  fused_kernel<<<256, 512, 163840, stream>>>(p);
}
```
